```python
import torch

def _softmax(x, axis=-1):
    return torch.softmax(x, dim=axis)
def _gelu(x):
    return torch.nn.functional.gelu(x, approximate="tanh")
def _silu(x):
    return torch.nn.functional.silu(x)
def _sigmoid(x):
    return torch.sigmoid(x)
def _layernorm(x, axis=-1, eps=1e-5):
    m = x.mean(dim=axis, keepdim=True)
    v = x.var(dim=axis, keepdim=True, unbiased=False)
    return (x - m) / torch.sqrt(v + eps)
def _scatter_add(tgt, idx, src, axis=0):
    return tgt.clone().index_add_(axis, idx, src)
def _segment_sum(src, seg, n):
    out = torch.zeros((n,) + src.shape[1:], dtype=src.dtype)
    return out.index_add_(0, seg, src)
def _pad(x, pw):
    flat = tuple(v for pair in reversed(pw) for v in pair)
    return torch.nn.functional.pad(x, flat)

kernel_name = 'concat_heads__mha_block_cached__gqa_block_cached__near_ridge'

def setup_inputs():

    _w = (lambda a: (lambda t: t.to(torch.bfloat16) if t.is_floating_point() else t)(torch.as_tensor(a)))
    def _A_setup():
        B, Sq, D = 8, 240, 512
        H, Sk, Dh = 32, 1024, 128
        return {
            "x": _w((lambda *s: torch.randn(*s, dtype=torch.bfloat16) * 0.8)(B, Sq, D)),
            "k_cache": _w((lambda *s: torch.randn(*s, dtype=torch.bfloat16) * 0.8)(B, H, Sk, Dh)),
            "v_cache": _w((lambda *s: torch.randn(*s, dtype=torch.bfloat16) * 0.05)(B, H, Sk, Dh)),
            "wq": _w((lambda *s: torch.randn(*s, dtype=torch.bfloat16) * 0.8)(D, H * Dh) * (D ** -0.5)),
            "wo": _w((lambda *s: torch.randn(*s, dtype=torch.bfloat16) * 0.05)(H * Dh, D) * ((H * Dh) ** -0.5)),
        }
    _A_in = _A_setup()

    def _B_setup():
        B, Sq, D = 8, 240, 512
        Hq, Hkv, Sk, Dh = 32, 32, 1024, 128
        assert Hq % Hkv == 0, (Hq, Hkv)
        return {
            "x": _w((lambda *s: torch.randn(*s, dtype=torch.bfloat16) * 0.8)(B, Sq, D)),
            "k_cache": _w((lambda *s: torch.randn(*s, dtype=torch.bfloat16) * 0.8)(B, Hkv, Sk, Dh)),
            "v_cache": _w((lambda *s: torch.randn(*s, dtype=torch.bfloat16) * 0.05)(B, Hkv, Sk, Dh)),
            "wq": _w((lambda *s: torch.randn(*s, dtype=torch.bfloat16) * 0.8)(D, Hq * Dh) * (D ** -0.5)),
            "wo": _w((lambda *s: torch.randn(*s, dtype=torch.bfloat16) * 0.05)(Hq * Dh, D) * ((Hq * Dh) ** -0.5)),
        }
    _B_in = _B_setup()

    return {
        "A_x": _A_in["x"],
        "A_k_cache": _A_in["k_cache"],
        "A_v_cache": _A_in["v_cache"],
        "A_wq": _A_in["wq"],
        "A_wo": _A_in["wo"],
        "B_x": _B_in["x"],
        "B_k_cache": _B_in["k_cache"],
        "B_v_cache": _B_in["v_cache"],
        "B_wq": _B_in["wq"],
        "B_wo": _B_in["wo"],
    }

def reference(A_x, A_k_cache, A_v_cache, A_wq, A_wo, B_x, B_k_cache, B_v_cache, B_wq, B_wo):
    def _A_ref(x, k_cache, v_cache, wq, wo):
        B, Sq, D = x.shape
        H, Dh = 32, 128
        q = (lambda x, *d: x.permute(*d))((x @ wq).reshape(B, Sq, H, Dh), 0, 2, 1, 3)
        scores = torch.einsum("bhqd,bhkd->bhqk", q, k_cache) * (Dh ** -0.5)
        p = _softmax(scores, axis=-1)
        o = torch.einsum("bhqk,bhkd->bhqd", p, v_cache)
        return (lambda x, *d: x.permute(*d))(o, 0, 2, 1, 3).reshape(B, Sq, H * Dh) @ wo

    def _B_ref(x, k_cache, v_cache, wq, wo):
        B, Sq, D = x.shape
        Hq, Dh = 32, 128
        Hkv = k_cache.shape[1]
        rep = Hq // Hkv
        q = (lambda x, *d: x.permute(*d))((x @ wq).reshape(B, Sq, Hq, Dh), 0, 2, 1, 3)
        k = (lambda x, n, axis: torch.repeat_interleave(x, n, dim=axis))(k_cache, rep, axis=1)
        v = (lambda x, n, axis: torch.repeat_interleave(x, n, dim=axis))(v_cache, rep, axis=1)
        scores = torch.einsum("bhqd,bhkd->bhqk", q, k) * (Dh ** -0.5)
        p = _softmax(scores, axis=-1)
        o = torch.einsum("bhqk,bhkd->bhqd", p, v)
        return (lambda x, *d: x.permute(*d))(o, 0, 2, 1, 3).reshape(B, Sq, Hq * Dh) @ wo

    a = _A_ref(A_x, A_k_cache, A_v_cache, A_wq, A_wo)
    b = _B_ref(B_x, B_k_cache, B_v_cache, B_wq, B_wo)
    return torch.concatenate([a, b], axis=-1)
```

```cpp
#include <hip/hip_runtime.h>
#include <hip/hip_bf16.h>
#include <stdint.h>

typedef unsigned short u16;
__device__ __forceinline__ float bf2f(u16 v) { return __uint_as_float(((unsigned)v) << 16); }
__device__ __forceinline__ u16 f2bf(float f) { unsigned u = __float_as_uint(f); u += 0x7fffu + ((u >> 16) & 1u); return (u16)(u >> 16); }

constexpr int NB = 8, SQ = 240, DM = 512, NH = 32, SK = 1024, DH = 128;

__global__ void ref_gemm(const u16* A, const u16* B, u16* C, int M, int N, int K, int ldc) {
  __shared__ float As[16][17], Bs[16][17];
  int tx = threadIdx.x & 15, ty = threadIdx.x >> 4;
  int row = blockIdx.y * 16 + ty, col = blockIdx.x * 16 + tx;
  float acc = 0.f;
  for (int k0 = 0; k0 < K; k0 += 16) {
    As[ty][tx] = bf2f(A[(long)row * K + k0 + tx]);
    Bs[ty][tx] = bf2f(B[(long)(k0 + ty) * N + col]);
    __syncthreads();
    for (int k = 0; k < 16; ++k) acc += As[ty][k] * Bs[k][tx];
    __syncthreads();
  }
  C[(long)row * ldc + col] = f2bf(acc);
}

__global__ void ref_attn(const u16* q, const u16* k, const u16* v, u16* o) {
  int qi = blockIdx.x, h = blockIdx.y, b = blockIdx.z, t = threadIdx.x;
  __shared__ float qs[DH], p[SK], red[256];
  if (t < DH) qs[t] = bf2f(q[((long)b * SQ + qi) * (NH * DH) + h * DH + t]);
  __syncthreads();
  const u16* kb = k + ((long)b * NH + h) * SK * DH;
  const u16* vb = v + ((long)b * NH + h) * SK * DH;
  float mx = -1e30f;
  for (int j = t; j < SK; j += 256) {
    float s = 0.f;
    for (int d = 0; d < DH; ++d) s += qs[d] * bf2f(kb[(long)j * DH + d]);
    s = bf2f(f2bf(s));
    s = bf2f(f2bf(s * 0.08838834764831845f));
    p[j] = s; mx = fmaxf(mx, s);
  }
  red[t] = mx; __syncthreads();
  for (int s = 128; s > 0; s >>= 1) { if (t < s) red[t] = fmaxf(red[t], red[t + s]); __syncthreads(); }
  mx = red[0]; __syncthreads();
  float sum = 0.f;
  for (int j = t; j < SK; j += 256) { float e = expf(p[j] - mx); p[j] = e; sum += e; }
  red[t] = sum; __syncthreads();
  for (int s = 128; s > 0; s >>= 1) { if (t < s) red[t] += red[t + s]; __syncthreads(); }
  sum = red[0]; __syncthreads();
  for (int j = t; j < SK; j += 256) p[j] = bf2f(f2bf(p[j] / sum));
  __syncthreads();
  if (t < DH) {
    float acc = 0.f;
    for (int j = 0; j < SK; ++j) acc += p[j] * bf2f(vb[(long)j * DH + t]);
    o[((long)b * SQ + qi) * (NH * DH) + h * DH + t] = f2bf(acc);
  }
}

extern "C" void kernel_launch(void* const* d_in, const int* in_sizes, int n_in, void* d_out, int out_size, void* d_ws, size_t ws_size, hipStream_t stream) {
  u16* out = (u16*)d_out;
  for (int blk = 0; blk < 2; ++blk) {
    const u16* x = (const u16*)d_in[blk * 5 + 0];
    const u16* k = (const u16*)d_in[blk * 5 + 1];
    const u16* v = (const u16*)d_in[blk * 5 + 2];
    const u16* wq = (const u16*)d_in[blk * 5 + 3];
    const u16* wo = (const u16*)d_in[blk * 5 + 4];
    u16* q = (u16*)d_ws + (size_t)blk * 2 * NB * SQ * NH * DH;
    u16* o = q + (size_t)NB * SQ * NH * DH;
    ref_gemm<<<dim3(NH * DH / 16, NB * SQ / 16), 256, 0, stream>>>(x, wq, q, NB * SQ, NH * DH, DM, NH * DH);
    ref_attn<<<dim3(SQ, NH, NB), 256, 0, stream>>>(q, k, v, o);
    ref_gemm<<<dim3(DM / 16, NB * SQ / 16), 256, 0, stream>>>(o, wo, out + blk * DM, NB * SQ, DM, NH * DH, 2 * DM);
  }
}
```

```cpp
#include <hip/hip_runtime.h>
#include <stdint.h>

struct Ptrs { const char* x[2]; const char* k[2]; const char* v[2]; const char* wq[2]; const char* wo[2]; char* ws; char* out; };

__global__ __launch_bounds__(256, 1) __attribute__((aligned(4096))) void fused_attn(Ptrs P) {
  __shared__ __attribute__((aligned(1024))) char lds[163840];
  const unsigned bid = blockIdx.x;
  const unsigned wid = __builtin_amdgcn_readfirstlane(threadIdx.x >> 6);
  const unsigned long long ka = (unsigned long long)__builtin_amdgcn_kernarg_segment_ptr();
  asm volatile("" :: "s"((unsigned)(size_t)lds));
  asm volatile(
R"(.macro zm a:vararg
v_mfma_f32_16x16x32_bf16 \a
.endm
.macro zt a:vararg
ds_read_b64_tr_b16 \a
.endm
.macro zc a:vararg
v_cvt_pk_bf16_f32 \a
.endm
.macro zr a:vararg
v_accvgpr_read_b32 \a
.endm
.macro zw a:vararg
v_accvgpr_write_b32 \a
.endm
.macro zl a:vararg
buffer_load_dwordx4 \a
.endm
.macro ze a:vararg
v_exp_f32 \a
.endm
.macro zk a:vararg
ds_read_b128 \a
.endm
.macro zp a:vararg
v_pk_mul_f32 \a
.endm
L_asm%=:
s_mov_b32 s76, 0
s_mov_b32 s63, 0
s_mov_b32 s78, 0x20000
L_top%=:
s_lshl_b32 s62, %[bid], 1
s_and_b32 s62, s62, 8
s_load_dwordx2 s[12:13], %[ka], s62 offset:48
s_load_dwordx2 s[6:7], %[ka], s62 offset:0
s_load_dwordx2 s[8:9], %[ka], s62 offset:16
s_load_dwordx2 s[10:11], %[ka], s62 offset:32
s_load_dwordx2 s[14:15], %[ka], s62 offset:64
s_load_dwordx4 s[16:19], %[ka], 0x50
s_mov_b32 s64, %[wid]
s_lshr_b32 s71, %[bid], 3
s_and_b32 s75, %[bid], 3
s_lshr_b32 s73, s71, 2
s_and_b32 s74, s71, 3
s_lshl_b32 s65, s75, 2
s_add_u32 s65, s65, s74
s_lshl_b32 s68, s74, 8
s_lshl_b32 s69, s74, 20
s_lshl_b32 s44, s64, 12
s_add_u32 s45, s44, 0x10000
s_lshl_b32 s46, s64, 13
v_mbcnt_lo_u32_b32 v236, -1, 0
v_mbcnt_hi_u32_b32 v236, -1, v236
v_lshrrev_b32 v239, 4, v236
s_lshl_b32 s54, s64, 17
v_and_b32 v238, 15, v236
v_lshlrev_b32 v240, 2, v239
v_xor_b32 v238, v238, v240
v_lshlrev_b32 v238, 4, v238
v_lshl_add_u32 v237, v239, 13, v238
v_add_u32 v228, s54, v237
v_add_u32 v229, 0x7c00, v228
v_add_u32 v230, 0xf800, v228
v_add_u32 v231, 0x17400, v228
s_lshl_b32 s54, s64, 16
v_lshrrev_b32 v237, 3, v236
v_and_b32 v238, 7, v236
v_xor_b32 v238, v238, v239
v_lshlrev_b32 v238, 4, v238
v_lshl_add_u32 v237, v237, 10, v238
v_add_u32 v237, s54, v237
v_xor_b32 v238, 64, v237
v_add_u32 v218, 0, v237
v_add_u32 v219, 0x1c00, v238
v_add_u32 v220, 0x3800, v237
v_add_u32 v221, 0x5400, v238
v_add_u32 v222, 0x8000, v237
v_add_u32 v223, 0x9c00, v238
v_add_u32 v224, 0xb800, v237
v_add_u32 v225, 0xd400, v238
s_lshl_b32 s54, s65, 9
s_mul_i32 s74, s73, 0x3c000
s_mov_b32 s30, 0x3fe200
s_mov_b32 s31, 0x20000
s_mov_b32 s26, 0x3c000
s_mov_b32 s27, 0x20000
s_waitcnt lgkmcnt(0)
s_add_u32 s28, s12, s54
s_addc_u32 s29, s13, 0
s_and_b32 s29, s29, 0xffff
s_mov_b32 s50, 0
s_add_u32 m0, s45, 0
s_nop 0
zl v228,s[28:31],s50 offen offset:0 lds
zl v229,s[28:31],s50 offen offset:1024 lds
zl v230,s[28:31],s50 offen offset:2048 lds
zl v231,s[28:31],s50 offen offset:3072 lds
s_add_u32 s24, s6, s74
s_addc_u32 s25, s7, 0
s_and_b32 s25, s25, 0xffff
s_mov_b32 s49, 0
s_add_u32 m0, s46, 0x20000
s_nop 0
zl v218,s[24:27],s49 offen offset:0 lds
zl v219,s[24:27],s49 offen offset:1024 lds
zl v220,s[24:27],s49 offen offset:2048 lds
zl v221,s[24:27],s49 offen offset:3072 lds
s_add_u32 m0, s46, 0x21000
s_nop 0
zl v222,s[24:27],s49 offen offset:0 lds
zl v223,s[24:27],s49 offen offset:1024 lds
zl v224,s[24:27],s49 offen offset:2048 lds
zl v225,s[24:27],s49 offen offset:3072 lds
s_getpc_b64 s[52:53]
L_pc%=:
s_lshl_b32 s54, s71, 2
s_add_u32 s54, s54, s64
s_lshl_b32 s54, s54, 3
v_and_b32 v246, 7, v236
v_add_u32 v246, s54, v246
v_lshlrev_b32 v246, 7, v246
v_min_u32 v255, L_hot_end%=-L_pc%=, v246
global_load_dword v254, v255, s[52:53]
s_lshl_b32 s54, s73, 23
s_lshl_b32 s74, s65, 19
s_add_u32 s54, s54, s74
s_add_u32 s8, s8, s54
s_addc_u32 s9, s9, 0
s_add_u32 s10, s10, s54
s_addc_u32 s11, s11, 0
s_lshl_b32 s54, s75, 20
s_add_u32 s54, s54, s68
s_add_u32 s14, s14, s54
s_addc_u32 s15, s15, 0
s_mul_i32 s54, s73, 0x78000
s_add_u32 s54, s54, s68
s_lshl_b32 s74, s75, 6
s_add_u32 s54, s54, s74
s_and_b32 s62, %[bid], 4
s_lshl_b32 s74, s62, 8
s_add_u32 s54, s54, s74
s_add_u32 s18, s18, s54
s_addc_u32 s19, s19, 0
s_lshl_b32 s62, s62, 3
s_lshl_b32 s54, s73, 2
s_add_u32 s62, s62, s54
s_and_b32 s54, s65, 3
s_add_u32 s54, s54, s62
s_mul_i32 s54, s54, 0x84500
s_add_u32 s54, s54, s46
s_add_u32 s54, s54, 0x2200000
s_add_u32 s22, s16, s54
s_addc_u32 s23, s17, 0
s_add_u32 s62, s62, s64
s_mul_i32 s54, s62, 0x84500
s_lshl_b32 s74, s75, 17
s_add_u32 s54, s54, s74
s_add_u32 s20, s16, s54
s_addc_u32 s21, s17, 0
s_lshl_b32 s54, s62, 13
s_add_u32 s54, s54, 0x4400000
s_add_u32 s16, s16, s54
s_addc_u32 s17, s17, 0
s_and_b32 s54, s65, 3
s_mul_i32 s54, s54, 3
s_lshr_b32 s54, s54, 1
s_lshl_b32 s68, s54, 7
s_lshl_b32 s69, s54, 19
s_lshl_b32 s47, s64, 10
s_mov_b32 s48, s47
s_mov_b32 s96, s8
s_and_b32 s97, s9, 0xffff
s_mov_b32 s98, 0x80000
s_mov_b32 s99, 0x20000
s_mov_b32 s36, s10
s_and_b32 s37, s11, 0xffff
s_mov_b32 s38, 0x80000
s_mov_b32 s39, 0x20000
s_mov_b32 s40, s14
s_and_b32 s41, s15, 0xffff
s_mov_b32 s42, 0x100000
s_mov_b32 s43, 0x20000
s_mov_b32 s58, 0x3e0293ee
s_mov_b32 s59, 0x3e0293ee
s_mov_b32 s55, 0x5d800000
s_mov_b32 s51, 0
s_lshl_b32 s77, s65, 6
s_and_b32 s54, s65, 3
s_lshl_b32 s84, s54, 18
s_lshl_b32 s88, s54, 15
s_and_b32 s54, s65, 3
s_cmp_le_u32 s54, 0
s_cselect_b32 s54, 1, 0
s_lshl_b32 s85, s54, 18
s_lshl_b32 s89, s54, 15
s_and_b32 s54, s65, 3
s_cmp_le_u32 s54, 1
s_cselect_b32 s54, 2, 1
s_lshl_b32 s86, s54, 18
s_lshl_b32 s90, s54, 15
s_and_b32 s54, s65, 3
s_cmp_le_u32 s54, 2
s_cselect_b32 s54, 3, 2
s_lshl_b32 s87, s54, 18
s_lshl_b32 s91, s54, 15
v_and_b32 v237, 15, v236
v_lshrrev_b32 v238, 4, v236
v_bfe_u32 v239, v236, 2, 2
v_and_b32 v240, 3, v236
v_bfe_u32 v242, v236, 1, 3
v_xor_b32 v242, v242, v238
v_lshlrev_b32 v242, 4, v242
v_lshl_add_u32 v242, v237, 7, v242
v_add_u32 v250, s46, v242
v_xor_b32 v251, 64, v250
v_add_u32 v226, 0x20000, v250
v_add_u32 v227, 0x20000, v251
v_lshl_add_u32 v242, v238, 3, v239
v_lshlrev_b32 v242, 8, v242
v_lshl_add_u32 v242, v239, 6, v242
v_lshl_add_u32 v242, v240, 4, v242
v_add_u32 v232, 0x10000, v242
v_xor_b32 v233, 64, v232
v_xor_b32 v234, 0x80, v232
v_xor_b32 v235, 0xc0, v232
v_lshl_add_u32 v242, v238, 2, v239
v_and_b32 v243, 7, v242
v_lshlrev_b32 v242, 8, v242
v_lshl_add_u32 v242, v243, 5, v242
v_lshl_add_u32 v242, v240, 3, v242
v_add_u32 v200, 0x10000, v242
v_xor_b32 v201, 32, v200
v_xor_b32 v202, 64, v200
v_xor_b32 v203, 0x60, v200
v_xor_b32 v204, 0x80, v200
v_xor_b32 v205, 0xa0, v200
v_xor_b32 v206, 0xc0, v200
v_xor_b32 v207, 0xe0, v200
v_and_b32 v242, 7, v237
v_lshlrev_b32 v242, 1, v242
v_xor_b32 v242, v238, v242
v_lshlrev_b32 v242, 4, v242
v_lshl_add_u32 v196, v237, 8, v242
v_xor_b32 v197, 64, v196
v_xor_b32 v198, 0x80, v196
v_xor_b32 v199, 0xc0, v196
v_lshlrev_b32 v244, 1, v238
v_xor_b32 v244, v237, v244
v_lshlrev_b32 v244, 4, v244
v_lshl_add_u32 v242, v238, 8, v244
s_and_b32 s54, s64, 1
s_lshl_b32 s54, s54, 7
v_xor_b32 v212, s54, v242
v_lshl_add_u32 v242, v238, 10, v244
s_lshl_b32 s54, s64, 14
v_add_u32 v208, s54, v242
v_xor_b32 v242, 0x80, v208
v_add_u32 v209, 0xc00, v242
v_add_u32 v210, 0x1800, v208
v_add_u32 v211, 0x2400, v242
v_lshlrev_b32 v214, 4, v236
s_and_b32 s54, s65, 12
v_add_u32 v242, s54, v240
v_lshlrev_b32 v215, 6, v242
v_bfe_u32 v242, v236, 1, 1
v_lshrrev_b32 v243, 5, v236
v_xor_b32 v242, v242, v243
v_add_u32 v242, -1, v242
v_and_b32 v192, 0x3f803f80, v242
v_and_b32 v193, 0x3f803f80, v242
v_and_b32 v194, 0x3f803f80, v242
v_and_b32 v195, 0x3f803f80, v242
v_and_b32 v242, 1, v236
v_mul_u32_u24 v242, 0xaaaa, v242
v_add_u32 v253, 0x4444, v242
s_lshl_b32 s70, s71, 15
s_add_u32 s70, s70, s46
s_and_b32 s54, s65, 3
s_lshl_b32 s54, s54, 8
s_sub_u32 s40, s40, s54
s_subb_u32 s41, s41, 0
s_mov_b32 s50, 0x100
s_add_u32 m0, s45, 0x4000
s_nop 0
zl v228,s[28:31],s50 offen offset:0 lds
zl v229,s[28:31],s50 offen offset:1024 lds
zl v230,s[28:31],s50 offen offset:2048 lds
zl v231,s[28:31],s50 offen offset:3072 lds
s_mov_b32 s50, 0x80000
s_add_u32 m0, s45, 0x8000
s_nop 0
zl v228,s[28:31],s50 offen offset:0 lds
zl v229,s[28:31],s50 offen offset:1024 lds
zl v230,s[28:31],s50 offen offset:2048 lds
zl v231,s[28:31],s50 offen offset:3072 lds
s_mov_b32 s92, s16
s_and_b32 s93, s17, 0xffff
s_mov_b32 s94, 0x2000
s_mov_b32 s95, 0x20000
buffer_load_dwordx2 v[216:217], off, s[92:95], 0 offset:3072 sc1
s_cmp_eq_u32 s64, 3
s_cbranch_scc1 L_w3%=
s_waitcnt vmcnt(10)
s_barrier
zk a[128:131],v226 offset:0
zk a[132:135],v227 offset:0
zk a[136:139],v226 offset:2048
zk a[140:143],v227 offset:2048
zk a[144:147],v226 offset:4096
zk a[148:151],v227 offset:4096
zk a[152:155],v226 offset:6144
zk a[156:159],v227 offset:6144
zt v[128:129],v232 offset:0
zt v[130:131],v232 offset:1024
zt v[132:133],v232 offset:8192
zt v[134:135],v232 offset:9216
zt v[136:137],v232 offset:8
zt v[138:139],v232 offset:1032
zt v[140:141],v232 offset:8200
zt v[142:143],v232 offset:9224
zt v[144:145],v233 offset:0
s_waitcnt lgkmcnt(7)
zm v[0:3],v[128:131],a[128:131],0
s_waitcnt lgkmcnt(5)
s_mov_b32 s49, 0x80
zt v[146:147],v233 offset:1024
zm v[0:3],v[132:135],a[132:135],v[0:3]
s_add_u32 m0, s46, 0x8000
zm v[4:7],v[132:135],a[140:143],0
zt v[148:149],v233 offset:8192
zl v218,s[24:27],s49 offen offset:0 lds
zm v[4:7],v[128:131],a[136:139],v[4:7]
zt v[150:151],v233 offset:9216
zl v219,s[24:27],s49 offen offset:1024 lds
zm v[8:11],v[128:131],a[144:147],0
zm v[8:11],v[132:135],a[148:151],v[8:11]
zt v[152:153],v233 offset:8
zm v[12:15],v[132:135],a[156:159],0
zm v[12:15],v[128:131],a[152:155],v[12:15]
s_waitcnt lgkmcnt(7)
zt v[154:155],v233 offset:1032
zl v220,s[24:27],s49 offen offset:2048 lds
zm v[28:31],v[136:139],a[152:155],0
s_waitcnt lgkmcnt(6)
zt v[156:157],v233 offset:8200
zm v[28:31],v[140:143],a[156:159],v[28:31]
zm v[24:27],v[140:143],a[148:151],0
zt v[158:159],v233 offset:9224
zm v[24:27],v[136:139],a[144:147],v[24:27]
zl v221,s[24:27],s49 offen offset:3072 lds
zm v[20:23],v[136:139],a[136:139],0
zt v[160:161],v234 offset:0
zm v[20:23],v[140:143],a[140:143],v[20:23]
zt v[162:163],v234 offset:1024
zm v[16:19],v[140:143],a[132:135],0
s_add_u32 m0, s46, 0x9000
zm v[16:19],v[136:139],a[128:131],v[16:19]
s_waitcnt lgkmcnt(8)
zt v[164:165],v234 offset:8192
zl v222,s[24:27],s49 offen offset:0 lds
zm v[32:35],v[144:147],a[128:131],0
s_waitcnt lgkmcnt(7)
zt v[166:167],v234 offset:9216
zm v[32:35],v[148:151],a[132:135],v[32:35]
zm v[36:39],v[148:151],a[140:143],0
zt v[168:169],v234 offset:8
zm v[36:39],v[144:147],a[136:139],v[36:39]
zl v223,s[24:27],s49 offen offset:1024 lds
zm v[40:43],v[144:147],a[144:147],0
zt v[170:171],v234 offset:1032
zm v[40:43],v[148:151],a[148:151],v[40:43]
zt v[172:173],v234 offset:8200
zm v[44:47],v[148:151],a[156:159],0
zm v[44:47],v[144:147],a[152:155],v[44:47]
s_waitcnt lgkmcnt(9)
zt v[174:175],v234 offset:9224
zl v224,s[24:27],s49 offen offset:2048 lds
zm v[60:63],v[152:155],a[152:155],0
s_waitcnt lgkmcnt(8)
zm v[60:63],v[156:159],a[156:159],v[60:63]
zt v[176:177],v235 offset:0
zm v[56:59],v[156:159],a[148:151],0
zt v[178:179],v235 offset:1024
zl v225,s[24:27],s49 offen offset:3072 lds
zm v[56:59],v[152:155],a[144:147],v[56:59]
zm v[52:55],v[152:155],a[136:139],0
s_mov_b32 s50, 0x80100
zt v[180:181],v235 offset:8192
zm v[52:55],v[156:159],a[140:143],v[52:55]
s_add_u32 m0, s45, 0xc000
zt v[182:183],v235 offset:9216
zm v[48:51],v[156:159],a[132:135],0
zl v228,s[28:31],s50 offen offset:0 lds
zm v[48:51],v[152:155],a[128:131],v[48:51]
s_waitcnt lgkmcnt(10)
zt v[184:185],v235 offset:8
zm v[64:67],v[160:163],a[128:131],0
s_waitcnt lgkmcnt(9)
zm v[64:67],v[164:167],a[132:135],v[64:67]
zt v[186:187],v235 offset:1032
zm v[68:71],v[164:167],a[140:143],0
zt v[188:189],v235 offset:8200
zl v229,s[28:31],s50 offen offset:1024 lds
zm v[68:71],v[160:163],a[136:139],v[68:71]
zm v[72:75],v[160:163],a[144:147],0
zt v[190:191],v235 offset:9224
zm v[72:75],v[164:167],a[148:151],v[72:75]
zm v[76:79],v[164:167],a[156:159],0
zl v230,s[28:31],s50 offen offset:2048 lds
zm v[76:79],v[160:163],a[152:155],v[76:79]
s_waitcnt lgkmcnt(10)
zm v[92:95],v[168:171],a[152:155],0
s_waitcnt lgkmcnt(8)
zm v[92:95],v[172:175],a[156:159],v[92:95]
zm v[88:91],v[172:175],a[148:151],0
zl v231,s[28:31],s50 offen offset:3072 lds
zm v[88:91],v[168:171],a[144:147],v[88:91]
zm v[84:87],v[168:171],a[136:139],0
zm v[84:87],v[172:175],a[140:143],v[84:87]
zm v[80:83],v[172:175],a[132:135],0
zm v[80:83],v[168:171],a[128:131],v[80:83]
s_waitcnt vmcnt(17) lgkmcnt(0)
zm v[96:99],v[176:179],a[128:131],0
s_barrier
zm v[96:99],v[180:183],a[132:135],v[96:99]
zm v[100:103],v[180:183],a[140:143],0
zt v[128:129],v232 offset:16384
zm v[100:103],v[176:179],a[136:139],v[100:103]
zt v[130:131],v232 offset:17408
zm v[104:107],v[176:179],a[144:147],0
zm v[104:107],v[180:183],a[148:151],v[104:107]
zt v[132:133],v232 offset:24576
zm v[108:111],v[180:183],a[156:159],0
zt v[134:135],v232 offset:25600
zm v[108:111],v[176:179],a[152:155],v[108:111]
zm v[124:127],v[184:187],a[152:155],0
zt v[136:137],v232 offset:16392
zm v[124:127],v[188:191],a[156:159],v[124:127]
s_cmp_ge_u32 s68, 0x300
zm v[120:123],v[188:191],a[148:151],0
s_cselect_b32 s49, -6, 0
zt v[138:139],v232 offset:17416
zm v[120:123],v[184:187],a[144:147],v[120:123]
s_lshl_b32 s49, s49, 7
zt v[140:141],v232 offset:24584
zm v[116:119],v[184:187],a[136:139],0
s_add_u32 s49, s49, s68
zm v[116:119],v[188:191],a[140:143],v[116:119]
s_add_u32 s49, s49, 0x100
zt v[142:143],v232 offset:25608
zm v[112:115],v[188:191],a[132:135],0
s_add_u32 m0, s46, 0x20000
zm v[112:115],v[184:187],a[128:131],v[112:115]
s_waitcnt lgkmcnt(6)
zt v[144:145],v233 offset:16384
zl v218,s[24:27],s49 offen offset:0 lds
zm a[0:3],v[128:131],a[128:131],0
s_waitcnt lgkmcnt(5)
zt v[146:147],v233 offset:17408
zl v219,s[24:27],s49 offen offset:1024 lds
zm a[0:3],v[132:135],a[132:135],a[0:3]
zm a[4:7],v[132:135],a[140:143],0
zt v[148:149],v233 offset:24576
zm a[4:7],v[128:131],a[136:139],a[4:7]
zt v[150:151],v233 offset:25600
zl v220,s[24:27],s49 offen offset:2048 lds
zm a[8:11],v[128:131],a[144:147],0
zm a[8:11],v[132:135],a[148:151],a[8:11]
zt v[152:153],v233 offset:16392
zm a[12:15],v[132:135],a[156:159],0
zm a[12:15],v[128:131],a[152:155],a[12:15]
s_waitcnt lgkmcnt(7)
zt v[154:155],v233 offset:17416
zm a[28:31],v[136:139],a[152:155],0
s_waitcnt lgkmcnt(6)
zt v[156:157],v233 offset:24584
zl v221,s[24:27],s49 offen offset:3072 lds
zm a[28:31],v[140:143],a[156:159],a[28:31]
zm a[24:27],v[140:143],a[148:151],0
zt v[158:159],v233 offset:25608
zm a[24:27],v[136:139],a[144:147],a[24:27]
zm a[20:23],v[136:139],a[136:139],0
zt v[160:161],v234 offset:16384
zm a[20:23],v[140:143],a[140:143],a[20:23]
s_add_u32 m0, s46, 0x21000
zt v[162:163],v234 offset:17408
zm a[16:19],v[140:143],a[132:135],0
zl v222,s[24:27],s49 offen offset:0 lds
zm a[16:19],v[136:139],a[128:131],a[16:19]
s_waitcnt lgkmcnt(8)
zt v[164:165],v234 offset:24576
zm a[32:35],v[144:147],a[128:131],0
s_waitcnt lgkmcnt(7)
zt v[166:167],v234 offset:25600
zm a[32:35],v[148:151],a[132:135],a[32:35]
zm a[36:39],v[148:151],a[140:143],0
zt v[168:169],v234 offset:16392
zm a[36:39],v[144:147],a[136:139],a[36:39]
zl v223,s[24:27],s49 offen offset:1024 lds
zm a[40:43],v[144:147],a[144:147],0
zt v[170:171],v234 offset:17416
zm a[40:43],v[148:151],a[148:151],a[40:43]
zt v[172:173],v234 offset:24584
zm a[44:47],v[148:151],a[156:159],0
zm a[44:47],v[144:147],a[152:155],a[44:47]
s_waitcnt lgkmcnt(9)
zt v[174:175],v234 offset:25608
zm a[60:63],v[152:155],a[152:155],0
s_waitcnt lgkmcnt(8)
zl v224,s[24:27],s49 offen offset:2048 lds
zm a[60:63],v[156:159],a[156:159],a[60:63]
zt v[176:177],v235 offset:16384
zm a[56:59],v[156:159],a[148:151],0
zt v[178:179],v235 offset:17408
zm a[56:59],v[152:155],a[144:147],a[56:59]
zm a[52:55],v[152:155],a[136:139],0
zt v[180:181],v235 offset:24576
zm a[52:55],v[156:159],a[140:143],a[52:55]
zt v[182:183],v235 offset:25600
zl v225,s[24:27],s49 offen offset:3072 lds
zm a[48:51],v[156:159],a[132:135],0
s_cmp_ge_u32 s69, 0x300000
zm a[48:51],v[152:155],a[128:131],a[48:51]
s_waitcnt lgkmcnt(10)
s_cselect_b32 s50, -6, 0
zt v[184:185],v235 offset:16392
zm a[64:67],v[160:163],a[128:131],0
s_waitcnt lgkmcnt(9)
s_lshl_b32 s50, s50, 19
zm a[64:67],v[164:167],a[132:135],a[64:67]
s_add_u32 s50, s50, s69
zt v[186:187],v235 offset:17416
zm a[68:71],v[164:167],a[140:143],0
s_add_u32 s50, s50, 0x100000
zt v[188:189],v235 offset:24584
zm a[68:71],v[160:163],a[136:139],a[68:71]
s_add_u32 m0, s45, 0
zm a[72:75],v[160:163],a[144:147],0
zt v[190:191],v235 offset:25608
zl v228,s[28:31],s50 offen offset:0 lds
zm a[72:75],v[164:167],a[148:151],a[72:75]
zm a[76:79],v[164:167],a[156:159],0
zm a[76:79],v[160:163],a[152:155],a[76:79]
s_waitcnt lgkmcnt(10)
zm a[92:95],v[168:171],a[152:155],0
s_waitcnt lgkmcnt(8)
zl v229,s[28:31],s50 offen offset:1024 lds
zm a[92:95],v[172:175],a[156:159],a[92:95]
zm a[88:91],v[172:175],a[148:151],0
zm a[88:91],v[168:171],a[144:147],a[88:91]
zm a[84:87],v[168:171],a[136:139],0
zm a[84:87],v[172:175],a[140:143],a[84:87]
zl v230,s[28:31],s50 offen offset:2048 lds
zm a[80:83],v[172:175],a[132:135],0
zm a[80:83],v[168:171],a[128:131],a[80:83]
s_waitcnt vmcnt(15) lgkmcnt(0)
zm a[96:99],v[176:179],a[128:131],0
s_barrier
zm a[96:99],v[180:183],a[132:135],a[96:99]
zk a[160:163],v250 offset:32768
zm a[100:103],v[180:183],a[140:143],0
zk a[164:167],v251 offset:32768
zt v[128:129],v232 offset:32768
zm a[100:103],v[176:179],a[136:139],a[100:103]
zk a[168:171],v250 offset:34816
zt v[130:131],v232 offset:33792
zm a[104:107],v[176:179],a[144:147],0
zk a[172:175],v251 offset:34816
zl v231,s[28:31],s50 offen offset:3072 lds
zm a[104:107],v[180:183],a[148:151],a[104:107]
zk a[176:179],v250 offset:36864
zt v[132:133],v232 offset:40960
zm a[108:111],v[180:183],a[156:159],0
zk a[180:183],v251 offset:36864
zt v[134:135],v232 offset:41984
zm a[108:111],v[176:179],a[152:155],a[108:111]
zk a[184:187],v250 offset:38912
zm a[124:127],v[184:187],a[152:155],0
zk a[188:191],v251 offset:38912
zt v[136:137],v232 offset:32776
zm a[124:127],v[188:191],a[156:159],a[124:127]
zm a[120:123],v[188:191],a[148:151],0
zt v[138:139],v232 offset:33800
zm a[120:123],v[184:187],a[144:147],a[120:123]
zt v[140:141],v232 offset:40968
zm a[116:119],v[184:187],a[136:139],0
s_cmp_ge_u32 s69, 0x300000
zm a[116:119],v[188:191],a[140:143],a[116:119]
s_cselect_b32 s50, -6, 0
zt v[142:143],v232 offset:41992
zm a[112:115],v[188:191],a[132:135],0
s_lshl_b32 s50, s50, 19
zm a[112:115],v[184:187],a[128:131],a[112:115]
s_waitcnt lgkmcnt(11)
s_add_u32 s50, s50, s69
zt v[144:145],v233 offset:32768
zm v[0:3],v[128:131],a[160:163],v[0:3]
s_waitcnt lgkmcnt(7)
s_add_u32 s50, s50, 0x100100
zt v[146:147],v233 offset:33792
zm v[0:3],v[132:135],a[164:167],v[0:3]
s_add_u32 m0, s45, 0x4000
zm v[4:7],v[132:135],a[172:175],v[4:7]
zt v[148:149],v233 offset:40960
zl v228,s[28:31],s50 offen offset:0 lds
zm v[4:7],v[128:131],a[168:171],v[4:7]
zt v[150:151],v233 offset:41984
zm v[8:11],v[128:131],a[176:179],v[8:11]
zm v[8:11],v[132:135],a[180:183],v[8:11]
s_waitcnt lgkmcnt(8)
zt v[152:153],v233 offset:32776
zm v[12:15],v[132:135],a[188:191],v[12:15]
zm v[12:15],v[128:131],a[184:187],v[12:15]
s_waitcnt lgkmcnt(7)
zt v[154:155],v233 offset:33800
zm v[28:31],v[136:139],a[184:187],v[28:31]
s_waitcnt lgkmcnt(6)
zt v[156:157],v233 offset:40968
zl v229,s[28:31],s50 offen offset:1024 lds
zm v[28:31],v[140:143],a[188:191],v[28:31]
zm v[24:27],v[140:143],a[180:183],v[24:27]
zt v[158:159],v233 offset:41992
zm v[24:27],v[136:139],a[176:179],v[24:27]
zm v[20:23],v[136:139],a[168:171],v[20:23]
zt v[160:161],v234 offset:32768
zm v[20:23],v[140:143],a[172:175],v[20:23]
zt v[162:163],v234 offset:33792
zm v[16:19],v[140:143],a[164:167],v[16:19]
zm v[16:19],v[136:139],a[160:163],v[16:19]
s_waitcnt lgkmcnt(8)
zt v[164:165],v234 offset:40960
zm v[32:35],v[144:147],a[160:163],v[32:35]
s_waitcnt lgkmcnt(7)
zt v[166:167],v234 offset:41984
zm v[32:35],v[148:151],a[164:167],v[32:35]
zm v[36:39],v[148:151],a[172:175],v[36:39]
zt v[168:169],v234 offset:32776
zm v[36:39],v[144:147],a[168:171],v[36:39]
zm v[40:43],v[144:147],a[176:179],v[40:43]
zt v[170:171],v234 offset:33800
zm v[40:43],v[148:151],a[180:183],v[40:43]
zt v[172:173],v234 offset:40968
zl v230,s[28:31],s50 offen offset:2048 lds
zm v[44:47],v[148:151],a[188:191],v[44:47]
zm v[44:47],v[144:147],a[184:187],v[44:47]
s_waitcnt lgkmcnt(9)
zt v[174:175],v234 offset:41992
zm v[60:63],v[152:155],a[184:187],v[60:63]
s_waitcnt lgkmcnt(8)
zm v[60:63],v[156:159],a[188:191],v[60:63]
zt v[176:177],v235 offset:32768
zm v[56:59],v[156:159],a[180:183],v[56:59]
zt v[178:179],v235 offset:33792
zm v[56:59],v[152:155],a[176:179],v[56:59]
zm v[52:55],v[152:155],a[168:171],v[52:55]
zt v[180:181],v235 offset:40960
zm v[52:55],v[156:159],a[172:175],v[52:55]
zt v[182:183],v235 offset:41984
zm v[48:51],v[156:159],a[164:167],v[48:51]
zm v[48:51],v[152:155],a[160:163],v[48:51]
s_waitcnt lgkmcnt(10)
zt v[184:185],v235 offset:32776
zm v[64:67],v[160:163],a[160:163],v[64:67]
s_waitcnt lgkmcnt(9)
zm v[64:67],v[164:167],a[164:167],v[64:67]
zt v[186:187],v235 offset:33800
zl v231,s[28:31],s50 offen offset:3072 lds
zm v[68:71],v[164:167],a[172:175],v[68:71]
zt v[188:189],v235 offset:40968
zm v[68:71],v[160:163],a[168:171],v[68:71]
zm v[72:75],v[160:163],a[176:179],v[72:75]
zt v[190:191],v235 offset:41992
zm v[72:75],v[164:167],a[180:183],v[72:75]
zm v[76:79],v[164:167],a[188:191],v[76:79]
zm v[76:79],v[160:163],a[184:187],v[76:79]
s_waitcnt lgkmcnt(10)
zm v[92:95],v[168:171],a[184:187],v[92:95]
s_waitcnt lgkmcnt(8)
zm v[92:95],v[172:175],a[188:191],v[92:95]
zm v[88:91],v[172:175],a[180:183],v[88:91]
zm v[88:91],v[168:171],a[176:179],v[88:91]
zm v[84:87],v[168:171],a[168:171],v[84:87]
zm v[84:87],v[172:175],a[172:175],v[84:87]
zm v[80:83],v[172:175],a[164:167],v[80:83]
zm v[80:83],v[168:171],a[160:163],v[80:83]
s_waitcnt vmcnt(8) lgkmcnt(0)
zm v[96:99],v[176:179],a[160:163],v[96:99]
s_barrier
zm v[96:99],v[180:183],a[164:167],v[96:99]
zk a[128:131],v226 offset:0
zm v[100:103],v[180:183],a[172:175],v[100:103]
zk a[132:135],v227 offset:0
zt v[128:129],v232 offset:49152
zm v[100:103],v[176:179],a[168:171],v[100:103]
zk a[136:139],v226 offset:2048
zt v[130:131],v232 offset:50176
zm v[104:107],v[176:179],a[176:179],v[104:107]
zk a[140:143],v227 offset:2048
zm v[104:107],v[180:183],a[180:183],v[104:107]
zk a[144:147],v226 offset:4096
zt v[132:133],v232 offset:57344
zm v[108:111],v[180:183],a[188:191],v[108:111]
zk a[148:151],v227 offset:4096
zt v[134:135],v232 offset:58368
zm v[108:111],v[176:179],a[184:187],v[108:111]
zk a[152:155],v226 offset:6144
zm v[124:127],v[184:187],a[184:187],v[124:127]
zk a[156:159],v227 offset:6144
zt v[136:137],v232 offset:49160
zm v[124:127],v[188:191],a[188:191],v[124:127]
s_cmp_ge_u32 s68, 0x280
zm v[120:123],v[188:191],a[180:183],v[120:123]
s_cselect_b32 s49, -6, 0
zt v[138:139],v232 offset:50184
zm v[120:123],v[184:187],a[176:179],v[120:123]
s_lshl_b32 s49, s49, 7
zt v[140:141],v232 offset:57352
zm v[116:119],v[184:187],a[168:171],v[116:119]
s_add_u32 s49, s49, s68
zm v[116:119],v[188:191],a[172:175],v[116:119]
s_add_u32 s49, s49, 0x180
zt v[142:143],v232 offset:58376
zm v[112:115],v[188:191],a[164:167],v[112:115]
s_waitcnt lgkmcnt(3)
s_add_u32 m0, s46, 0x20000
zm v[112:115],v[184:187],a[160:163],v[112:115]
zt v[144:145],v233 offset:49152
zl v218,s[24:27],s49 offen offset:0 lds
zm a[0:3],v[128:131],a[160:163],a[0:3]
zt v[146:147],v233 offset:50176
zl v219,s[24:27],s49 offen offset:1024 lds
zm a[0:3],v[132:135],a[164:167],a[0:3]
zm a[4:7],v[132:135],a[172:175],a[4:7]
zt v[148:149],v233 offset:57344
zm a[4:7],v[128:131],a[168:171],a[4:7]
zt v[150:151],v233 offset:58368
zl v220,s[24:27],s49 offen offset:2048 lds
zm a[8:11],v[128:131],a[176:179],a[8:11]
zm a[8:11],v[132:135],a[180:183],a[8:11]
zt v[152:153],v233 offset:49160
zm a[12:15],v[132:135],a[188:191],a[12:15]
zm a[12:15],v[128:131],a[184:187],a[12:15]
s_waitcnt lgkmcnt(7)
zt v[154:155],v233 offset:50184
zm a[28:31],v[136:139],a[184:187],a[28:31]
s_waitcnt lgkmcnt(6)
zt v[156:157],v233 offset:57352
zl v221,s[24:27],s49 offen offset:3072 lds
zm a[28:31],v[140:143],a[188:191],a[28:31]
zm a[24:27],v[140:143],a[180:183],a[24:27]
zt v[158:159],v233 offset:58376
zm a[24:27],v[136:139],a[176:179],a[24:27]
zm a[20:23],v[136:139],a[168:171],a[20:23]
zt v[160:161],v234 offset:49152
zm a[20:23],v[140:143],a[172:175],a[20:23]
s_add_u32 m0, s46, 0x21000
zt v[162:163],v234 offset:50176
zm a[16:19],v[140:143],a[164:167],a[16:19]
zl v222,s[24:27],s49 offen offset:0 lds
zm a[16:19],v[136:139],a[160:163],a[16:19]
s_waitcnt lgkmcnt(8)
zt v[164:165],v234 offset:57344
zm a[32:35],v[144:147],a[160:163],a[32:35]
s_waitcnt lgkmcnt(7)
zt v[166:167],v234 offset:58368
zm a[32:35],v[148:151],a[164:167],a[32:35]
zm a[36:39],v[148:151],a[172:175],a[36:39]
zt v[168:169],v234 offset:49160
zm a[36:39],v[144:147],a[168:171],a[36:39]
zl v223,s[24:27],s49 offen offset:1024 lds
zm a[40:43],v[144:147],a[176:179],a[40:43]
zt v[170:171],v234 offset:50184
zm a[40:43],v[148:151],a[180:183],a[40:43]
zt v[172:173],v234 offset:57352
zm a[44:47],v[148:151],a[188:191],a[44:47]
zm a[44:47],v[144:147],a[184:187],a[44:47]
s_waitcnt lgkmcnt(9)
zt v[174:175],v234 offset:58376
zm a[60:63],v[152:155],a[184:187],a[60:63]
s_waitcnt lgkmcnt(8)
zl v224,s[24:27],s49 offen offset:2048 lds
zm a[60:63],v[156:159],a[188:191],a[60:63]
zt v[176:177],v235 offset:49152
zm a[56:59],v[156:159],a[180:183],a[56:59]
zt v[178:179],v235 offset:50176
zm a[56:59],v[152:155],a[176:179],a[56:59]
zm a[52:55],v[152:155],a[168:171],a[52:55]
zt v[180:181],v235 offset:57344
zm a[52:55],v[156:159],a[172:175],a[52:55]
zt v[182:183],v235 offset:58368
zl v225,s[24:27],s49 offen offset:3072 lds
zm a[48:51],v[156:159],a[164:167],a[48:51]
s_cmp_ge_u32 s69, 0x280000
zm a[48:51],v[152:155],a[160:163],a[48:51]
s_waitcnt lgkmcnt(10)
s_cselect_b32 s50, -6, 0
zt v[184:185],v235 offset:49160
zm a[64:67],v[160:163],a[160:163],a[64:67]
s_waitcnt lgkmcnt(9)
s_lshl_b32 s50, s50, 19
zm a[64:67],v[164:167],a[164:167],a[64:67]
s_add_u32 s50, s50, s69
zt v[186:187],v235 offset:50184
zm a[68:71],v[164:167],a[172:175],a[68:71]
s_add_u32 s50, s50, 0x180000
zt v[188:189],v235 offset:57352
zm a[68:71],v[160:163],a[168:171],a[68:71]
s_add_u32 m0, s45, 0x8000
zm a[72:75],v[160:163],a[176:179],a[72:75]
zt v[190:191],v235 offset:58376
zl v228,s[28:31],s50 offen offset:0 lds
zm a[72:75],v[164:167],a[180:183],a[72:75]
zm a[76:79],v[164:167],a[188:191],a[76:79]
zm a[76:79],v[160:163],a[184:187],a[76:79]
s_waitcnt lgkmcnt(10)
zm a[92:95],v[168:171],a[184:187],a[92:95]
s_waitcnt lgkmcnt(8)
zl v229,s[28:31],s50 offen offset:1024 lds
zm a[92:95],v[172:175],a[188:191],a[92:95]
zm a[88:91],v[172:175],a[180:183],a[88:91]
zm a[88:91],v[168:171],a[176:179],a[88:91]
zm a[84:87],v[168:171],a[168:171],a[84:87]
zm a[84:87],v[172:175],a[172:175],a[84:87]
zl v230,s[28:31],s50 offen offset:2048 lds
zm a[80:83],v[172:175],a[164:167],a[80:83]
zm a[80:83],v[168:171],a[160:163],a[80:83]
s_waitcnt vmcnt(15) lgkmcnt(0)
zm a[96:99],v[176:179],a[160:163],a[96:99]
s_barrier
zm a[96:99],v[180:183],a[164:167],a[96:99]
zm a[100:103],v[180:183],a[172:175],a[100:103]
zt v[128:129],v232 offset:0
zl v231,s[28:31],s50 offen offset:3072 lds
zm a[100:103],v[176:179],a[168:171],a[100:103]
zt v[130:131],v232 offset:1024
zm a[104:107],v[176:179],a[176:179],a[104:107]
zm a[104:107],v[180:183],a[180:183],a[104:107]
zt v[132:133],v232 offset:8192
zm a[108:111],v[180:183],a[188:191],a[108:111]
zt v[134:135],v232 offset:9216
zm a[108:111],v[176:179],a[184:187],a[108:111]
zm a[124:127],v[184:187],a[184:187],a[124:127]
zt v[136:137],v232 offset:8
zm a[124:127],v[188:191],a[188:191],a[124:127]
zm a[120:123],v[188:191],a[180:183],a[120:123]
zt v[138:139],v232 offset:1032
zm a[120:123],v[184:187],a[176:179],a[120:123]
zt v[140:141],v232 offset:8200
zm a[116:119],v[184:187],a[168:171],a[116:119]
s_cmp_ge_u32 s69, 0x280000
zm a[116:119],v[188:191],a[172:175],a[116:119]
s_cselect_b32 s50, -6, 0
zt v[142:143],v232 offset:9224
zm a[112:115],v[188:191],a[164:167],a[112:115]
s_lshl_b32 s50, s50, 19
zm a[112:115],v[184:187],a[160:163],a[112:115]
s_waitcnt lgkmcnt(6)
s_add_u32 s50, s50, s69
zt v[144:145],v233 offset:0
zm v[0:3],v[128:131],a[128:131],v[0:3]
s_waitcnt lgkmcnt(5)
s_add_u32 s50, s50, 0x180100
zt v[146:147],v233 offset:1024
zm v[0:3],v[132:135],a[132:135],v[0:3]
s_add_u32 m0, s45, 0xc000
zm v[4:7],v[132:135],a[140:143],v[4:7]
zt v[148:149],v233 offset:8192
zl v228,s[28:31],s50 offen offset:0 lds
zm v[4:7],v[128:131],a[136:139],v[4:7]
zt v[150:151],v233 offset:9216
zl v229,s[28:31],s50 offen offset:1024 lds
zm v[8:11],v[128:131],a[144:147],v[8:11]
zm v[8:11],v[132:135],a[148:151],v[8:11]
zt v[152:153],v233 offset:8
zm v[12:15],v[132:135],a[156:159],v[12:15]
zm v[12:15],v[128:131],a[152:155],v[12:15]
s_waitcnt lgkmcnt(7)
zt v[154:155],v233 offset:1032
zm v[28:31],v[136:139],a[152:155],v[28:31]
s_waitcnt lgkmcnt(6)
zt v[156:157],v233 offset:8200
zl v230,s[28:31],s50 offen offset:2048 lds
zm v[28:31],v[140:143],a[156:159],v[28:31]
zm v[24:27],v[140:143],a[148:151],v[24:27]
zt v[158:159],v233 offset:9224
zm v[24:27],v[136:139],a[144:147],v[24:27]
zm v[20:23],v[136:139],a[136:139],v[20:23]
zt v[160:161],v234 offset:0
zm v[20:23],v[140:143],a[140:143],v[20:23]
zt v[162:163],v234 offset:1024
zm v[16:19],v[140:143],a[132:135],v[16:19]
zl v231,s[28:31],s50 offen offset:3072 lds
zm v[16:19],v[136:139],a[128:131],v[16:19]
s_waitcnt lgkmcnt(8)
zt v[164:165],v234 offset:8192
zm v[32:35],v[144:147],a[128:131],v[32:35]
s_waitcnt lgkmcnt(7)
zt v[166:167],v234 offset:9216
zm v[32:35],v[148:151],a[132:135],v[32:35]
zm v[36:39],v[148:151],a[140:143],v[36:39]
zt v[168:169],v234 offset:8
zm v[36:39],v[144:147],a[136:139],v[36:39]
zm v[40:43],v[144:147],a[144:147],v[40:43]
zt v[170:171],v234 offset:1032
zm v[40:43],v[148:151],a[148:151],v[40:43]
zt v[172:173],v234 offset:8200
buffer_load_dword v254, v214, s[40:43], s70 offen offset:0
zm v[44:47],v[148:151],a[156:159],v[44:47]
zm v[44:47],v[144:147],a[152:155],v[44:47]
s_waitcnt lgkmcnt(9)
zt v[174:175],v234 offset:9224
zm v[60:63],v[152:155],a[152:155],v[60:63]
s_waitcnt lgkmcnt(8)
zm v[60:63],v[156:159],a[156:159],v[60:63]
zt v[176:177],v235 offset:0
zm v[56:59],v[156:159],a[148:151],v[56:59]
zt v[178:179],v235 offset:1024
zm v[56:59],v[152:155],a[144:147],v[56:59]
buffer_load_dword v254, v214, s[40:43], s70 offen offset:1024
zm v[52:55],v[152:155],a[136:139],v[52:55]
zt v[180:181],v235 offset:8192
zm v[52:55],v[156:159],a[140:143],v[52:55]
zt v[182:183],v235 offset:9216
zm v[48:51],v[156:159],a[132:135],v[48:51]
zm v[48:51],v[152:155],a[128:131],v[48:51]
s_waitcnt lgkmcnt(10)
zt v[184:185],v235 offset:8
zm v[64:67],v[160:163],a[128:131],v[64:67]
s_waitcnt lgkmcnt(9)
zm v[64:67],v[164:167],a[132:135],v[64:67]
zt v[186:187],v235 offset:1032
buffer_load_dword v254, v214, s[40:43], s70 offen offset:2048
zm v[68:71],v[164:167],a[140:143],v[68:71]
zt v[188:189],v235 offset:8200
zm v[68:71],v[160:163],a[136:139],v[68:71]
zm v[72:75],v[160:163],a[144:147],v[72:75]
zt v[190:191],v235 offset:9224
zm v[72:75],v[164:167],a[148:151],v[72:75]
zm v[76:79],v[164:167],a[156:159],v[76:79]
zm v[76:79],v[160:163],a[152:155],v[76:79]
s_waitcnt lgkmcnt(10)
buffer_load_dword v254, v214, s[40:43], s70 offen offset:3072
zm v[92:95],v[168:171],a[152:155],v[92:95]
s_waitcnt lgkmcnt(8)
s_add_u32 s70, s70, 0x1000
zm v[92:95],v[172:175],a[156:159],v[92:95]
zm v[88:91],v[172:175],a[148:151],v[88:91]
zm v[88:91],v[168:171],a[144:147],v[88:91]
zm v[84:87],v[168:171],a[136:139],v[84:87]
zm v[84:87],v[172:175],a[140:143],v[84:87]
zm v[80:83],v[172:175],a[132:135],v[80:83]
zm v[80:83],v[168:171],a[128:131],v[80:83]
s_waitcnt vmcnt(12) lgkmcnt(0)
zm v[96:99],v[176:179],a[128:131],v[96:99]
s_barrier
zm v[96:99],v[180:183],a[132:135],v[96:99]
zk a[160:163],v226 offset:0
zm v[100:103],v[180:183],a[140:143],v[100:103]
zk a[164:167],v227 offset:0
zt v[128:129],v232 offset:16384
zm v[100:103],v[176:179],a[136:139],v[100:103]
zk a[168:171],v226 offset:2048
zt v[130:131],v232 offset:17408
zm v[104:107],v[176:179],a[144:147],v[104:107]
zk a[172:175],v227 offset:2048
zm v[104:107],v[180:183],a[148:151],v[104:107]
zk a[176:179],v226 offset:4096
zt v[132:133],v232 offset:24576
zm v[108:111],v[180:183],a[156:159],v[108:111]
zk a[180:183],v227 offset:4096
zt v[134:135],v232 offset:25600
zm v[108:111],v[176:179],a[152:155],v[108:111]
zk a[184:187],v226 offset:6144
zm v[124:127],v[184:187],a[152:155],v[124:127]
zk a[188:191],v227 offset:6144
zt v[136:137],v232 offset:16392
zm v[124:127],v[188:191],a[156:159],v[124:127]
s_cmp_ge_u32 s68, 0x200
zm v[120:123],v[188:191],a[148:151],v[120:123]
s_cselect_b32 s49, -6, 0
zt v[138:139],v232 offset:17416
zm v[120:123],v[184:187],a[144:147],v[120:123]
s_lshl_b32 s49, s49, 7
zt v[140:141],v232 offset:24584
zm v[116:119],v[184:187],a[136:139],v[116:119]
s_add_u32 s49, s49, s68
zm v[116:119],v[188:191],a[140:143],v[116:119]
s_add_u32 s49, s49, 0x200
zt v[142:143],v232 offset:25608
zm v[112:115],v[188:191],a[132:135],v[112:115]
s_waitcnt lgkmcnt(3)
s_add_u32 m0, s46, 0x20000
zm v[112:115],v[184:187],a[128:131],v[112:115]
zt v[144:145],v233 offset:16384
zl v218,s[24:27],s49 offen offset:0 lds
zm a[0:3],v[128:131],a[128:131],a[0:3]
zt v[146:147],v233 offset:17408
zl v219,s[24:27],s49 offen offset:1024 lds
zm a[0:3],v[132:135],a[132:135],a[0:3]
zm a[4:7],v[132:135],a[140:143],a[4:7]
zt v[148:149],v233 offset:24576
zm a[4:7],v[128:131],a[136:139],a[4:7]
zt v[150:151],v233 offset:25600
zl v220,s[24:27],s49 offen offset:2048 lds
zm a[8:11],v[128:131],a[144:147],a[8:11]
zm a[8:11],v[132:135],a[148:151],a[8:11]
zt v[152:153],v233 offset:16392
zm a[12:15],v[132:135],a[156:159],a[12:15]
zm a[12:15],v[128:131],a[152:155],a[12:15]
s_waitcnt lgkmcnt(7)
zt v[154:155],v233 offset:17416
zm a[28:31],v[136:139],a[152:155],a[28:31]
s_waitcnt lgkmcnt(6)
zt v[156:157],v233 offset:24584
zl v221,s[24:27],s49 offen offset:3072 lds
zm a[28:31],v[140:143],a[156:159],a[28:31]
zm a[24:27],v[140:143],a[148:151],a[24:27]
)"
R"(zt v[158:159],v233 offset:25608
zm a[24:27],v[136:139],a[144:147],a[24:27]
zm a[20:23],v[136:139],a[136:139],a[20:23]
zt v[160:161],v234 offset:16384
zm a[20:23],v[140:143],a[140:143],a[20:23]
s_add_u32 m0, s46, 0x21000
zt v[162:163],v234 offset:17408
zm a[16:19],v[140:143],a[132:135],a[16:19]
zl v222,s[24:27],s49 offen offset:0 lds
zm a[16:19],v[136:139],a[128:131],a[16:19]
s_waitcnt lgkmcnt(8)
zt v[164:165],v234 offset:24576
zm a[32:35],v[144:147],a[128:131],a[32:35]
s_waitcnt lgkmcnt(7)
zt v[166:167],v234 offset:25600
zm a[32:35],v[148:151],a[132:135],a[32:35]
zm a[36:39],v[148:151],a[140:143],a[36:39]
zt v[168:169],v234 offset:16392
zm a[36:39],v[144:147],a[136:139],a[36:39]
zl v223,s[24:27],s49 offen offset:1024 lds
zm a[40:43],v[144:147],a[144:147],a[40:43]
zt v[170:171],v234 offset:17416
zm a[40:43],v[148:151],a[148:151],a[40:43]
zt v[172:173],v234 offset:24584
zm a[44:47],v[148:151],a[156:159],a[44:47]
zm a[44:47],v[144:147],a[152:155],a[44:47]
s_waitcnt lgkmcnt(9)
zt v[174:175],v234 offset:25608
zm a[60:63],v[152:155],a[152:155],a[60:63]
s_waitcnt lgkmcnt(8)
zl v224,s[24:27],s49 offen offset:2048 lds
zm a[60:63],v[156:159],a[156:159],a[60:63]
zt v[176:177],v235 offset:16384
zm a[56:59],v[156:159],a[148:151],a[56:59]
zt v[178:179],v235 offset:17408
zm a[56:59],v[152:155],a[144:147],a[56:59]
zm a[52:55],v[152:155],a[136:139],a[52:55]
zt v[180:181],v235 offset:24576
zm a[52:55],v[156:159],a[140:143],a[52:55]
zt v[182:183],v235 offset:25600
zl v225,s[24:27],s49 offen offset:3072 lds
zm a[48:51],v[156:159],a[132:135],a[48:51]
s_cmp_ge_u32 s69, 0x200000
zm a[48:51],v[152:155],a[128:131],a[48:51]
s_waitcnt lgkmcnt(10)
s_cselect_b32 s50, -6, 0
zt v[184:185],v235 offset:16392
zm a[64:67],v[160:163],a[128:131],a[64:67]
s_waitcnt lgkmcnt(9)
s_lshl_b32 s50, s50, 19
zm a[64:67],v[164:167],a[132:135],a[64:67]
s_add_u32 s50, s50, s69
zt v[186:187],v235 offset:17416
zm a[68:71],v[164:167],a[140:143],a[68:71]
s_add_u32 s50, s50, 0x200000
zt v[188:189],v235 offset:24584
zm a[68:71],v[160:163],a[136:139],a[68:71]
s_add_u32 m0, s45, 0
zm a[72:75],v[160:163],a[144:147],a[72:75]
zt v[190:191],v235 offset:25608
zl v228,s[28:31],s50 offen offset:0 lds
zm a[72:75],v[164:167],a[148:151],a[72:75]
zm a[76:79],v[164:167],a[156:159],a[76:79]
zm a[76:79],v[160:163],a[152:155],a[76:79]
s_waitcnt lgkmcnt(10)
zm a[92:95],v[168:171],a[152:155],a[92:95]
s_waitcnt lgkmcnt(8)
zl v229,s[28:31],s50 offen offset:1024 lds
zm a[92:95],v[172:175],a[156:159],a[92:95]
zm a[88:91],v[172:175],a[148:151],a[88:91]
zm a[88:91],v[168:171],a[144:147],a[88:91]
zm a[84:87],v[168:171],a[136:139],a[84:87]
zm a[84:87],v[172:175],a[140:143],a[84:87]
zl v230,s[28:31],s50 offen offset:2048 lds
zm a[80:83],v[172:175],a[132:135],a[80:83]
zm a[80:83],v[168:171],a[128:131],a[80:83]
s_waitcnt vmcnt(19) lgkmcnt(0)
zm a[96:99],v[176:179],a[128:131],a[96:99]
s_barrier
zm a[96:99],v[180:183],a[132:135],a[96:99]
zm a[100:103],v[180:183],a[140:143],a[100:103]
zt v[128:129],v232 offset:32768
zl v231,s[28:31],s50 offen offset:3072 lds
zm a[100:103],v[176:179],a[136:139],a[100:103]
zt v[130:131],v232 offset:33792
zm a[104:107],v[176:179],a[144:147],a[104:107]
zm a[104:107],v[180:183],a[148:151],a[104:107]
zt v[132:133],v232 offset:40960
zm a[108:111],v[180:183],a[156:159],a[108:111]
zt v[134:135],v232 offset:41984
zm a[108:111],v[176:179],a[152:155],a[108:111]
zm a[124:127],v[184:187],a[152:155],a[124:127]
zt v[136:137],v232 offset:32776
zm a[124:127],v[188:191],a[156:159],a[124:127]
zm a[120:123],v[188:191],a[148:151],a[120:123]
zt v[138:139],v232 offset:33800
zm a[120:123],v[184:187],a[144:147],a[120:123]
zt v[140:141],v232 offset:40968
zm a[116:119],v[184:187],a[136:139],a[116:119]
s_cmp_ge_u32 s69, 0x200000
zm a[116:119],v[188:191],a[140:143],a[116:119]
s_cselect_b32 s50, -6, 0
zt v[142:143],v232 offset:41992
zm a[112:115],v[188:191],a[132:135],a[112:115]
s_lshl_b32 s50, s50, 19
zm a[112:115],v[184:187],a[128:131],a[112:115]
s_waitcnt lgkmcnt(6)
s_add_u32 s50, s50, s69
zt v[144:145],v233 offset:32768
zm v[0:3],v[128:131],a[160:163],v[0:3]
s_waitcnt lgkmcnt(5)
s_add_u32 s50, s50, 0x200100
zt v[146:147],v233 offset:33792
zm v[0:3],v[132:135],a[164:167],v[0:3]
s_add_u32 m0, s45, 0x4000
zm v[4:7],v[132:135],a[172:175],v[4:7]
zt v[148:149],v233 offset:40960
zl v228,s[28:31],s50 offen offset:0 lds
zm v[4:7],v[128:131],a[168:171],v[4:7]
zt v[150:151],v233 offset:41984
zl v229,s[28:31],s50 offen offset:1024 lds
zm v[8:11],v[128:131],a[176:179],v[8:11]
zm v[8:11],v[132:135],a[180:183],v[8:11]
zt v[152:153],v233 offset:32776
zm v[12:15],v[132:135],a[188:191],v[12:15]
zm v[12:15],v[128:131],a[184:187],v[12:15]
s_waitcnt lgkmcnt(7)
zt v[154:155],v233 offset:33800
zm v[28:31],v[136:139],a[184:187],v[28:31]
s_waitcnt lgkmcnt(6)
zt v[156:157],v233 offset:40968
zl v230,s[28:31],s50 offen offset:2048 lds
zm v[28:31],v[140:143],a[188:191],v[28:31]
zm v[24:27],v[140:143],a[180:183],v[24:27]
zt v[158:159],v233 offset:41992
zm v[24:27],v[136:139],a[176:179],v[24:27]
zm v[20:23],v[136:139],a[168:171],v[20:23]
zt v[160:161],v234 offset:32768
zm v[20:23],v[140:143],a[172:175],v[20:23]
zt v[162:163],v234 offset:33792
zm v[16:19],v[140:143],a[164:167],v[16:19]
zl v231,s[28:31],s50 offen offset:3072 lds
zm v[16:19],v[136:139],a[160:163],v[16:19]
s_waitcnt lgkmcnt(8)
zt v[164:165],v234 offset:40960
zm v[32:35],v[144:147],a[160:163],v[32:35]
s_waitcnt lgkmcnt(7)
zt v[166:167],v234 offset:41984
zm v[32:35],v[148:151],a[164:167],v[32:35]
zm v[36:39],v[148:151],a[172:175],v[36:39]
zt v[168:169],v234 offset:32776
zm v[36:39],v[144:147],a[168:171],v[36:39]
zm v[40:43],v[144:147],a[176:179],v[40:43]
s_and_b32 m0, s47, 0xffff
zt v[170:171],v234 offset:33800
zm v[40:43],v[148:151],a[180:183],v[40:43]
zt v[172:173],v234 offset:40968
zl v212,s[96:99],s47 offen offset:0 lds
zm v[44:47],v[148:151],a[188:191],v[44:47]
zm v[44:47],v[144:147],a[184:187],v[44:47]
s_waitcnt lgkmcnt(9)
zt v[174:175],v234 offset:41992
zm v[60:63],v[152:155],a[184:187],v[60:63]
s_waitcnt lgkmcnt(8)
zm v[60:63],v[156:159],a[188:191],v[60:63]
s_add_u32 s47, s47, 0x1000
zt v[176:177],v235 offset:32768
zm v[56:59],v[156:159],a[180:183],v[56:59]
s_and_b32 m0, s47, 0xffff
zt v[178:179],v235 offset:33792
zm v[56:59],v[152:155],a[176:179],v[56:59]
zl v212,s[96:99],s47 offen offset:0 lds
zm v[52:55],v[152:155],a[168:171],v[52:55]
zt v[180:181],v235 offset:40960
zm v[52:55],v[156:159],a[172:175],v[52:55]
zt v[182:183],v235 offset:41984
zm v[48:51],v[156:159],a[164:167],v[48:51]
zm v[48:51],v[152:155],a[160:163],v[48:51]
s_waitcnt lgkmcnt(10)
s_add_u32 s47, s47, 0x1000
zt v[184:185],v235 offset:32776
zm v[64:67],v[160:163],a[160:163],v[64:67]
s_waitcnt lgkmcnt(9)
s_and_b32 m0, s47, 0xffff
zm v[64:67],v[164:167],a[164:167],v[64:67]
zt v[186:187],v235 offset:33800
zl v212,s[96:99],s47 offen offset:0 nt lds
zm v[68:71],v[164:167],a[172:175],v[68:71]
zt v[188:189],v235 offset:40968
zm v[68:71],v[160:163],a[168:171],v[68:71]
zm v[72:75],v[160:163],a[176:179],v[72:75]
zt v[190:191],v235 offset:41992
zm v[72:75],v[164:167],a[180:183],v[72:75]
s_add_u32 s47, s47, 0x1000
zm v[76:79],v[164:167],a[188:191],v[76:79]
s_and_b32 m0, s47, 0xffff
zm v[76:79],v[160:163],a[184:187],v[76:79]
s_waitcnt lgkmcnt(10)
zl v212,s[96:99],s47 offen offset:0 nt lds
zm v[92:95],v[168:171],a[184:187],v[92:95]
s_waitcnt lgkmcnt(8)
s_add_u32 s47, s47, 0x1000
zm v[92:95],v[172:175],a[188:191],v[92:95]
zm v[88:91],v[172:175],a[180:183],v[88:91]
zm v[88:91],v[168:171],a[176:179],v[88:91]
zm v[84:87],v[168:171],a[168:171],v[84:87]
zm v[84:87],v[172:175],a[172:175],v[84:87]
zm v[80:83],v[172:175],a[164:167],v[80:83]
zm v[80:83],v[168:171],a[160:163],v[80:83]
s_waitcnt vmcnt(12) lgkmcnt(0)
zm v[96:99],v[176:179],a[160:163],v[96:99]
s_barrier
zm v[96:99],v[180:183],a[164:167],v[96:99]
zk a[128:131],v226 offset:0
zm v[100:103],v[180:183],a[172:175],v[100:103]
zk a[132:135],v227 offset:0
zt v[128:129],v232 offset:49152
zm v[100:103],v[176:179],a[168:171],v[100:103]
zk a[136:139],v226 offset:2048
zt v[130:131],v232 offset:50176
zm v[104:107],v[176:179],a[176:179],v[104:107]
zk a[140:143],v227 offset:2048
zm v[104:107],v[180:183],a[180:183],v[104:107]
zk a[144:147],v226 offset:4096
zt v[132:133],v232 offset:57344
zm v[108:111],v[180:183],a[188:191],v[108:111]
zk a[148:151],v227 offset:4096
zt v[134:135],v232 offset:58368
zm v[108:111],v[176:179],a[184:187],v[108:111]
zk a[152:155],v226 offset:6144
zm v[124:127],v[184:187],a[184:187],v[124:127]
zk a[156:159],v227 offset:6144
zt v[136:137],v232 offset:49160
zm v[124:127],v[188:191],a[188:191],v[124:127]
s_cmp_ge_u32 s68, 0x180
zm v[120:123],v[188:191],a[180:183],v[120:123]
s_cselect_b32 s49, -6, 0
zt v[138:139],v232 offset:50184
zm v[120:123],v[184:187],a[176:179],v[120:123]
s_lshl_b32 s49, s49, 7
zt v[140:141],v232 offset:57352
zm v[116:119],v[184:187],a[168:171],v[116:119]
s_add_u32 s49, s49, s68
zm v[116:119],v[188:191],a[172:175],v[116:119]
s_add_u32 s49, s49, 0x280
zt v[142:143],v232 offset:58376
zm v[112:115],v[188:191],a[164:167],v[112:115]
s_waitcnt lgkmcnt(3)
s_add_u32 m0, s46, 0x20000
zm v[112:115],v[184:187],a[160:163],v[112:115]
zt v[144:145],v233 offset:49152
zl v218,s[24:27],s49 offen offset:0 lds
zm a[0:3],v[128:131],a[160:163],a[0:3]
zt v[146:147],v233 offset:50176
zl v219,s[24:27],s49 offen offset:1024 lds
zm a[0:3],v[132:135],a[164:167],a[0:3]
zm a[4:7],v[132:135],a[172:175],a[4:7]
zt v[148:149],v233 offset:57344
zm a[4:7],v[128:131],a[168:171],a[4:7]
zt v[150:151],v233 offset:58368
zl v220,s[24:27],s49 offen offset:2048 lds
zm a[8:11],v[128:131],a[176:179],a[8:11]
zm a[8:11],v[132:135],a[180:183],a[8:11]
zt v[152:153],v233 offset:49160
zm a[12:15],v[132:135],a[188:191],a[12:15]
zm a[12:15],v[128:131],a[184:187],a[12:15]
s_waitcnt lgkmcnt(7)
zt v[154:155],v233 offset:50184
zm a[28:31],v[136:139],a[184:187],a[28:31]
s_waitcnt lgkmcnt(6)
zt v[156:157],v233 offset:57352
zl v221,s[24:27],s49 offen offset:3072 lds
zm a[28:31],v[140:143],a[188:191],a[28:31]
zm a[24:27],v[140:143],a[180:183],a[24:27]
zt v[158:159],v233 offset:58376
zm a[24:27],v[136:139],a[176:179],a[24:27]
zm a[20:23],v[136:139],a[168:171],a[20:23]
zt v[160:161],v234 offset:49152
zm a[20:23],v[140:143],a[172:175],a[20:23]
s_add_u32 m0, s46, 0x21000
zt v[162:163],v234 offset:50176
zm a[16:19],v[140:143],a[164:167],a[16:19]
zl v222,s[24:27],s49 offen offset:0 lds
zm a[16:19],v[136:139],a[160:163],a[16:19]
s_waitcnt lgkmcnt(8)
zt v[164:165],v234 offset:57344
zm a[32:35],v[144:147],a[160:163],a[32:35]
s_waitcnt lgkmcnt(7)
zt v[166:167],v234 offset:58368
zm a[32:35],v[148:151],a[164:167],a[32:35]
zm a[36:39],v[148:151],a[172:175],a[36:39]
zt v[168:169],v234 offset:49160
zm a[36:39],v[144:147],a[168:171],a[36:39]
zl v223,s[24:27],s49 offen offset:1024 lds
zm a[40:43],v[144:147],a[176:179],a[40:43]
zt v[170:171],v234 offset:50184
zm a[40:43],v[148:151],a[180:183],a[40:43]
zt v[172:173],v234 offset:57352
zm a[44:47],v[148:151],a[188:191],a[44:47]
zm a[44:47],v[144:147],a[184:187],a[44:47]
s_waitcnt lgkmcnt(9)
zt v[174:175],v234 offset:58376
zm a[60:63],v[152:155],a[184:187],a[60:63]
s_waitcnt lgkmcnt(8)
zl v224,s[24:27],s49 offen offset:2048 lds
zm a[60:63],v[156:159],a[188:191],a[60:63]
zt v[176:177],v235 offset:49152
zm a[56:59],v[156:159],a[180:183],a[56:59]
zt v[178:179],v235 offset:50176
zm a[56:59],v[152:155],a[176:179],a[56:59]
zm a[52:55],v[152:155],a[168:171],a[52:55]
zt v[180:181],v235 offset:57344
zm a[52:55],v[156:159],a[172:175],a[52:55]
zt v[182:183],v235 offset:58368
zl v225,s[24:27],s49 offen offset:3072 lds
zm a[48:51],v[156:159],a[164:167],a[48:51]
s_cmp_ge_u32 s69, 0x180000
zm a[48:51],v[152:155],a[160:163],a[48:51]
s_waitcnt lgkmcnt(10)
s_cselect_b32 s50, -6, 0
zt v[184:185],v235 offset:49160
zm a[64:67],v[160:163],a[160:163],a[64:67]
s_waitcnt lgkmcnt(9)
s_lshl_b32 s50, s50, 19
zm a[64:67],v[164:167],a[164:167],a[64:67]
s_add_u32 s50, s50, s69
zt v[186:187],v235 offset:50184
zm a[68:71],v[164:167],a[172:175],a[68:71]
s_add_u32 s50, s50, 0x280000
zt v[188:189],v235 offset:57352
zm a[68:71],v[160:163],a[168:171],a[68:71]
s_add_u32 m0, s45, 0x8000
zm a[72:75],v[160:163],a[176:179],a[72:75]
zt v[190:191],v235 offset:58376
zl v228,s[28:31],s50 offen offset:0 lds
zm a[72:75],v[164:167],a[180:183],a[72:75]
zm a[76:79],v[164:167],a[188:191],a[76:79]
zm a[76:79],v[160:163],a[184:187],a[76:79]
s_waitcnt lgkmcnt(10)
zm a[92:95],v[168:171],a[184:187],a[92:95]
s_waitcnt lgkmcnt(8)
zl v229,s[28:31],s50 offen offset:1024 lds
zm a[92:95],v[172:175],a[188:191],a[92:95]
zm a[88:91],v[172:175],a[180:183],a[88:91]
zm a[88:91],v[168:171],a[176:179],a[88:91]
zm a[84:87],v[168:171],a[168:171],a[84:87]
zm a[84:87],v[172:175],a[172:175],a[84:87]
zl v230,s[28:31],s50 offen offset:2048 lds
zm a[80:83],v[172:175],a[164:167],a[80:83]
zm a[80:83],v[168:171],a[160:163],a[80:83]
s_waitcnt vmcnt(19) lgkmcnt(0)
zm a[96:99],v[176:179],a[160:163],a[96:99]
s_barrier
zm a[96:99],v[180:183],a[164:167],a[96:99]
zm a[100:103],v[180:183],a[172:175],a[100:103]
zt v[128:129],v232 offset:0
zl v231,s[28:31],s50 offen offset:3072 lds
zm a[100:103],v[176:179],a[168:171],a[100:103]
zt v[130:131],v232 offset:1024
zm a[104:107],v[176:179],a[176:179],a[104:107]
zm a[104:107],v[180:183],a[180:183],a[104:107]
zt v[132:133],v232 offset:8192
zm a[108:111],v[180:183],a[188:191],a[108:111]
zt v[134:135],v232 offset:9216
zm a[108:111],v[176:179],a[184:187],a[108:111]
zm a[124:127],v[184:187],a[184:187],a[124:127]
zt v[136:137],v232 offset:8
zm a[124:127],v[188:191],a[188:191],a[124:127]
zm a[120:123],v[188:191],a[180:183],a[120:123]
zt v[138:139],v232 offset:1032
zm a[120:123],v[184:187],a[176:179],a[120:123]
zt v[140:141],v232 offset:8200
zm a[116:119],v[184:187],a[168:171],a[116:119]
s_cmp_ge_u32 s69, 0x180000
zm a[116:119],v[188:191],a[172:175],a[116:119]
s_cselect_b32 s50, -6, 0
zt v[142:143],v232 offset:9224
zm a[112:115],v[188:191],a[164:167],a[112:115]
s_lshl_b32 s50, s50, 19
zm a[112:115],v[184:187],a[160:163],a[112:115]
s_waitcnt lgkmcnt(6)
s_add_u32 s50, s50, s69
zt v[144:145],v233 offset:0
zm v[0:3],v[128:131],a[128:131],v[0:3]
s_waitcnt lgkmcnt(5)
s_add_u32 s50, s50, 0x280100
zt v[146:147],v233 offset:1024
zm v[0:3],v[132:135],a[132:135],v[0:3]
s_add_u32 m0, s45, 0xc000
zm v[4:7],v[132:135],a[140:143],v[4:7]
zt v[148:149],v233 offset:8192
zl v228,s[28:31],s50 offen offset:0 lds
zm v[4:7],v[128:131],a[136:139],v[4:7]
zt v[150:151],v233 offset:9216
zl v229,s[28:31],s50 offen offset:1024 lds
zm v[8:11],v[128:131],a[144:147],v[8:11]
zm v[8:11],v[132:135],a[148:151],v[8:11]
zt v[152:153],v233 offset:8
zm v[12:15],v[132:135],a[156:159],v[12:15]
zm v[12:15],v[128:131],a[152:155],v[12:15]
s_waitcnt lgkmcnt(7)
zt v[154:155],v233 offset:1032
zm v[28:31],v[136:139],a[152:155],v[28:31]
s_waitcnt lgkmcnt(6)
zt v[156:157],v233 offset:8200
zl v230,s[28:31],s50 offen offset:2048 lds
zm v[28:31],v[140:143],a[156:159],v[28:31]
zm v[24:27],v[140:143],a[148:151],v[24:27]
zt v[158:159],v233 offset:9224
zm v[24:27],v[136:139],a[144:147],v[24:27]
zm v[20:23],v[136:139],a[136:139],v[20:23]
zt v[160:161],v234 offset:0
zm v[20:23],v[140:143],a[140:143],v[20:23]
zt v[162:163],v234 offset:1024
zm v[16:19],v[140:143],a[132:135],v[16:19]
zl v231,s[28:31],s50 offen offset:3072 lds
zm v[16:19],v[136:139],a[128:131],v[16:19]
s_waitcnt lgkmcnt(8)
zt v[164:165],v234 offset:8192
zm v[32:35],v[144:147],a[128:131],v[32:35]
s_waitcnt lgkmcnt(7)
zt v[166:167],v234 offset:9216
zm v[32:35],v[148:151],a[132:135],v[32:35]
zm v[36:39],v[148:151],a[140:143],v[36:39]
zt v[168:169],v234 offset:8
zm v[36:39],v[144:147],a[136:139],v[36:39]
zm v[40:43],v[144:147],a[144:147],v[40:43]
s_and_b32 m0, s47, 0xffff
zt v[170:171],v234 offset:1032
zm v[40:43],v[148:151],a[148:151],v[40:43]
zt v[172:173],v234 offset:8200
zl v212,s[96:99],s47 offen offset:0 lds
zm v[44:47],v[148:151],a[156:159],v[44:47]
zm v[44:47],v[144:147],a[152:155],v[44:47]
s_waitcnt lgkmcnt(9)
zt v[174:175],v234 offset:9224
zm v[60:63],v[152:155],a[152:155],v[60:63]
s_waitcnt lgkmcnt(8)
zm v[60:63],v[156:159],a[156:159],v[60:63]
s_add_u32 s47, s47, 0x1000
zt v[176:177],v235 offset:0
zm v[56:59],v[156:159],a[148:151],v[56:59]
s_and_b32 m0, s47, 0xffff
zt v[178:179],v235 offset:1024
zm v[56:59],v[152:155],a[144:147],v[56:59]
zl v212,s[96:99],s47 offen offset:0 lds
zm v[52:55],v[152:155],a[136:139],v[52:55]
zt v[180:181],v235 offset:8192
zm v[52:55],v[156:159],a[140:143],v[52:55]
zt v[182:183],v235 offset:9216
zm v[48:51],v[156:159],a[132:135],v[48:51]
zm v[48:51],v[152:155],a[128:131],v[48:51]
s_waitcnt lgkmcnt(10)
s_add_u32 s47, s47, 0x1000
zt v[184:185],v235 offset:8
zm v[64:67],v[160:163],a[128:131],v[64:67]
s_waitcnt lgkmcnt(9)
s_and_b32 m0, s47, 0xffff
zm v[64:67],v[164:167],a[132:135],v[64:67]
zt v[186:187],v235 offset:1032
zl v212,s[96:99],s47 offen offset:0 nt lds
zm v[68:71],v[164:167],a[140:143],v[68:71]
zt v[188:189],v235 offset:8200
zm v[68:71],v[160:163],a[136:139],v[68:71]
zm v[72:75],v[160:163],a[144:147],v[72:75]
zt v[190:191],v235 offset:9224
zm v[72:75],v[164:167],a[148:151],v[72:75]
s_add_u32 s47, s47, 0x1000
zm v[76:79],v[164:167],a[156:159],v[76:79]
s_and_b32 m0, s47, 0xffff
zm v[76:79],v[160:163],a[152:155],v[76:79]
s_waitcnt lgkmcnt(10)
zl v212,s[96:99],s47 offen offset:0 nt lds
zm v[92:95],v[168:171],a[152:155],v[92:95]
s_waitcnt lgkmcnt(8)
s_add_u32 s47, s47, 0x1000
zm v[92:95],v[172:175],a[156:159],v[92:95]
zm v[88:91],v[172:175],a[148:151],v[88:91]
zm v[88:91],v[168:171],a[144:147],v[88:91]
zm v[84:87],v[168:171],a[136:139],v[84:87]
zm v[84:87],v[172:175],a[140:143],v[84:87]
zm v[80:83],v[172:175],a[132:135],v[80:83]
zm v[80:83],v[168:171],a[128:131],v[80:83]
s_waitcnt vmcnt(12) lgkmcnt(0)
zm v[96:99],v[176:179],a[128:131],v[96:99]
s_barrier
zm v[96:99],v[180:183],a[132:135],v[96:99]
zk a[160:163],v226 offset:0
zm v[100:103],v[180:183],a[140:143],v[100:103]
zk a[164:167],v227 offset:0
zt v[128:129],v232 offset:16384
zm v[100:103],v[176:179],a[136:139],v[100:103]
zk a[168:171],v226 offset:2048
zt v[130:131],v232 offset:17408
zm v[104:107],v[176:179],a[144:147],v[104:107]
zk a[172:175],v227 offset:2048
zm v[104:107],v[180:183],a[148:151],v[104:107]
zk a[176:179],v226 offset:4096
zt v[132:133],v232 offset:24576
zm v[108:111],v[180:183],a[156:159],v[108:111]
zk a[180:183],v227 offset:4096
zt v[134:135],v232 offset:25600
zm v[108:111],v[176:179],a[152:155],v[108:111]
zk a[184:187],v226 offset:6144
zm v[124:127],v[184:187],a[152:155],v[124:127]
zk a[188:191],v227 offset:6144
zt v[136:137],v232 offset:16392
zm v[124:127],v[188:191],a[156:159],v[124:127]
s_cmp_ge_u32 s68, 0x100
zm v[120:123],v[188:191],a[148:151],v[120:123]
s_cselect_b32 s49, -6, 0
zt v[138:139],v232 offset:17416
zm v[120:123],v[184:187],a[144:147],v[120:123]
s_lshl_b32 s49, s49, 7
zt v[140:141],v232 offset:24584
zm v[116:119],v[184:187],a[136:139],v[116:119]
s_add_u32 s49, s49, s68
zm v[116:119],v[188:191],a[140:143],v[116:119]
s_add_u32 s49, s49, 0x300
zt v[142:143],v232 offset:25608
zm v[112:115],v[188:191],a[132:135],v[112:115]
s_waitcnt lgkmcnt(3)
s_add_u32 m0, s46, 0x20000
zm v[112:115],v[184:187],a[128:131],v[112:115]
zt v[144:145],v233 offset:16384
zl v218,s[24:27],s49 offen offset:0 lds
zm a[0:3],v[128:131],a[128:131],a[0:3]
zt v[146:147],v233 offset:17408
zl v219,s[24:27],s49 offen offset:1024 lds
zm a[0:3],v[132:135],a[132:135],a[0:3]
zm a[4:7],v[132:135],a[140:143],a[4:7]
zt v[148:149],v233 offset:24576
zm a[4:7],v[128:131],a[136:139],a[4:7]
zt v[150:151],v233 offset:25600
zl v220,s[24:27],s49 offen offset:2048 lds
zm a[8:11],v[128:131],a[144:147],a[8:11]
zm a[8:11],v[132:135],a[148:151],a[8:11]
zt v[152:153],v233 offset:16392
zm a[12:15],v[132:135],a[156:159],a[12:15]
zm a[12:15],v[128:131],a[152:155],a[12:15]
s_waitcnt lgkmcnt(7)
zt v[154:155],v233 offset:17416
zm a[28:31],v[136:139],a[152:155],a[28:31]
s_waitcnt lgkmcnt(6)
zt v[156:157],v233 offset:24584
zl v221,s[24:27],s49 offen offset:3072 lds
zm a[28:31],v[140:143],a[156:159],a[28:31]
zm a[24:27],v[140:143],a[148:151],a[24:27]
zt v[158:159],v233 offset:25608
zm a[24:27],v[136:139],a[144:147],a[24:27]
zm a[20:23],v[136:139],a[136:139],a[20:23]
zt v[160:161],v234 offset:16384
zm a[20:23],v[140:143],a[140:143],a[20:23]
s_add_u32 m0, s46, 0x21000
zt v[162:163],v234 offset:17408
zm a[16:19],v[140:143],a[132:135],a[16:19]
zl v222,s[24:27],s49 offen offset:0 lds
zm a[16:19],v[136:139],a[128:131],a[16:19]
s_waitcnt lgkmcnt(8)
zt v[164:165],v234 offset:24576
zm a[32:35],v[144:147],a[128:131],a[32:35]
s_waitcnt lgkmcnt(7)
zt v[166:167],v234 offset:25600
zm a[32:35],v[148:151],a[132:135],a[32:35]
zm a[36:39],v[148:151],a[140:143],a[36:39]
zt v[168:169],v234 offset:16392
zm a[36:39],v[144:147],a[136:139],a[36:39]
zl v223,s[24:27],s49 offen offset:1024 lds
zm a[40:43],v[144:147],a[144:147],a[40:43]
zt v[170:171],v234 offset:17416
zm a[40:43],v[148:151],a[148:151],a[40:43]
zt v[172:173],v234 offset:24584
zm a[44:47],v[148:151],a[156:159],a[44:47]
zm a[44:47],v[144:147],a[152:155],a[44:47]
s_waitcnt lgkmcnt(9)
zt v[174:175],v234 offset:25608
zm a[60:63],v[152:155],a[152:155],a[60:63]
s_waitcnt lgkmcnt(8)
zl v224,s[24:27],s49 offen offset:2048 lds
zm a[60:63],v[156:159],a[156:159],a[60:63]
zt v[176:177],v235 offset:16384
zm a[56:59],v[156:159],a[148:151],a[56:59]
zt v[178:179],v235 offset:17408
zm a[56:59],v[152:155],a[144:147],a[56:59]
zm a[52:55],v[152:155],a[136:139],a[52:55]
zt v[180:181],v235 offset:24576
zm a[52:55],v[156:159],a[140:143],a[52:55]
zt v[182:183],v235 offset:25600
zl v225,s[24:27],s49 offen offset:3072 lds
zm a[48:51],v[156:159],a[132:135],a[48:51]
s_cmp_ge_u32 s69, 0x100000
zm a[48:51],v[152:155],a[128:131],a[48:51]
s_waitcnt lgkmcnt(10)
s_cselect_b32 s50, -6, 0
zt v[184:185],v235 offset:16392
zm a[64:67],v[160:163],a[128:131],a[64:67]
s_waitcnt lgkmcnt(9)
s_lshl_b32 s50, s50, 19
zm a[64:67],v[164:167],a[132:135],a[64:67]
s_add_u32 s50, s50, s69
zt v[186:187],v235 offset:17416
zm a[68:71],v[164:167],a[140:143],a[68:71]
s_add_u32 s50, s50, 0x300000
zt v[188:189],v235 offset:24584
zm a[68:71],v[160:163],a[136:139],a[68:71]
s_add_u32 m0, s45, 0
zm a[72:75],v[160:163],a[144:147],a[72:75]
zt v[190:191],v235 offset:25608
zl v228,s[28:31],s50 offen offset:0 lds
zm a[72:75],v[164:167],a[148:151],a[72:75]
zm a[76:79],v[164:167],a[156:159],a[76:79]
zm a[76:79],v[160:163],a[152:155],a[76:79]
s_waitcnt lgkmcnt(10)
zm a[92:95],v[168:171],a[152:155],a[92:95]
s_waitcnt lgkmcnt(8)
zl v229,s[28:31],s50 offen offset:1024 lds
zm a[92:95],v[172:175],a[156:159],a[92:95]
zm a[88:91],v[172:175],a[148:151],a[88:91]
zm a[88:91],v[168:171],a[144:147],a[88:91]
zm a[84:87],v[168:171],a[136:139],a[84:87]
zm a[84:87],v[172:175],a[140:143],a[84:87]
zl v230,s[28:31],s50 offen offset:2048 lds
zm a[80:83],v[172:175],a[132:135],a[80:83]
zm a[80:83],v[168:171],a[128:131],a[80:83]
s_waitcnt vmcnt(19) lgkmcnt(0)
zm a[96:99],v[176:179],a[128:131],a[96:99]
s_barrier
zm a[96:99],v[180:183],a[132:135],a[96:99]
zm a[100:103],v[180:183],a[140:143],a[100:103]
zt v[128:129],v232 offset:32768
zl v231,s[28:31],s50 offen offset:3072 lds
zm a[100:103],v[176:179],a[136:139],a[100:103]
zt v[130:131],v232 offset:33792
zm a[104:107],v[176:179],a[144:147],a[104:107]
zm a[104:107],v[180:183],a[148:151],a[104:107]
zt v[132:133],v232 offset:40960
zm a[108:111],v[180:183],a[156:159],a[108:111]
zt v[134:135],v232 offset:41984
zm a[108:111],v[176:179],a[152:155],a[108:111]
zm a[124:127],v[184:187],a[152:155],a[124:127]
zt v[136:137],v232 offset:32776
zm a[124:127],v[188:191],a[156:159],a[124:127]
zm a[120:123],v[188:191],a[148:151],a[120:123]
zt v[138:139],v232 offset:33800
zm a[120:123],v[184:187],a[144:147],a[120:123]
zt v[140:141],v232 offset:40968
zm a[116:119],v[184:187],a[136:139],a[116:119]
s_cmp_ge_u32 s69, 0x80000
zm a[116:119],v[188:191],a[140:143],a[116:119]
s_cselect_b32 s50, -6, 0
zt v[142:143],v232 offset:41992
zm a[112:115],v[188:191],a[132:135],a[112:115]
s_lshl_b32 s50, s50, 19
zm a[112:115],v[184:187],a[128:131],a[112:115]
s_waitcnt lgkmcnt(6)
s_add_u32 s50, s50, s69
zt v[144:145],v233 offset:32768
zm v[0:3],v[128:131],a[160:163],v[0:3]
s_waitcnt lgkmcnt(5)
s_add_u32 s50, s50, 0x380000
zt v[146:147],v233 offset:33792
zm v[0:3],v[132:135],a[164:167],v[0:3]
s_add_u32 m0, s45, 0x4000
zm v[4:7],v[132:135],a[172:175],v[4:7]
zt v[148:149],v233 offset:40960
zl v228,s[28:31],s50 offen offset:0 lds
zm v[4:7],v[128:131],a[168:171],v[4:7]
zt v[150:151],v233 offset:41984
zl v229,s[28:31],s50 offen offset:1024 lds
zm v[8:11],v[128:131],a[176:179],v[8:11]
zm v[8:11],v[132:135],a[180:183],v[8:11]
zt v[152:153],v233 offset:32776
zm v[12:15],v[132:135],a[188:191],v[12:15]
zm v[12:15],v[128:131],a[184:187],v[12:15]
s_waitcnt lgkmcnt(7)
zt v[154:155],v233 offset:33800
zm v[28:31],v[136:139],a[184:187],v[28:31]
s_waitcnt lgkmcnt(6)
zt v[156:157],v233 offset:40968
zl v230,s[28:31],s50 offen offset:2048 lds
zm v[28:31],v[140:143],a[188:191],v[28:31]
zm v[24:27],v[140:143],a[180:183],v[24:27]
zt v[158:159],v233 offset:41992
zm v[24:27],v[136:139],a[176:179],v[24:27]
zm v[20:23],v[136:139],a[168:171],v[20:23]
zt v[160:161],v234 offset:32768
zm v[20:23],v[140:143],a[172:175],v[20:23]
zt v[162:163],v234 offset:33792
zm v[16:19],v[140:143],a[164:167],v[16:19]
zl v231,s[28:31],s50 offen offset:3072 lds
zm v[16:19],v[136:139],a[160:163],v[16:19]
s_waitcnt lgkmcnt(8)
zt v[164:165],v234 offset:40960
zm v[32:35],v[144:147],a[160:163],v[32:35]
s_waitcnt lgkmcnt(7)
zt v[166:167],v234 offset:41984
zm v[32:35],v[148:151],a[164:167],v[32:35]
zm v[36:39],v[148:151],a[172:175],v[36:39]
zt v[168:169],v234 offset:32776
zm v[36:39],v[144:147],a[168:171],v[36:39]
zm v[40:43],v[144:147],a[176:179],v[40:43]
s_and_b32 m0, s47, 0xffff
zt v[170:171],v234 offset:33800
zm v[40:43],v[148:151],a[180:183],v[40:43]
zt v[172:173],v234 offset:40968
zl v212,s[96:99],s47 offen offset:0 lds
zm v[44:47],v[148:151],a[188:191],v[44:47]
zm v[44:47],v[144:147],a[184:187],v[44:47]
s_waitcnt lgkmcnt(9)
zt v[174:175],v234 offset:41992
zm v[60:63],v[152:155],a[184:187],v[60:63]
s_waitcnt lgkmcnt(8)
zm v[60:63],v[156:159],a[188:191],v[60:63]
s_add_u32 s47, s47, 0x1000
zt v[176:177],v235 offset:32768
zm v[56:59],v[156:159],a[180:183],v[56:59]
s_and_b32 m0, s47, 0xffff
zt v[178:179],v235 offset:33792
zm v[56:59],v[152:155],a[176:179],v[56:59]
zl v212,s[96:99],s47 offen offset:0 lds
zm v[52:55],v[152:155],a[168:171],v[52:55]
zt v[180:181],v235 offset:40960
zm v[52:55],v[156:159],a[172:175],v[52:55]
zt v[182:183],v235 offset:41984
zm v[48:51],v[156:159],a[164:167],v[48:51]
zm v[48:51],v[152:155],a[160:163],v[48:51]
s_waitcnt lgkmcnt(10)
s_add_u32 s47, s47, 0x1000
zt v[184:185],v235 offset:32776
zm v[64:67],v[160:163],a[160:163],v[64:67]
s_waitcnt lgkmcnt(9)
s_and_b32 m0, s47, 0xffff
zm v[64:67],v[164:167],a[164:167],v[64:67]
zt v[186:187],v235 offset:33800
zl v212,s[96:99],s47 offen offset:0 nt lds
zm v[68:71],v[164:167],a[172:175],v[68:71]
zt v[188:189],v235 offset:40968
zm v[68:71],v[160:163],a[168:171],v[68:71]
zm v[72:75],v[160:163],a[176:179],v[72:75]
zt v[190:191],v235 offset:41992
zm v[72:75],v[164:167],a[180:183],v[72:75]
s_add_u32 s47, s47, 0x1000
zm v[76:79],v[164:167],a[188:191],v[76:79]
s_and_b32 m0, s47, 0xffff
zm v[76:79],v[160:163],a[184:187],v[76:79]
s_waitcnt lgkmcnt(10)
zl v212,s[96:99],s47 offen offset:0 nt lds
zm v[92:95],v[168:171],a[184:187],v[92:95]
s_waitcnt lgkmcnt(8)
s_add_u32 s47, s47, 0x1000
zm v[92:95],v[172:175],a[188:191],v[92:95]
zm v[88:91],v[172:175],a[180:183],v[88:91]
zm v[88:91],v[168:171],a[176:179],v[88:91]
zm v[84:87],v[168:171],a[168:171],v[84:87]
zm v[84:87],v[172:175],a[172:175],v[84:87]
zm v[80:83],v[172:175],a[164:167],v[80:83]
zm v[80:83],v[168:171],a[160:163],v[80:83]
s_waitcnt vmcnt(12) lgkmcnt(0)
zm v[96:99],v[176:179],a[160:163],v[96:99]
s_barrier
zm v[96:99],v[180:183],a[164:167],v[96:99]
zk a[128:131],v226 offset:0
zm v[100:103],v[180:183],a[172:175],v[100:103]
zk a[132:135],v227 offset:0
zt v[128:129],v232 offset:49152
zm v[100:103],v[176:179],a[168:171],v[100:103]
zk a[136:139],v226 offset:2048
zt v[130:131],v232 offset:50176
zm v[104:107],v[176:179],a[176:179],v[104:107]
zk a[140:143],v227 offset:2048
zm v[104:107],v[180:183],a[180:183],v[104:107]
zk a[144:147],v226 offset:4096
zt v[132:133],v232 offset:57344
zm v[108:111],v[180:183],a[188:191],v[108:111]
zk a[148:151],v227 offset:4096
zt v[134:135],v232 offset:58368
zm v[108:111],v[176:179],a[184:187],v[108:111]
zk a[152:155],v226 offset:6144
zm v[124:127],v[184:187],a[184:187],v[124:127]
zk a[156:159],v227 offset:6144
zt v[136:137],v232 offset:49160
zm v[124:127],v[188:191],a[188:191],v[124:127]
s_cmp_ge_u32 s68, 0x80
zm v[120:123],v[188:191],a[180:183],v[120:123]
s_cselect_b32 s49, -6, 0
zt v[138:139],v232 offset:50184
zm v[120:123],v[184:187],a[176:179],v[120:123]
s_lshl_b32 s49, s49, 7
zt v[140:141],v232 offset:57352
zm v[116:119],v[184:187],a[168:171],v[116:119]
s_add_u32 s49, s49, s68
zm v[116:119],v[188:191],a[172:175],v[116:119]
s_add_u32 s49, s49, 0x380
zt v[142:143],v232 offset:58376
zm v[112:115],v[188:191],a[164:167],v[112:115]
s_waitcnt lgkmcnt(3)
s_add_u32 m0, s46, 0x20000
zm v[112:115],v[184:187],a[160:163],v[112:115]
zt v[144:145],v233 offset:49152
zl v218,s[24:27],s49 offen offset:0 lds
zm a[0:3],v[128:131],a[160:163],a[0:3]
zt v[146:147],v233 offset:50176
zl v219,s[24:27],s49 offen offset:1024 lds
zm a[0:3],v[132:135],a[164:167],a[0:3]
zm a[4:7],v[132:135],a[172:175],a[4:7]
zt v[148:149],v233 offset:57344
zm a[4:7],v[128:131],a[168:171],a[4:7]
zt v[150:151],v233 offset:58368
zl v220,s[24:27],s49 offen offset:2048 lds
zm a[8:11],v[128:131],a[176:179],a[8:11]
zm a[8:11],v[132:135],a[180:183],a[8:11]
zt v[152:153],v233 offset:49160
zm a[12:15],v[132:135],a[188:191],a[12:15]
zm a[12:15],v[128:131],a[184:187],a[12:15]
s_waitcnt lgkmcnt(7)
zt v[154:155],v233 offset:50184
zm a[28:31],v[136:139],a[184:187],a[28:31]
s_waitcnt lgkmcnt(6)
zt v[156:157],v233 offset:57352
zl v221,s[24:27],s49 offen offset:3072 lds
zm a[28:31],v[140:143],a[188:191],a[28:31]
zm a[24:27],v[140:143],a[180:183],a[24:27]
zt v[158:159],v233 offset:58376
zm a[24:27],v[136:139],a[176:179],a[24:27]
zm a[20:23],v[136:139],a[168:171],a[20:23]
zt v[160:161],v234 offset:49152
zm a[20:23],v[140:143],a[172:175],a[20:23]
s_add_u32 m0, s46, 0x21000
zt v[162:163],v234 offset:50176
zm a[16:19],v[140:143],a[164:167],a[16:19]
zl v222,s[24:27],s49 offen offset:0 lds
zm a[16:19],v[136:139],a[160:163],a[16:19]
s_waitcnt lgkmcnt(8)
zt v[164:165],v234 offset:57344
zm a[32:35],v[144:147],a[160:163],a[32:35]
s_waitcnt lgkmcnt(7)
zt v[166:167],v234 offset:58368
zm a[32:35],v[148:151],a[164:167],a[32:35]
zm a[36:39],v[148:151],a[172:175],a[36:39]
zt v[168:169],v234 offset:49160
zm a[36:39],v[144:147],a[168:171],a[36:39]
zl v223,s[24:27],s49 offen offset:1024 lds
zm a[40:43],v[144:147],a[176:179],a[40:43]
zt v[170:171],v234 offset:50184
zm a[40:43],v[148:151],a[180:183],a[40:43]
zt v[172:173],v234 offset:57352
zm a[44:47],v[148:151],a[188:191],a[44:47]
zm a[44:47],v[144:147],a[184:187],a[44:47]
s_waitcnt lgkmcnt(9)
zt v[174:175],v234 offset:58376
zm a[60:63],v[152:155],a[184:187],a[60:63]
s_waitcnt lgkmcnt(8)
zl v224,s[24:27],s49 offen offset:2048 lds
zm a[60:63],v[156:159],a[188:191],a[60:63]
zt v[176:177],v235 offset:49152
zm a[56:59],v[156:159],a[180:183],a[56:59]
zt v[178:179],v235 offset:50176
zm a[56:59],v[152:155],a[176:179],a[56:59]
zm a[52:55],v[152:155],a[168:171],a[52:55]
zt v[180:181],v235 offset:57344
zm a[52:55],v[156:159],a[172:175],a[52:55]
zt v[182:183],v235 offset:58368
zl v225,s[24:27],s49 offen offset:3072 lds
zm a[48:51],v[156:159],a[164:167],a[48:51]
s_cmp_ge_u32 s69, 0x100000
zm a[48:51],v[152:155],a[160:163],a[48:51]
s_waitcnt lgkmcnt(10)
s_cselect_b32 s50, -6, 0
zt v[184:185],v235 offset:49160
zm a[64:67],v[160:163],a[160:163],a[64:67]
s_waitcnt lgkmcnt(9)
s_lshl_b32 s50, s50, 19
zm a[64:67],v[164:167],a[164:167],a[64:67]
s_add_u32 s50, s50, s69
zt v[186:187],v235 offset:50184
zm a[68:71],v[164:167],a[172:175],a[68:71]
s_add_u32 s50, s50, 0x300100
zt v[188:189],v235 offset:57352
zm a[68:71],v[160:163],a[168:171],a[68:71]
s_add_u32 m0, s45, 0x8000
zm a[72:75],v[160:163],a[176:179],a[72:75]
zt v[190:191],v235 offset:58376
zl v228,s[28:31],s50 offen offset:0 lds
zm a[72:75],v[164:167],a[180:183],a[72:75]
zm a[76:79],v[164:167],a[188:191],a[76:79]
zm a[76:79],v[160:163],a[184:187],a[76:79]
s_waitcnt lgkmcnt(10)
zm a[92:95],v[168:171],a[184:187],a[92:95]
s_waitcnt lgkmcnt(8)
zl v229,s[28:31],s50 offen offset:1024 lds
zm a[92:95],v[172:175],a[188:191],a[92:95]
zm a[88:91],v[172:175],a[180:183],a[88:91]
zm a[88:91],v[168:171],a[176:179],a[88:91]
zm a[84:87],v[168:171],a[168:171],a[84:87]
zm a[84:87],v[172:175],a[172:175],a[84:87]
zl v230,s[28:31],s50 offen offset:2048 lds
zm a[80:83],v[172:175],a[164:167],a[80:83]
zm a[80:83],v[168:171],a[160:163],a[80:83]
s_waitcnt vmcnt(19) lgkmcnt(0)
zm a[96:99],v[176:179],a[160:163],a[96:99]
s_barrier
zm a[96:99],v[180:183],a[164:167],a[96:99]
zm a[100:103],v[180:183],a[172:175],a[100:103]
zt v[128:129],v232 offset:0
zl v231,s[28:31],s50 offen offset:3072 lds
zm a[100:103],v[176:179],a[168:171],a[100:103]
zt v[130:131],v232 offset:1024
zm a[104:107],v[176:179],a[176:179],a[104:107]
zm a[104:107],v[180:183],a[180:183],a[104:107]
zt v[132:133],v232 offset:8192
zm a[108:111],v[180:183],a[188:191],a[108:111]
zt v[134:135],v232 offset:9216
zm a[108:111],v[176:179],a[184:187],a[108:111]
zm a[124:127],v[184:187],a[184:187],a[124:127]
zt v[136:137],v232 offset:8
zm a[124:127],v[188:191],a[188:191],a[124:127]
zm a[120:123],v[188:191],a[180:183],a[120:123]
zt v[138:139],v232 offset:1032
zm a[120:123],v[184:187],a[176:179],a[120:123]
zt v[140:141],v232 offset:8200
zm a[116:119],v[184:187],a[168:171],a[116:119]
s_cmp_ge_u32 s69, 0x80000
zm a[116:119],v[188:191],a[172:175],a[116:119]
s_cselect_b32 s50, -6, 0
zt v[142:143],v232 offset:9224
zm a[112:115],v[188:191],a[164:167],a[112:115]
s_lshl_b32 s50, s50, 19
zm a[112:115],v[184:187],a[160:163],a[112:115]
s_waitcnt lgkmcnt(6)
s_add_u32 s50, s50, s69
zt v[144:145],v233 offset:0
zm v[0:3],v[128:131],a[128:131],v[0:3]
s_waitcnt lgkmcnt(5)
s_add_u32 s50, s50, 0x380100
zt v[146:147],v233 offset:1024
zm v[0:3],v[132:135],a[132:135],v[0:3]
s_add_u32 m0, s45, 0xc000
zm v[4:7],v[132:135],a[140:143],v[4:7]
zt v[148:149],v233 offset:8192
zl v228,s[28:31],s50 offen offset:0 lds
zm v[4:7],v[128:131],a[136:139],v[4:7]
zt v[150:151],v233 offset:9216
zl v229,s[28:31],s50 offen offset:1024 lds
zm v[8:11],v[128:131],a[144:147],v[8:11]
zm v[8:11],v[132:135],a[148:151],v[8:11]
zt v[152:153],v233 offset:8
zm v[12:15],v[132:135],a[156:159],v[12:15]
zm v[12:15],v[128:131],a[152:155],v[12:15]
s_waitcnt lgkmcnt(7)
zt v[154:155],v233 offset:1032
zl v230,s[28:31],s50 offen offset:2048 lds
zm v[28:31],v[136:139],a[152:155],v[28:31]
s_waitcnt lgkmcnt(6)
zt v[156:157],v233 offset:8200
zm v[28:31],v[140:143],a[156:159],v[28:31]
zm v[24:27],v[140:143],a[148:151],v[24:27]
zt v[158:159],v233 offset:9224
zm v[24:27],v[136:139],a[144:147],v[24:27]
zm v[20:23],v[136:139],a[136:139],v[20:23]
zt v[160:161],v234 offset:0
zl v231,s[28:31],s50 offen offset:3072 lds
zm v[20:23],v[140:143],a[140:143],v[20:23]
zt v[162:163],v234 offset:1024
zm v[16:19],v[140:143],a[132:135],v[16:19]
zm v[16:19],v[136:139],a[128:131],v[16:19]
s_waitcnt lgkmcnt(8)
zt v[164:165],v234 offset:8192
zm v[32:35],v[144:147],a[128:131],v[32:35]
s_waitcnt lgkmcnt(7)
zt v[166:167],v234 offset:9216
zm v[32:35],v[148:151],a[132:135],v[32:35]
s_and_b32 m0, s47, 0xffff
zm v[36:39],v[148:151],a[140:143],v[36:39]
zt v[168:169],v234 offset:8
zl v212,s[96:99],s47 offen offset:0 lds
zm v[36:39],v[144:147],a[136:139],v[36:39]
zm v[40:43],v[144:147],a[144:147],v[40:43]
zt v[170:171],v234 offset:1032
zm v[40:43],v[148:151],a[148:151],v[40:43]
s_add_u32 s47, s47, 0x1000
zt v[172:173],v234 offset:8200
zm v[44:47],v[148:151],a[156:159],v[44:47]
s_and_b32 m0, s47, 0xffff
zm v[44:47],v[144:147],a[152:155],v[44:47]
s_waitcnt lgkmcnt(9)
zt v[174:175],v234 offset:9224
zl v212,s[96:99],s47 offen offset:0 lds
zm v[60:63],v[152:155],a[152:155],v[60:63]
s_waitcnt lgkmcnt(8)
zm v[60:63],v[156:159],a[156:159],v[60:63]
zt v[176:177],v235 offset:0
zm v[56:59],v[156:159],a[148:151],v[56:59]
zt v[178:179],v235 offset:1024
zm v[56:59],v[152:155],a[144:147],v[56:59]
s_add_u32 s47, s47, 0x1000
zm v[52:55],v[152:155],a[136:139],v[52:55]
s_and_b32 m0, s47, 0xffff
zt v[180:181],v235 offset:8192
zm v[52:55],v[156:159],a[140:143],v[52:55]
zt v[182:183],v235 offset:9216
)"
R"(zl v212,s[96:99],s47 offen offset:0 nt lds
zm v[48:51],v[156:159],a[132:135],v[48:51]
zm v[48:51],v[152:155],a[128:131],v[48:51]
s_waitcnt lgkmcnt(10)
zt v[184:185],v235 offset:8
zm v[64:67],v[160:163],a[128:131],v[64:67]
s_waitcnt lgkmcnt(9)
s_add_u32 s47, s47, 0x1000
zm v[64:67],v[164:167],a[132:135],v[64:67]
s_and_b32 m0, s47, 0xffff
zt v[186:187],v235 offset:1032
zm v[68:71],v[164:167],a[140:143],v[68:71]
zt v[188:189],v235 offset:8200
zl v212,s[96:99],s47 offen offset:0 nt lds
zm v[68:71],v[160:163],a[136:139],v[68:71]
zm v[72:75],v[160:163],a[144:147],v[72:75]
zt v[190:191],v235 offset:9224
zm v[72:75],v[164:167],a[148:151],v[72:75]
zm v[76:79],v[164:167],a[156:159],v[76:79]
zm v[76:79],v[160:163],a[152:155],v[76:79]
s_waitcnt lgkmcnt(10)
s_add_u32 s47, s47, 0x1000
zm v[92:95],v[168:171],a[152:155],v[92:95]
s_waitcnt lgkmcnt(8)
global_load_dword v254, v255, s[52:53]
zm v[92:95],v[172:175],a[156:159],v[92:95]
zm v[88:91],v[172:175],a[148:151],v[88:91]
zm v[88:91],v[168:171],a[144:147],v[88:91]
zm v[84:87],v[168:171],a[136:139],v[84:87]
zm v[84:87],v[172:175],a[140:143],v[84:87]
zm v[80:83],v[172:175],a[132:135],v[80:83]
zm v[80:83],v[168:171],a[128:131],v[80:83]
s_waitcnt vmcnt(13) lgkmcnt(0)
zm v[96:99],v[176:179],a[128:131],v[96:99]
s_barrier
zm v[96:99],v[180:183],a[132:135],v[96:99]
zk a[160:163],v226 offset:0
zm v[100:103],v[180:183],a[140:143],v[100:103]
s_and_b32 m0, s48, 0xffff
zk a[164:167],v227 offset:0
zt v[128:129],v232 offset:16384
zm v[100:103],v[176:179],a[136:139],v[100:103]
s_bitset1_b32 m0, 16
zk a[168:171],v226 offset:2048
zt v[130:131],v232 offset:17408
zm v[104:107],v[176:179],a[144:147],v[104:107]
zk a[172:175],v227 offset:2048
zl v212,s[36:39],s48 offen offset:0 nt lds
zm v[104:107],v[180:183],a[148:151],v[104:107]
zk a[176:179],v226 offset:4096
zt v[132:133],v232 offset:24576
zm v[108:111],v[180:183],a[156:159],v[108:111]
zk a[180:183],v227 offset:4096
zt v[134:135],v232 offset:25600
zm v[108:111],v[176:179],a[152:155],v[108:111]
zk a[184:187],v226 offset:6144
zm v[124:127],v[184:187],a[152:155],v[124:127]
zk a[188:191],v227 offset:6144
zt v[136:137],v232 offset:16392
zm v[124:127],v[188:191],a[156:159],v[124:127]
zm v[120:123],v[188:191],a[148:151],v[120:123]
zt v[138:139],v232 offset:17416
zm v[120:123],v[184:187],a[144:147],v[120:123]
zt v[140:141],v232 offset:24584
zm v[116:119],v[184:187],a[136:139],v[116:119]
zm v[116:119],v[188:191],a[140:143],v[116:119]
zt v[142:143],v232 offset:25608
zm v[112:115],v[188:191],a[132:135],v[112:115]
s_add_u32 s48, s48, 0x1000
zm v[112:115],v[184:187],a[128:131],v[112:115]
s_waitcnt lgkmcnt(11)
s_and_b32 m0, s48, 0xffff
zt v[144:145],v233 offset:16384
zm v[0:3],v[128:131],a[160:163],v[0:3]
s_waitcnt lgkmcnt(7)
s_bitset1_b32 m0, 16
zt v[146:147],v233 offset:17408
zm v[0:3],v[132:135],a[164:167],v[0:3]
zl v212,s[36:39],s48 offen offset:0 nt lds
zm v[4:7],v[132:135],a[172:175],v[4:7]
zt v[148:149],v233 offset:24576
zm v[4:7],v[128:131],a[168:171],v[4:7]
zt v[150:151],v233 offset:25600
zm v[8:11],v[128:131],a[176:179],v[8:11]
zm v[8:11],v[132:135],a[180:183],v[8:11]
s_waitcnt lgkmcnt(8)
zt v[152:153],v233 offset:16392
zm v[12:15],v[132:135],a[188:191],v[12:15]
zm v[12:15],v[128:131],a[184:187],v[12:15]
s_waitcnt lgkmcnt(7)
zt v[154:155],v233 offset:17416
zm v[28:31],v[136:139],a[184:187],v[28:31]
s_waitcnt lgkmcnt(6)
zt v[156:157],v233 offset:24584
zm v[28:31],v[140:143],a[188:191],v[28:31]
zm v[24:27],v[140:143],a[180:183],v[24:27]
zt v[158:159],v233 offset:25608
zm v[24:27],v[136:139],a[176:179],v[24:27]
zm v[20:23],v[136:139],a[168:171],v[20:23]
zt v[160:161],v234 offset:16384
zm v[20:23],v[140:143],a[172:175],v[20:23]
s_add_u32 s48, s48, 0x1000
zt v[162:163],v234 offset:17408
zm v[16:19],v[140:143],a[164:167],v[16:19]
s_and_b32 m0, s48, 0xffff
zm v[16:19],v[136:139],a[160:163],v[16:19]
s_waitcnt lgkmcnt(8)
s_bitset1_b32 m0, 16
zt v[164:165],v234 offset:24576
zm v[32:35],v[144:147],a[160:163],v[32:35]
s_waitcnt lgkmcnt(7)
zt v[166:167],v234 offset:25600
zl v212,s[36:39],s48 offen offset:0 nt lds
zm v[32:35],v[148:151],a[164:167],v[32:35]
zm v[36:39],v[148:151],a[172:175],v[36:39]
zt v[168:169],v234 offset:16392
zm v[36:39],v[144:147],a[168:171],v[36:39]
zm v[40:43],v[144:147],a[176:179],v[40:43]
zt v[170:171],v234 offset:17416
zm v[40:43],v[148:151],a[180:183],v[40:43]
zt v[172:173],v234 offset:24584
zm v[44:47],v[148:151],a[188:191],v[44:47]
v_mul_f32 v0, s58, v0
v_mul_f32 v1, s58, v1
zm v[44:47],v[144:147],a[184:187],v[44:47]
s_waitcnt lgkmcnt(9)
zt v[174:175],v234 offset:25608
v_mul_f32 v2, s58, v2
zm v[60:63],v[152:155],a[184:187],v[60:63]
s_waitcnt lgkmcnt(8)
v_mul_f32 v3, s58, v3
v_mul_f32 v16, s58, v16
zm v[60:63],v[156:159],a[188:191],v[60:63]
zt v[176:177],v235 offset:16384
v_mul_f32 v17, s58, v17
zm v[56:59],v[156:159],a[180:183],v[56:59]
zt v[178:179],v235 offset:17408
v_mul_f32 v18, s58, v18
zm v[56:59],v[152:155],a[176:179],v[56:59]
v_mul_f32 v19, s58, v19
zc v0,v0,v1
zm v[52:55],v[152:155],a[168:171],v[52:55]
s_add_u32 s48, s48, 0x1000
zt v[180:181],v235 offset:24576
zc v2,v2,v3
zm v[52:55],v[156:159],a[172:175],v[52:55]
s_and_b32 m0, s48, 0xffff
zt v[182:183],v235 offset:25600
zc v16,v16,v17
zm v[48:51],v[156:159],a[164:167],v[48:51]
s_bitset1_b32 m0, 16
zc v18,v18,v19
zw a192,v0
zm v[48:51],v[152:155],a[160:163],v[48:51]
s_waitcnt lgkmcnt(10)
zt v[184:185],v235 offset:16392
zl v212,s[36:39],s48 offen offset:0 nt lds
zm v[64:67],v[160:163],a[160:163],v[64:67]
s_waitcnt lgkmcnt(9)
s_add_u32 s48, s48, 0x1000
zw a193,v2
zw a194,v16
zm v[64:67],v[164:167],a[164:167],v[64:67]
zt v[186:187],v235 offset:17416
zw a195,v18
zm v[68:71],v[164:167],a[172:175],v[68:71]
zt v[188:189],v235 offset:24584
v_mul_f32 v4, s58, v4
zm v[68:71],v[160:163],a[168:171],v[68:71]
v_mul_f32 v5, s58, v5
v_mul_f32 v6, s58, v6
zm v[72:75],v[160:163],a[176:179],v[72:75]
zt v[190:191],v235 offset:25608
v_mul_f32 v7, s58, v7
zm v[72:75],v[164:167],a[180:183],v[72:75]
v_mul_f32 v20, s58, v20
v_mul_f32 v21, s58, v21
zm v[76:79],v[164:167],a[188:191],v[76:79]
v_mul_f32 v22, s58, v22
zm v[76:79],v[160:163],a[184:187],v[76:79]
s_waitcnt lgkmcnt(10)
v_mul_f32 v23, s58, v23
zc v4,v4,v5
zm v[92:95],v[168:171],a[184:187],v[92:95]
s_waitcnt lgkmcnt(8)
zc v6,v6,v7
zm v[92:95],v[172:175],a[188:191],v[92:95]
zc v20,v20,v21
zc v22,v22,v23
zm v[88:91],v[172:175],a[180:183],v[88:91]
zw a208,v4
zm v[88:91],v[168:171],a[176:179],v[88:91]
zw a209,v6
zw a210,v20
zm v[84:87],v[168:171],a[168:171],v[84:87]
zw a211,v22
zm v[84:87],v[172:175],a[172:175],v[84:87]
v_mul_f32 v8, s58, v8
v_mul_f32 v9, s58, v9
zm v[80:83],v[172:175],a[164:167],v[80:83]
v_mul_f32 v10, s58, v10
zm v[80:83],v[168:171],a[160:163],v[80:83]
s_waitcnt vmcnt(13) lgkmcnt(0)
v_mul_f32 v11, s58, v11
v_mul_f32 v24, s58, v24
zm v[96:99],v[176:179],a[160:163],v[96:99]
s_barrier
v_mul_f32 v25, s58, v25
zm v[96:99],v[180:183],a[164:167],v[96:99]
v_mul_f32 v26, s58, v26
v_mul_f32 v27, s58, v27
zm v[100:103],v[180:183],a[172:175],v[100:103]
s_and_b32 m0, s48, 0xffff
zt v[128:129],v232 offset:32768
zc v8,v8,v9
zm v[100:103],v[176:179],a[168:171],v[100:103]
s_bitset1_b32 m0, 16
zt v[130:131],v232 offset:33792
zc v10,v10,v11
zm v[104:107],v[176:179],a[176:179],v[104:107]
zl v212,s[36:39],s48 offen offset:0 nt lds
zc v24,v24,v25
zm v[104:107],v[180:183],a[180:183],v[104:107]
zt v[132:133],v232 offset:40960
zc v26,v26,v27
zm v[108:111],v[180:183],a[188:191],v[108:111]
s_add_u32 s48, s48, 0x1000
zt v[134:135],v232 offset:41984
zw a224,v8
zm v[108:111],v[176:179],a[184:187],v[108:111]
s_and_b32 m0, s48, 0xffff
zw a225,v10
zw a226,v24
zm v[124:127],v[184:187],a[184:187],v[124:127]
s_bitset1_b32 m0, 16
zt v[136:137],v232 offset:32776
zw a227,v26
zm v[124:127],v[188:191],a[188:191],v[124:127]
zl v212,s[36:39],s48 offen offset:0 nt lds
v_mul_f32 v12, s58, v12
zm v[120:123],v[188:191],a[180:183],v[120:123]
zt v[138:139],v232 offset:33800
v_mul_f32 v13, s58, v13
zm v[120:123],v[184:187],a[176:179],v[120:123]
zt v[140:141],v232 offset:40968
v_mul_f32 v14, s58, v14
zm v[116:119],v[184:187],a[168:171],v[116:119]
v_mul_f32 v15, s58, v15
v_mul_f32 v28, s58, v28
zm v[116:119],v[188:191],a[172:175],v[116:119]
zt v[142:143],v232 offset:41992
v_mul_f32 v29, s58, v29
zm v[112:115],v[188:191],a[164:167],v[112:115]
s_add_u32 s48, s48, 0x1000
v_mul_f32 v30, s58, v30
v_mul_f32 v31, s58, v31
zm v[112:115],v[184:187],a[160:163],v[112:115]
s_waitcnt lgkmcnt(6)
s_and_b32 m0, s48, 0xffff
zt v[144:145],v233 offset:32768
zc v12,v12,v13
zm a[0:3],v[128:131],a[128:131],a[0:3]
s_waitcnt lgkmcnt(5)
s_bitset1_b32 m0, 16
zt v[146:147],v233 offset:33792
zc v14,v14,v15
zm a[0:3],v[132:135],a[132:135],a[0:3]
zl v212,s[36:39],s48 offen offset:0 nt lds
zc v28,v28,v29
zm a[4:7],v[132:135],a[140:143],a[4:7]
zt v[148:149],v233 offset:40960
zc v30,v30,v31
zm a[4:7],v[128:131],a[136:139],a[4:7]
zt v[150:151],v233 offset:41984
zw a240,v12
zm a[8:11],v[128:131],a[144:147],a[8:11]
zw a241,v14
zw a242,v28
zm a[8:11],v[132:135],a[148:151],a[8:11]
s_add_u32 s48, s48, 0x1000
zt v[152:153],v233 offset:32776
zw a243,v30
zm a[12:15],v[132:135],a[156:159],a[12:15]
s_and_b32 m0, s48, 0xffff
v_mul_f32 v32, s58, v32
v_mul_f32 v33, s58, v33
zm a[12:15],v[128:131],a[152:155],a[12:15]
s_waitcnt lgkmcnt(7)
s_bitset1_b32 m0, 16
zt v[154:155],v233 offset:33800
v_mul_f32 v34, s58, v34
zm a[28:31],v[136:139],a[152:155],a[28:31]
s_waitcnt lgkmcnt(6)
zt v[156:157],v233 offset:40968
zl v212,s[36:39],s48 offen offset:0 nt lds
zm a[28:31],v[140:143],a[156:159],a[28:31]
v_mul_f32 v35, s58, v35
v_mul_f32 v48, s58, v48
zm a[24:27],v[140:143],a[148:151],a[24:27]
zt v[158:159],v233 offset:41992
v_mul_f32 v49, s58, v49
zm a[24:27],v[136:139],a[144:147],a[24:27]
v_mul_f32 v50, s58, v50
v_mul_f32 v51, s58, v51
zm a[20:23],v[136:139],a[136:139],a[20:23]
zt v[160:161],v234 offset:32768
zc v32,v32,v33
zm a[20:23],v[140:143],a[140:143],a[20:23]
zt v[162:163],v234 offset:33792
zc v34,v34,v35
zm a[16:19],v[140:143],a[132:135],a[16:19]
zc v48,v48,v49
zc v50,v50,v51
zm a[16:19],v[136:139],a[128:131],a[16:19]
s_waitcnt lgkmcnt(8)
s_add_u32 s48, s48, 0x1000
zt v[164:165],v234 offset:40960
zw a196,v32
zm a[32:35],v[144:147],a[128:131],a[32:35]
s_waitcnt lgkmcnt(7)
zt v[166:167],v234 offset:41984
buffer_load_dword v254, v214, s[40:43], s70 offen offset:0
zm a[32:35],v[148:151],a[132:135],a[32:35]
zw a197,v34
zw a198,v48
zm a[36:39],v[148:151],a[140:143],a[36:39]
zt v[168:169],v234 offset:32776
zw a199,v50
zm a[36:39],v[144:147],a[136:139],a[36:39]
v_mul_f32 v36, s58, v36
v_mul_f32 v37, s58, v37
zm a[40:43],v[144:147],a[144:147],a[40:43]
zt v[170:171],v234 offset:33800
v_mul_f32 v38, s58, v38
zm a[40:43],v[148:151],a[148:151],a[40:43]
zt v[172:173],v234 offset:40968
v_mul_f32 v39, s58, v39
zm a[44:47],v[148:151],a[156:159],a[44:47]
v_mul_f32 v52, s58, v52
v_mul_f32 v53, s58, v53
zm a[44:47],v[144:147],a[152:155],a[44:47]
s_waitcnt lgkmcnt(9)
zt v[174:175],v234 offset:41992
buffer_load_dword v254, v214, s[40:43], s70 offen offset:1024
zm a[60:63],v[152:155],a[152:155],a[60:63]
s_waitcnt lgkmcnt(8)
v_mul_f32 v54, s58, v54
v_mul_f32 v55, s58, v55
zm a[60:63],v[156:159],a[156:159],a[60:63]
zt v[176:177],v235 offset:32768
zc v36,v36,v37
zm a[56:59],v[156:159],a[148:151],a[56:59]
zt v[178:179],v235 offset:33792
zc v38,v38,v39
zm a[56:59],v[152:155],a[144:147],a[56:59]
zc v52,v52,v53
zc v54,v54,v55
zm a[52:55],v[152:155],a[136:139],a[52:55]
zt v[180:181],v235 offset:40960
zw a212,v36
zm a[52:55],v[156:159],a[140:143],a[52:55]
zt v[182:183],v235 offset:41984
zw a213,v38
zm a[48:51],v[156:159],a[132:135],a[48:51]
zw a214,v52
zw a215,v54
zm a[48:51],v[152:155],a[128:131],a[48:51]
s_waitcnt lgkmcnt(10)
zt v[184:185],v235 offset:32776
buffer_load_dword v254, v214, s[40:43], s70 offen offset:2048
zm a[64:67],v[160:163],a[128:131],a[64:67]
s_waitcnt lgkmcnt(9)
v_mul_f32 v40, s58, v40
v_mul_f32 v41, s58, v41
zm a[64:67],v[164:167],a[132:135],a[64:67]
zt v[186:187],v235 offset:33800
v_mul_f32 v42, s58, v42
zm a[68:71],v[164:167],a[140:143],a[68:71]
zt v[188:189],v235 offset:40968
v_mul_f32 v43, s58, v43
zm a[68:71],v[160:163],a[136:139],a[68:71]
v_mul_f32 v56, s58, v56
v_mul_f32 v57, s58, v57
zm a[72:75],v[160:163],a[144:147],a[72:75]
zt v[190:191],v235 offset:41992
v_mul_f32 v58, s58, v58
zm a[72:75],v[164:167],a[148:151],a[72:75]
v_mul_f32 v59, s58, v59
zc v40,v40,v41
zm a[76:79],v[164:167],a[156:159],a[76:79]
buffer_load_dword v254, v214, s[40:43], s70 offen offset:3072
zc v42,v42,v43
zm a[76:79],v[160:163],a[152:155],a[76:79]
s_waitcnt lgkmcnt(10)
zc v56,v56,v57
zc v58,v58,v59
zm a[92:95],v[168:171],a[152:155],a[92:95]
s_waitcnt lgkmcnt(8)
zw a228,v40
zm a[92:95],v[172:175],a[156:159],a[92:95]
zw a229,v42
zw a230,v56
zm a[88:91],v[172:175],a[148:151],a[88:91]
zw a231,v58
zm a[88:91],v[168:171],a[144:147],a[88:91]
v_mul_f32 v44, s58, v44
v_mul_f32 v45, s58, v45
zm a[84:87],v[168:171],a[136:139],a[84:87]
v_mul_f32 v46, s58, v46
zm a[84:87],v[172:175],a[140:143],a[84:87]
v_mul_f32 v47, s58, v47
v_mul_f32 v60, s58, v60
zm a[80:83],v[172:175],a[132:135],a[80:83]
v_mul_f32 v61, s58, v61
zm a[80:83],v[168:171],a[128:131],a[80:83]
s_waitcnt vmcnt(17) lgkmcnt(0)
v_mul_f32 v62, s58, v62
v_mul_f32 v63, s58, v63
zm a[96:99],v[176:179],a[128:131],a[96:99]
s_barrier
zc v44,v44,v45
zm a[96:99],v[180:183],a[132:135],a[96:99]
zc v46,v46,v47
zc v60,v60,v61
zm a[100:103],v[180:183],a[140:143],a[100:103]
s_and_b32 m0, s48, 0xffff
zt v[128:129],v232 offset:49152
zc v62,v62,v63
zm a[100:103],v[176:179],a[136:139],a[100:103]
s_bitset1_b32 m0, 16
zt v[130:131],v232 offset:50176
zw a244,v44
zm a[104:107],v[176:179],a[144:147],a[104:107]
zl v212,s[36:39],s48 offen offset:0 nt lds
zw a245,v46
zm a[104:107],v[180:183],a[148:151],a[104:107]
zt v[132:133],v232 offset:57344
zw a246,v60
zm a[108:111],v[180:183],a[156:159],a[108:111]
zt v[134:135],v232 offset:58368
zw a247,v62
zm a[108:111],v[176:179],a[152:155],a[108:111]
v_mul_f32 v64, s58, v64
v_mul_f32 v65, s58, v65
zm a[124:127],v[184:187],a[152:155],a[124:127]
zt v[136:137],v232 offset:49160
v_mul_f32 v66, s58, v66
zm a[124:127],v[188:191],a[156:159],a[124:127]
v_mul_f32 v67, s58, v67
v_mul_f32 v80, s58, v80
zm a[120:123],v[188:191],a[148:151],a[120:123]
zt v[138:139],v232 offset:50184
v_mul_f32 v81, s58, v81
zm a[120:123],v[184:187],a[144:147],a[120:123]
zt v[140:141],v232 offset:57352
v_mul_f32 v82, s58, v82
zm a[116:119],v[184:187],a[136:139],a[116:119]
v_mul_f32 v83, s58, v83
zc v64,v64,v65
zm a[116:119],v[188:191],a[140:143],a[116:119]
zt v[142:143],v232 offset:58376
zc v66,v66,v67
zm a[112:115],v[188:191],a[132:135],a[112:115]
s_add_u32 s48, s48, 0x1000
zc v80,v80,v81
zc v82,v82,v83
zm a[112:115],v[184:187],a[128:131],a[112:115]
s_waitcnt lgkmcnt(6)
s_and_b32 m0, s48, 0xffff
zt v[144:145],v233 offset:49152
zw a200,v64
zm a[0:3],v[128:131],a[160:163],a[0:3]
s_waitcnt lgkmcnt(5)
s_bitset1_b32 m0, 16
zt v[146:147],v233 offset:50176
zw a201,v66
zm a[0:3],v[132:135],a[164:167],a[0:3]
zl v212,s[36:39],s48 offen offset:0 nt lds
zw a202,v80
zm a[4:7],v[132:135],a[172:175],a[4:7]
zt v[148:149],v233 offset:57344
zw a203,v82
zm a[4:7],v[128:131],a[168:171],a[4:7]
zt v[150:151],v233 offset:58368
v_mul_f32 v68, s58, v68
zm a[8:11],v[128:131],a[176:179],a[8:11]
v_mul_f32 v69, s58, v69
v_mul_f32 v70, s58, v70
zm a[8:11],v[132:135],a[180:183],a[8:11]
zt v[152:153],v233 offset:49160
v_mul_f32 v71, s58, v71
zm a[12:15],v[132:135],a[188:191],a[12:15]
v_mul_f32 v84, s58, v84
v_mul_f32 v85, s58, v85
zm a[12:15],v[128:131],a[184:187],a[12:15]
s_waitcnt lgkmcnt(7)
zt v[154:155],v233 offset:50184
v_mul_f32 v86, s58, v86
zm a[28:31],v[136:139],a[184:187],a[28:31]
s_waitcnt lgkmcnt(6)
zt v[156:157],v233 offset:57352
v_mul_f32 v87, s58, v87
zm a[28:31],v[140:143],a[188:191],a[28:31]
zc v68,v68,v69
zc v70,v70,v71
zm a[24:27],v[140:143],a[180:183],a[24:27]
zt v[158:159],v233 offset:58376
zc v84,v84,v85
zm a[24:27],v[136:139],a[176:179],a[24:27]
zc v86,v86,v87
zw a216,v68
zm a[20:23],v[136:139],a[168:171],a[20:23]
zt v[160:161],v234 offset:49152
zw a217,v70
zm a[20:23],v[140:143],a[172:175],a[20:23]
s_add_u32 s48, s48, 0x1000
zt v[162:163],v234 offset:50176
zw a218,v84
zm a[16:19],v[140:143],a[164:167],a[16:19]
s_and_b32 m0, s48, 0xffff
zw a219,v86
v_mul_f32 v72, s58, v72
zm a[16:19],v[136:139],a[160:163],a[16:19]
s_waitcnt lgkmcnt(8)
s_bitset1_b32 m0, 16
zt v[164:165],v234 offset:57344
v_mul_f32 v73, s58, v73
zm a[32:35],v[144:147],a[160:163],a[32:35]
s_waitcnt lgkmcnt(7)
zt v[166:167],v234 offset:58368
zl v212,s[36:39],s48 offen offset:0 nt lds
zm a[32:35],v[148:151],a[164:167],a[32:35]
v_mul_f32 v74, s58, v74
v_mul_f32 v75, s58, v75
zm a[36:39],v[148:151],a[172:175],a[36:39]
zt v[168:169],v234 offset:49160
v_mul_f32 v88, s58, v88
zm a[36:39],v[144:147],a[168:171],a[36:39]
v_mul_f32 v89, s58, v89
v_mul_f32 v90, s58, v90
zm a[40:43],v[144:147],a[176:179],a[40:43]
zt v[170:171],v234 offset:50184
v_mul_f32 v91, s58, v91
zm a[40:43],v[148:151],a[180:183],a[40:43]
zt v[172:173],v234 offset:57352
zc v72,v72,v73
zm a[44:47],v[148:151],a[188:191],a[44:47]
zc v74,v74,v75
zc v88,v88,v89
zm a[44:47],v[144:147],a[184:187],a[44:47]
s_waitcnt lgkmcnt(9)
zt v[174:175],v234 offset:58376
zc v90,v90,v91
zm a[60:63],v[152:155],a[184:187],a[60:63]
s_waitcnt lgkmcnt(8)
zw a232,v72
zw a233,v74
zm a[60:63],v[156:159],a[188:191],a[60:63]
zt v[176:177],v235 offset:49152
zw a234,v88
zm a[56:59],v[156:159],a[180:183],a[56:59]
zt v[178:179],v235 offset:50176
zw a235,v90
zm a[56:59],v[152:155],a[176:179],a[56:59]
v_mul_f32 v76, s58, v76
v_mul_f32 v77, s58, v77
zm a[52:55],v[152:155],a[168:171],a[52:55]
s_add_u32 s48, s48, 0x1000
zt v[180:181],v235 offset:57344
v_mul_f32 v78, s58, v78
zm a[52:55],v[156:159],a[172:175],a[52:55]
s_and_b32 m0, s48, 0xffff
zt v[182:183],v235 offset:58368
v_mul_f32 v79, s58, v79
zm a[48:51],v[156:159],a[164:167],a[48:51]
s_bitset1_b32 m0, 16
v_mul_f32 v92, s58, v92
v_mul_f32 v93, s58, v93
zm a[48:51],v[152:155],a[160:163],a[48:51]
s_waitcnt lgkmcnt(10)
zt v[184:185],v235 offset:49160
zl v212,s[36:39],s48 offen offset:0 nt lds
zm a[64:67],v[160:163],a[160:163],a[64:67]
s_waitcnt lgkmcnt(9)
s_add_u32 s48, s48, 0x1000
v_mul_f32 v94, s58, v94
v_mul_f32 v95, s58, v95
zm a[64:67],v[164:167],a[164:167],a[64:67]
zt v[186:187],v235 offset:50184
zc v76,v76,v77
zm a[68:71],v[164:167],a[172:175],a[68:71]
zt v[188:189],v235 offset:57352
zc v78,v78,v79
zm a[68:71],v[160:163],a[168:171],a[68:71]
zc v92,v92,v93
zc v94,v94,v95
zm a[72:75],v[160:163],a[176:179],a[72:75]
zt v[190:191],v235 offset:58376
zw a248,v76
zm a[72:75],v[164:167],a[180:183],a[72:75]
zw a249,v78
zw a250,v92
zm a[76:79],v[164:167],a[188:191],a[76:79]
zw a251,v94
zm a[76:79],v[160:163],a[184:187],a[76:79]
s_waitcnt lgkmcnt(10)
v_mul_f32 v96, s58, v96
v_mul_f32 v97, s58, v97
zm a[92:95],v[168:171],a[184:187],a[92:95]
s_waitcnt lgkmcnt(8)
v_mul_f32 v98, s58, v98
zm a[92:95],v[172:175],a[188:191],a[92:95]
zk a[128:131],v196 offset:0
v_mul_f32 v99, s58, v99
zm a[88:91],v[172:175],a[180:183],a[88:91]
v_mul_f32 v112, s58, v112
v_mul_f32 v113, s58, v113
zm a[88:91],v[168:171],a[176:179],a[88:91]
zk a[132:135],v197 offset:0
v_mul_f32 v114, s58, v114
zm a[84:87],v[168:171],a[168:171],a[84:87]
v_mul_f32 v115, s58, v115
zc v96,v96,v97
zm a[84:87],v[172:175],a[172:175],a[84:87]
zk a[136:139],v198 offset:0
zc v98,v98,v99
zm a[80:83],v[172:175],a[164:167],a[80:83]
zc v112,v112,v113
zc v114,v114,v115
zm a[80:83],v[168:171],a[160:163],a[80:83]
s_waitcnt lgkmcnt(9)
zk a[140:143],v199 offset:0
zw a204,v96
zm a[96:99],v[176:179],a[160:163],a[96:99]
s_waitcnt lgkmcnt(8)
zw a205,v98
zw a206,v112
zm a[96:99],v[180:183],a[164:167],a[96:99]
zk a[144:147],v196 offset:4096
zw a207,v114
zm a[100:103],v[180:183],a[172:175],a[100:103]
v_mul_f32 v100, s58, v100
v_mul_f32 v101, s58, v101
zm a[100:103],v[176:179],a[168:171],a[100:103]
zk a[148:151],v197 offset:4096
v_mul_f32 v102, s58, v102
zm a[104:107],v[176:179],a[176:179],a[104:107]
v_mul_f32 v103, s58, v103
v_mul_f32 v116, s58, v116
zm a[104:107],v[180:183],a[180:183],a[104:107]
zk a[152:155],v198 offset:4096
v_mul_f32 v117, s58, v117
zm a[108:111],v[180:183],a[188:191],a[108:111]
v_mul_f32 v118, s58, v118
v_mul_f32 v119, s58, v119
zm a[108:111],v[176:179],a[184:187],a[108:111]
s_waitcnt lgkmcnt(9)
zk a[156:159],v199 offset:4096
zc v100,v100,v101
zm a[124:127],v[184:187],a[184:187],a[124:127]
s_waitcnt lgkmcnt(8)
zc v102,v102,v103
zc v116,v116,v117
zm a[124:127],v[188:191],a[188:191],a[124:127]
zc v118,v118,v119
zm a[120:123],v[188:191],a[180:183],a[120:123]
zw a220,v100
zw a221,v102
zm a[120:123],v[184:187],a[176:179],a[120:123]
zw a222,v116
zm a[116:119],v[184:187],a[168:171],a[116:119]
zw a223,v118
v_mul_f32 v104, s58, v104
zm a[116:119],v[188:191],a[172:175],a[116:119]
v_mul_f32 v105, s58, v105
zm a[112:115],v[188:191],a[164:167],a[112:115]
v_mul_f32 v106, s58, v106
v_mul_f32 v107, s58, v107
zm a[112:115],v[184:187],a[160:163],a[112:115]
s_waitcnt lgkmcnt(7)
v_mul_f32 v120, s58, v120
v_mul_f32 v121, s58, v121
v_mul_f32 v122, s58, v122
v_mul_f32 v123, s58, v123
zc v104,v104,v105
zc v106,v106,v107
zc v120,v120,v121
zc v122,v122,v123
zw a236,v104
zw a237,v106
zw a238,v120
zw a239,v122
.p2align 3
zp v[108:109],v[108:109],s[58:59]
zp v[110:111],v[110:111],s[58:59]
zp v[124:125],v[124:125],s[58:59]
zp v[126:127],v[126:127],s[58:59]
zc v108,v108,v109
zc v110,v110,v111
zc v124,v124,v125
zc v126,v126,v127
zw a252,v108
zw a253,v110
zw a254,v124
zw a255,v126
s_mov_b32 s40, s20
s_and_b32 s41, s21, 0xffff
s_mov_b32 s42, 0x20000
s_mov_b32 s43, 0x20000
s_mov_b32 s24, s14
s_and_b32 s25, s15, 0xffff
s_mov_b32 s26, 0xffd00
s_mov_b32 s27, 0x20000
s_mov_b32 s28, s18
s_and_b32 s29, s19, 0xffff
s_mov_b32 s30, 0x77840
s_mov_b32 s31, 0x20000
s_nop 1
zm v[0:3],a[128:131],a[192:195],0
s_waitcnt lgkmcnt(6)
v_add_u32 v216, 1, v216
zr v236,a0
zm v[0:3],a[132:135],a[196:199],v[0:3]
s_waitcnt lgkmcnt(5)
zr v237,a1
zm v[0:3],a[136:139],a[200:203],v[0:3]
s_waitcnt lgkmcnt(4)
zr v238,a2
zr v239,a3
zm v[0:3],a[140:143],a[204:207],v[0:3]
s_waitcnt lgkmcnt(0)
zr v240,a16
zm v[4:7],a[156:159],a[204:207],0
zr v241,a17
zr v242,a18
zm v[4:7],a[152:155],a[200:203],v[4:7]
zr v243,a19
zm v[4:7],a[148:151],a[196:199],v[4:7]
v_mul_f32 v236, s58, v236
v_mul_f32 v237, s58, v237
zm v[4:7],a[144:147],a[192:195],v[4:7]
v_mul_f32 v238, s58, v238
zm v[12:15],a[144:147],a[208:211],0
v_mul_f32 v239, s58, v239
v_mul_f32 v240, s58, v240
zm v[12:15],a[148:151],a[212:215],v[12:15]
v_mul_f32 v241, s58, v241
zm v[12:15],a[152:155],a[216:219],v[12:15]
v_mul_f32 v242, s58, v242
v_mul_f32 v243, s58, v243
zm v[12:15],a[156:159],a[220:223],v[12:15]
zc v128,v236,v237
zm v[8:11],a[140:143],a[220:223],0
zc v129,v238,v239
zc v130,v240,v241
zm v[8:11],a[136:139],a[216:219],v[8:11]
zc v131,v242,v243
zm v[8:11],a[132:135],a[212:215],v[8:11]
zr v244,a4
zr v245,a5
zm v[8:11],a[128:131],a[208:211],v[8:11]
zr v246,a6
zm v[16:19],a[128:131],a[224:227],0
zr v247,a7
zr v248,a20
zm v[16:19],a[132:135],a[228:231],v[16:19]
zr v249,a21
zm v[16:19],a[136:139],a[232:235],v[16:19]
zr v250,a22
zr v251,a23
zm v[16:19],a[140:143],a[236:239],v[16:19]
v_mul_f32 v244, s58, v244
zm v[20:23],a[156:159],a[236:239],0
v_mul_f32 v245, s58, v245
v_mul_f32 v246, s58, v246
zm v[20:23],a[152:155],a[232:235],v[20:23]
v_mul_f32 v247, s58, v247
zm v[20:23],a[148:151],a[228:231],v[20:23]
v_mul_f32 v248, s58, v248
v_mul_f32 v249, s58, v249
zm v[20:23],a[144:147],a[224:227],v[20:23]
v_mul_f32 v250, s58, v250
zm v[28:31],a[144:147],a[240:243],0
v_mul_f32 v251, s58, v251
zc v132,v244,v245
zm v[28:31],a[148:151],a[244:247],v[28:31]
zc v133,v246,v247
zm v[28:31],a[152:155],a[248:251],v[28:31]
zc v134,v248,v249
zc v135,v250,v251
zm v[28:31],a[156:159],a[252:255],v[28:31]
zm v[24:27],a[140:143],a[252:255],0
zm v[24:27],a[136:139],a[248:251],v[24:27]
zm v[24:27],a[132:135],a[244:247],v[24:27]
zm v[24:27],a[128:131],a[240:243],v[24:27]
zk a[128:131],v196 offset:8192
zk a[132:135],v197 offset:8192
zk a[136:139],v198 offset:8192
zk a[140:143],v199 offset:8192
zk a[144:147],v196 offset:12288
zk a[148:151],v197 offset:12288
zk a[152:155],v198 offset:12288
zk a[156:159],v199 offset:12288
.p2align 3
zr v236,a8
zr v237,a9
zr v238,a10
zr v239,a11
zr v240,a24
zr v241,a25
zr v242,a26
zr v243,a27
zp v[236:237],v[236:237],s[58:59]
zp v[238:239],v[238:239],s[58:59]
zp v[240:241],v[240:241],s[58:59]
zp v[242:243],v[242:243],s[58:59]
zc v136,v236,v237
zc v137,v238,v239
zc v138,v240,v241
zc v139,v242,v243
zr v244,a12
zr v245,a13
zr v246,a14
zr v247,a15
zr v248,a28
zr v249,a29
zr v250,a30
zr v251,a31
zp v[244:245],v[244:245],s[58:59]
zp v[246:247],v[246:247],s[58:59]
zp v[248:249],v[248:249],s[58:59]
zp v[250:251],v[250:251],s[58:59]
zc v140,v244,v245
zc v141,v246,v247
zc v142,v248,v249
zc v143,v250,v251
zr v236,a32
zr v237,a33
zr v238,a34
zr v239,a35
zr v240,a48
zr v241,a49
zr v242,a50
zr v243,a51
zp v[236:237],v[236:237],s[58:59]
zp v[238:239],v[238:239],s[58:59]
zp v[240:241],v[240:241],s[58:59]
zp v[242:243],v[242:243],s[58:59]
zc v144,v236,v237
zc v145,v238,v239
zc v146,v240,v241
zc v147,v242,v243
zr v244,a36
zr v245,a37
zr v246,a38
zr v247,a39
zr v248,a52
zr v249,a53
zr v250,a54
zr v251,a55
zp v[244:245],v[244:245],s[58:59]
zp v[246:247],v[246:247],s[58:59]
zp v[248:249],v[248:249],s[58:59]
zp v[250:251],v[250:251],s[58:59]
zc v148,v244,v245
zc v149,v246,v247
zc v150,v248,v249
zc v151,v250,v251
zr v236,a40
zr v237,a41
zr v238,a42
zr v239,a43
zr v240,a56
zr v241,a57
zr v242,a58
zr v243,a59
zp v[236:237],v[236:237],s[58:59]
zp v[238:239],v[238:239],s[58:59]
zp v[240:241],v[240:241],s[58:59]
zp v[242:243],v[242:243],s[58:59]
zc v152,v236,v237
zc v153,v238,v239
zc v154,v240,v241
zc v155,v242,v243
zr v244,a44
zr v245,a45
zr v246,a46
zr v247,a47
zr v248,a60
zr v249,a61
zr v250,a62
zr v251,a63
zp v[244:245],v[244:245],s[58:59]
zp v[246:247],v[246:247],s[58:59]
zp v[248:249],v[248:249],s[58:59]
zp v[250:251],v[250:251],s[58:59]
zc v156,v244,v245
zc v157,v246,v247
zc v158,v248,v249
zc v159,v250,v251
zr v236,a64
zr v237,a65
zr v238,a66
zr v239,a67
zr v240,a80
zr v241,a81
zr v242,a82
zr v243,a83
zp v[236:237],v[236:237],s[58:59]
zp v[238:239],v[238:239],s[58:59]
zp v[240:241],v[240:241],s[58:59]
zp v[242:243],v[242:243],s[58:59]
zc v160,v236,v237
zc v161,v238,v239
zc v162,v240,v241
zc v163,v242,v243
zr v244,a68
zr v245,a69
zr v246,a70
zr v247,a71
zr v248,a84
zr v249,a85
zr v250,a86
zr v251,a87
zp v[244:245],v[244:245],s[58:59]
zp v[246:247],v[246:247],s[58:59]
zp v[248:249],v[248:249],s[58:59]
zp v[250:251],v[250:251],s[58:59]
zc v164,v244,v245
zc v165,v246,v247
zc v166,v248,v249
zc v167,v250,v251
zr v236,a72
zr v237,a73
zr v238,a74
zr v239,a75
zr v240,a88
zr v241,a89
zr v242,a90
zr v243,a91
zp v[236:237],v[236:237],s[58:59]
zp v[238:239],v[238:239],s[58:59]
zp v[240:241],v[240:241],s[58:59]
zp v[242:243],v[242:243],s[58:59]
zc v168,v236,v237
zc v169,v238,v239
zc v170,v240,v241
zc v171,v242,v243
zr v244,a76
zr v245,a77
zr v246,a78
zr v247,a79
zr v248,a92
zr v249,a93
zr v250,a94
zr v251,a95
zp v[244:245],v[244:245],s[58:59]
zp v[246:247],v[246:247],s[58:59]
zp v[248:249],v[248:249],s[58:59]
zp v[250:251],v[250:251],s[58:59]
zc v172,v244,v245
zc v173,v246,v247
zc v174,v248,v249
zc v175,v250,v251
zr v236,a96
zr v237,a97
zr v238,a98
zr v239,a99
zr v240,a112
zr v241,a113
zr v242,a114
zr v243,a115
zp v[236:237],v[236:237],s[58:59]
zp v[238:239],v[238:239],s[58:59]
zp v[240:241],v[240:241],s[58:59]
zp v[242:243],v[242:243],s[58:59]
zc v176,v236,v237
zc v177,v238,v239
zc v178,v240,v241
zc v179,v242,v243
zr v244,a100
zr v245,a101
zr v246,a102
zr v247,a103
zr v248,a116
zr v249,a117
zr v250,a118
zr v251,a119
zp v[244:245],v[244:245],s[58:59]
zp v[246:247],v[246:247],s[58:59]
zp v[248:249],v[248:249],s[58:59]
zp v[250:251],v[250:251],s[58:59]
zc v180,v244,v245
zc v181,v246,v247
zc v182,v248,v249
zc v183,v250,v251
zr v236,a104
zr v237,a105
zr v238,a106
zr v239,a107
)"
R"(zr v240,a120
zr v241,a121
zr v242,a122
zr v243,a123
zp v[236:237],v[236:237],s[58:59]
zp v[238:239],v[238:239],s[58:59]
zp v[240:241],v[240:241],s[58:59]
zp v[242:243],v[242:243],s[58:59]
zc v184,v236,v237
zc v185,v238,v239
zc v186,v240,v241
zc v187,v242,v243
zr v244,a108
zr v245,a109
zr v246,a110
zr v247,a111
zr v248,a124
zr v249,a125
zr v250,a126
zr v251,a127
zp v[244:245],v[244:245],s[58:59]
zp v[246:247],v[246:247],s[58:59]
zp v[248:249],v[248:249],s[58:59]
zp v[250:251],v[250:251],s[58:59]
zc v188,v244,v245
zc v189,v246,v247
zc v190,v248,v249
zc v191,v250,v251
v_mov_b32 v218, 0xff800000
v_mov_b32 v219, 0xff800000
v_mov_b32 v220, 0xff800000
v_mov_b32 v221, 0xff800000
v_mov_b32 v222, 0xff800000
v_mov_b32 v223, 0xff800000
v_mov_b32 v224, 0xff800000
v_mov_b32 v225, 0xff800000
s_waitcnt vmcnt(12) lgkmcnt(0)
s_barrier
s_cmp_lg_u32 s76, 0
s_cbranch_scc1 L_safe%=
L_start%=:
.p2align 3
v_max3_f32 v236, v0, v1, v218
v_max3_f32 v237, v8, v9, v219
v_max3_f32 v238, v16, v17, v220
v_max3_f32 v239, v24, v25, v221
v_max3_f32 v236, v236, v2, v3
v_max3_f32 v237, v237, v10, v11
v_max3_f32 v238, v238, v18, v19
v_max3_f32 v239, v239, v26, v27
v_max3_f32 v236, v236, v4, v5
v_max3_f32 v237, v237, v12, v13
v_max3_f32 v238, v238, v20, v21
v_max3_f32 v239, v239, v28, v29
v_max3_f32 v236, v236, v6, v7
v_max3_f32 v237, v237, v14, v15
v_max3_f32 v238, v238, v22, v23
v_max3_f32 v239, v239, v30, v31
v_mov_b32 v240, v236
v_mov_b32 v241, v237
v_mov_b32 v242, v238
v_mov_b32 v243, v239
v_mov_b32 v80, 0
v_permlane16_swap_b32 v236, v240
v_permlane16_swap_b32 v237, v241
v_permlane16_swap_b32 v238, v242
v_permlane16_swap_b32 v239, v243
v_max_f32 v236, v236, v240
v_max_f32 v237, v237, v241
v_max_f32 v238, v238, v242
v_max_f32 v239, v239, v243
v_mov_b32 v240, v236
v_mov_b32 v241, v237
v_mov_b32 v242, v238
v_mov_b32 v243, v239
v_mov_b32 v81, 0
v_permlane32_swap_b32 v236, v240
v_permlane32_swap_b32 v237, v241
v_permlane32_swap_b32 v238, v242
v_permlane32_swap_b32 v239, v243
v_max_f32 v236, v236, v240
v_max_f32 v237, v237, v241
v_max_f32 v238, v238, v242
v_max_f32 v239, v239, v243
.p2align 3
zp v[64:65],v[236:237],-1.0 op_sel:[0,0] op_sel_hi:[0,0]
zp v[66:67],v[236:237],-1.0 op_sel:[0,0] op_sel_hi:[0,0]
zp v[68:69],v[236:237],-1.0 op_sel:[1,0] op_sel_hi:[1,0]
zp v[70:71],v[236:237],-1.0 op_sel:[1,0] op_sel_hi:[1,0]
zp v[72:73],v[238:239],-1.0 op_sel:[0,0] op_sel_hi:[0,0]
zp v[74:75],v[238:239],-1.0 op_sel:[0,0] op_sel_hi:[0,0]
zp v[76:77],v[238:239],-1.0 op_sel:[1,0] op_sel_hi:[1,0]
zp v[78:79],v[238:239],-1.0 op_sel:[1,0] op_sel_hi:[1,0]
v_pk_add_f32 v[0:1], v[0:1], v[64:65]
v_pk_add_f32 v[2:3], v[2:3], v[64:65]
v_pk_add_f32 v[4:5], v[4:5], v[64:65]
v_pk_add_f32 v[6:7], v[6:7], v[64:65]
v_pk_add_f32 v[8:9], v[8:9], v[68:69]
v_pk_add_f32 v[10:11], v[10:11], v[68:69]
v_pk_add_f32 v[12:13], v[12:13], v[68:69]
v_pk_add_f32 v[14:15], v[14:15], v[68:69]
v_pk_add_f32 v[16:17], v[16:17], v[72:73]
v_pk_add_f32 v[18:19], v[18:19], v[72:73]
v_pk_add_f32 v[20:21], v[20:21], v[72:73]
v_pk_add_f32 v[22:23], v[22:23], v[72:73]
v_pk_add_f32 v[24:25], v[24:25], v[76:77]
v_pk_add_f32 v[26:27], v[26:27], v[76:77]
v_pk_add_f32 v[28:29], v[28:29], v[76:77]
v_pk_add_f32 v[30:31], v[30:31], v[76:77]
ze v0,v0
ze v1,v1
ze v2,v2
ze v3,v3
ze v4,v4
ze v5,v5
ze v6,v6
ze v7,v7
ze v8,v8
ze v9,v9
ze v10,v10
ze v11,v11
ze v12,v12
ze v13,v13
ze v14,v14
ze v15,v15
v_pk_mov_b32 v[82:83], v[80:81], v[80:81]
v_pk_mov_b32 v[84:85], v[80:81], v[80:81]
v_pk_mov_b32 v[86:87], v[80:81], v[80:81]
v_pk_mov_b32 v[218:219], v[222:223], v[222:223] op_sel:[0,1]
v_pk_mov_b32 v[220:221], v[224:225], v[224:225] op_sel:[0,1]
zc v96,v0,v1
zc v97,v2,v3
zc v98,v4,v5
zc v99,v6,v7
zc v100,v8,v9
zc v101,v10,v11
zc v102,v12,v13
zc v103,v14,v15
zm v[32:35],a[128:131],a[192:195],v[64:67]
ze v16,v16
zm v[32:35],a[132:135],a[196:199],v[32:35]
zt a[160:161],v200 offset:0
zm v[32:35],a[136:139],a[200:203],v[32:35]
ze v17,v17
zm v[32:35],a[140:143],a[204:207],v[32:35]
zt a[162:163],v200 offset:4096
zc v104,v16,v17
zm v[36:39],a[156:159],a[204:207],v[64:67]
s_and_b32 m0, s47, 0xffff
ze v18,v18
zm v[36:39],a[152:155],a[200:203],v[36:39]
zt a[164:165],v201 offset:0
zl v212,s[96:99],s47 offen offset:0 lds
zm v[36:39],a[148:151],a[196:199],v[36:39]
ze v19,v19
zm v[36:39],a[144:147],a[192:195],v[36:39]
zt a[166:167],v201 offset:4096
zc v105,v18,v19
zm v[44:47],a[144:147],a[208:211],v[68:71]
ze v20,v20
zm v[44:47],a[148:151],a[212:215],v[44:47]
zt a[168:169],v202 offset:0
zm v[44:47],a[152:155],a[216:219],v[44:47]
ze v21,v21
zm v[44:47],a[156:159],a[220:223],v[44:47]
s_add_u32 s47, s47, 0x1000
zt a[170:171],v202 offset:4096
zc v106,v20,v21
zm v[40:43],a[140:143],a[220:223],v[68:71]
s_and_b32 m0, s47, 0xffff
ze v22,v22
zm v[40:43],a[136:139],a[216:219],v[40:43]
zt a[172:173],v203 offset:0
zl v212,s[96:99],s47 offen offset:0 lds
zm v[40:43],a[132:135],a[212:215],v[40:43]
ze v23,v23
zm v[40:43],a[128:131],a[208:211],v[40:43]
zt a[174:175],v203 offset:4096
zc v107,v22,v23
zm v[48:51],a[128:131],a[224:227],v[72:75]
ze v24,v24
zm v[48:51],a[132:135],a[228:231],v[48:51]
zt a[176:177],v204 offset:0
zm v[48:51],a[136:139],a[232:235],v[48:51]
ze v25,v25
zm v[48:51],a[140:143],a[236:239],v[48:51]
s_add_u32 s47, s47, 0x1000
zt a[178:179],v204 offset:4096
zc v108,v24,v25
zm v[52:55],a[156:159],a[236:239],v[72:75]
s_and_b32 m0, s47, 0xffff
ze v26,v26
zm v[52:55],a[152:155],a[232:235],v[52:55]
zt a[180:181],v205 offset:0
zl v212,s[96:99],s47 offen offset:0 nt lds
zm v[52:55],a[148:151],a[228:231],v[52:55]
ze v27,v27
zm v[52:55],a[144:147],a[224:227],v[52:55]
zt a[182:183],v205 offset:4096
zc v109,v26,v27
zm v[60:63],a[144:147],a[240:243],v[76:79]
ze v28,v28
zm v[60:63],a[148:151],a[244:247],v[60:63]
zt a[184:185],v206 offset:0
zm v[60:63],a[152:155],a[248:251],v[60:63]
s_waitcnt lgkmcnt(5)
ze v29,v29
zm v[60:63],a[156:159],a[252:255],v[60:63]
s_add_u32 s47, s47, 0x1000
zt a[186:187],v206 offset:4096
zc v110,v28,v29
zm v[56:59],a[140:143],a[252:255],v[76:79]
s_and_b32 m0, s47, 0xffff
ze v30,v30
zm v[56:59],a[136:139],a[248:251],v[56:59]
zt a[188:189],v207 offset:0
zl v212,s[96:99],s47 offen offset:0 nt lds
zm v[56:59],a[132:135],a[244:247],v[56:59]
s_add_u32 s47, s47, 0x1000
ze v31,v31
zm v[56:59],a[128:131],a[240:243],v[56:59]
zt a[190:191],v207 offset:4096
zc v111,v30,v31
zm a[0:3],a[160:163],v[96:99],0
ze v32,v32
zm a[16:19],a[164:167],v[96:99],0
zk a[128:131],v196 offset:16384
zm a[20:23],a[164:167],v[100:103],0
ze v33,v33
zm a[4:7],a[160:163],v[100:103],0
zk a[132:135],v197 offset:16384
zc v112,v32,v33
zm a[8:11],a[160:163],v[104:107],0
ze v34,v34
zm a[24:27],a[164:167],v[104:107],0
zk a[136:139],v198 offset:16384
zm a[28:31],a[164:167],v[108:111],0
ze v35,v35
zm a[12:15],a[160:163],v[108:111],0
zk a[140:143],v199 offset:16384
zc v113,v34,v35
v_smfmac_f32_16x16x64_bf16 v[80:83], v[192:195], v[96:103], v253
ze v36,v36
v_smfmac_f32_16x16x64_bf16 v[84:87], v[192:195], v[104:111], v253
zk a[144:147],v196 offset:20480
zm a[44:47],a[168:171],v[108:111],0
ze v37,v37
zm a[60:63],a[172:175],v[108:111],0
zk a[148:151],v197 offset:20480
zc v114,v36,v37
zm a[56:59],a[172:175],v[104:107],0
s_waitcnt lgkmcnt(6)
ze v38,v38
zm a[40:43],a[168:171],v[104:107],0
zk a[152:155],v198 offset:20480
zm a[36:39],a[168:171],v[100:103],0
ze v39,v39
zm a[52:55],a[172:175],v[100:103],0
s_and_b32 m0, s48, 0xffff
zk a[156:159],v199 offset:20480
zc v115,v38,v39
zm a[48:51],a[172:175],v[96:99],0
s_bitset1_b32 m0, 16
ze v40,v40
zm a[32:35],a[168:171],v[96:99],0
zl v212,s[36:39],s48 offen offset:0 nt lds
zm a[64:67],a[176:179],v[96:99],0
s_add_u32 s48, s48, 0x1000
ze v41,v41
zm a[80:83],a[180:183],v[96:99],0
s_and_b32 m0, s48, 0xffff
zc v116,v40,v41
zm a[84:87],a[180:183],v[100:103],0
s_bitset1_b32 m0, 16
ze v42,v42
zm a[68:71],a[176:179],v[100:103],0
zl v212,s[36:39],s48 offen offset:0 nt lds
zm a[72:75],a[176:179],v[104:107],0
s_add_u32 s48, s48, 0x1000
ze v43,v43
zm a[88:91],a[180:183],v[104:107],0
s_and_b32 m0, s48, 0xffff
zc v117,v42,v43
zm a[92:95],a[180:183],v[108:111],0
s_bitset1_b32 m0, 16
ze v44,v44
zm a[76:79],a[176:179],v[108:111],0
zl v212,s[36:39],s48 offen offset:0 nt lds
zm a[108:111],a[184:187],v[108:111],0
s_add_u32 s48, s48, 0x1000
ze v45,v45
zm a[124:127],a[188:191],v[108:111],0
s_and_b32 m0, s48, 0xffff
zc v118,v44,v45
zm a[120:123],a[188:191],v[104:107],0
s_bitset1_b32 m0, 16
ze v46,v46
zm a[104:107],a[184:187],v[104:107],0
zl v212,s[36:39],s48 offen offset:0 nt lds
zm a[100:103],a[184:187],v[100:103],0
s_add_u32 s48, s48, 0x1000
ze v47,v47
zm a[116:119],a[188:191],v[100:103],0
zc v119,v46,v47
zm a[112:115],a[188:191],v[96:99],0
s_waitcnt lgkmcnt(0)
zm a[96:99],a[184:187],v[96:99],0
L_A1%=:
zm v[0:3],a[128:131],a[192:195],v[64:67]
ze v48,v48
zm v[0:3],a[132:135],a[196:199],v[0:3]
zt a[160:161],v200 offset:8192
zm v[0:3],a[136:139],a[200:203],v[0:3]
ze v49,v49
zm v[0:3],a[140:143],a[204:207],v[0:3]
zt a[162:163],v200 offset:12288
zc v120,v48,v49
zm v[4:7],a[156:159],a[204:207],v[64:67]
ze v50,v50
zm v[4:7],a[152:155],a[200:203],v[4:7]
zt a[164:165],v201 offset:8192
zm v[4:7],a[148:151],a[196:199],v[4:7]
ze v51,v51
zm v[4:7],a[144:147],a[192:195],v[4:7]
zt a[166:167],v201 offset:12288
zc v121,v50,v51
zm v[12:15],a[144:147],a[208:211],v[68:71]
ze v52,v52
zm v[12:15],a[148:151],a[212:215],v[12:15]
zt a[168:169],v202 offset:8192
zm v[12:15],a[152:155],a[216:219],v[12:15]
ze v53,v53
zm v[12:15],a[156:159],a[220:223],v[12:15]
zt a[170:171],v202 offset:12288
zc v122,v52,v53
zm v[8:11],a[140:143],a[220:223],v[68:71]
ze v54,v54
zm v[8:11],a[136:139],a[216:219],v[8:11]
zt a[172:173],v203 offset:8192
zm v[8:11],a[132:135],a[212:215],v[8:11]
ze v55,v55
zm v[8:11],a[128:131],a[208:211],v[8:11]
zt a[174:175],v203 offset:12288
zc v123,v54,v55
zm v[16:19],a[128:131],a[224:227],v[72:75]
ze v56,v56
zm v[16:19],a[132:135],a[228:231],v[16:19]
zt a[176:177],v204 offset:8192
zm v[16:19],a[136:139],a[232:235],v[16:19]
ze v57,v57
zm v[16:19],a[140:143],a[236:239],v[16:19]
zt a[178:179],v204 offset:12288
zc v124,v56,v57
zm v[20:23],a[156:159],a[236:239],v[72:75]
ze v58,v58
zm v[20:23],a[152:155],a[232:235],v[20:23]
zt a[180:181],v205 offset:8192
zm v[20:23],a[148:151],a[228:231],v[20:23]
ze v59,v59
zm v[20:23],a[144:147],a[224:227],v[20:23]
zt a[182:183],v205 offset:12288
zc v125,v58,v59
zm v[28:31],a[144:147],a[240:243],v[76:79]
ze v60,v60
zm v[28:31],a[148:151],a[244:247],v[28:31]
zt a[184:185],v206 offset:8192
zm v[28:31],a[152:155],a[248:251],v[28:31]
s_waitcnt lgkmcnt(5)
ze v61,v61
zm v[28:31],a[156:159],a[252:255],v[28:31]
zt a[186:187],v206 offset:12288
zc v126,v60,v61
zm v[24:27],a[140:143],a[252:255],v[76:79]
ze v62,v62
zm v[24:27],a[136:139],a[248:251],v[24:27]
zt a[188:189],v207 offset:8192
zm v[24:27],a[132:135],a[244:247],v[24:27]
ze v63,v63
zm v[24:27],a[128:131],a[240:243],v[24:27]
zt a[190:191],v207 offset:12288
zc v127,v62,v63
zm a[0:3],a[160:163],v[112:115],a[0:3]
ze v0,v0
zm a[16:19],a[164:167],v[112:115],a[16:19]
zk a[128:131],v196 offset:24576
zm a[20:23],a[164:167],v[116:119],a[20:23]
ze v1,v1
zm a[4:7],a[160:163],v[116:119],a[4:7]
zk a[132:135],v197 offset:24576
zc v96,v0,v1
zm a[8:11],a[160:163],v[120:123],a[8:11]
ze v2,v2
zm a[24:27],a[164:167],v[120:123],a[24:27]
zk a[136:139],v198 offset:24576
zm a[28:31],a[164:167],v[124:127],a[28:31]
ze v3,v3
zm a[12:15],a[160:163],v[124:127],a[12:15]
zk a[140:143],v199 offset:24576
zc v97,v2,v3
v_smfmac_f32_16x16x64_bf16 v[80:83], v[192:195], v[112:119], v253
ze v4,v4
v_smfmac_f32_16x16x64_bf16 v[84:87], v[192:195], v[120:127], v253
zk a[144:147],v196 offset:28672
zm a[44:47],a[168:171],v[124:127],a[44:47]
ze v5,v5
zm a[60:63],a[172:175],v[124:127],a[60:63]
zk a[148:151],v197 offset:28672
zc v98,v4,v5
zm a[56:59],a[172:175],v[120:123],a[56:59]
s_waitcnt lgkmcnt(6)
ze v6,v6
zm a[40:43],a[168:171],v[120:123],a[40:43]
zk a[152:155],v198 offset:28672
zm a[36:39],a[168:171],v[116:119],a[36:39]
ze v7,v7
zm a[52:55],a[172:175],v[116:119],a[52:55]
zk a[156:159],v199 offset:28672
zc v99,v6,v7
zm a[48:51],a[172:175],v[112:115],a[48:51]
ze v8,v8
zm a[32:35],a[168:171],v[112:115],a[32:35]
zm a[64:67],a[176:179],v[112:115],a[64:67]
ze v9,v9
zm a[80:83],a[180:183],v[112:115],a[80:83]
zc v100,v8,v9
zm a[84:87],a[180:183],v[116:119],a[84:87]
ze v10,v10
zm a[68:71],a[176:179],v[116:119],a[68:71]
zm a[72:75],a[176:179],v[120:123],a[72:75]
ze v11,v11
zm a[88:91],a[180:183],v[120:123],a[88:91]
zc v101,v10,v11
zm a[92:95],a[180:183],v[124:127],a[92:95]
ze v12,v12
zm a[76:79],a[176:179],v[124:127],a[76:79]
zm a[108:111],a[184:187],v[124:127],a[108:111]
ze v13,v13
zm a[124:127],a[188:191],v[124:127],a[124:127]
zc v102,v12,v13
zm a[120:123],a[188:191],v[120:123],a[120:123]
ze v14,v14
zm a[104:107],a[184:187],v[120:123],a[104:107]
zm a[100:103],a[184:187],v[116:119],a[100:103]
ze v15,v15
zm a[116:119],a[188:191],v[116:119],a[116:119]
zc v103,v14,v15
zm a[112:115],a[188:191],v[112:115],a[112:115]
s_waitcnt vmcnt(6) lgkmcnt(0)
zm a[96:99],a[184:187],v[112:115],a[96:99]
s_barrier
zm v[32:35],a[128:131],a[192:195],v[64:67]
ze v16,v16
zm v[32:35],a[132:135],a[196:199],v[32:35]
zt a[160:161],v200 offset:16384
zm v[32:35],a[136:139],a[200:203],v[32:35]
ze v17,v17
zm v[32:35],a[140:143],a[204:207],v[32:35]
zt a[162:163],v200 offset:20480
zc v104,v16,v17
zm v[36:39],a[156:159],a[204:207],v[64:67]
s_and_b32 m0, s47, 0xffff
ze v18,v18
zm v[36:39],a[152:155],a[200:203],v[36:39]
zt a[164:165],v201 offset:16384
zl v212,s[96:99],s47 offen offset:0 lds
zm v[36:39],a[148:151],a[196:199],v[36:39]
ze v19,v19
zm v[36:39],a[144:147],a[192:195],v[36:39]
zt a[166:167],v201 offset:20480
zc v105,v18,v19
zm v[44:47],a[144:147],a[208:211],v[68:71]
ze v20,v20
zm v[44:47],a[148:151],a[212:215],v[44:47]
zt a[168:169],v202 offset:16384
zm v[44:47],a[152:155],a[216:219],v[44:47]
ze v21,v21
zm v[44:47],a[156:159],a[220:223],v[44:47]
s_add_u32 s47, s47, 0x1000
zt a[170:171],v202 offset:20480
zc v106,v20,v21
zm v[40:43],a[140:143],a[220:223],v[68:71]
s_and_b32 m0, s47, 0xffff
ze v22,v22
zm v[40:43],a[136:139],a[216:219],v[40:43]
zt a[172:173],v203 offset:16384
zl v212,s[96:99],s47 offen offset:0 lds
zm v[40:43],a[132:135],a[212:215],v[40:43]
ze v23,v23
zm v[40:43],a[128:131],a[208:211],v[40:43]
zt a[174:175],v203 offset:20480
zc v107,v22,v23
zm v[48:51],a[128:131],a[224:227],v[72:75]
ze v24,v24
zm v[48:51],a[132:135],a[228:231],v[48:51]
zt a[176:177],v204 offset:16384
zm v[48:51],a[136:139],a[232:235],v[48:51]
ze v25,v25
zm v[48:51],a[140:143],a[236:239],v[48:51]
s_add_u32 s47, s47, 0x1000
zt a[178:179],v204 offset:20480
zc v108,v24,v25
zm v[52:55],a[156:159],a[236:239],v[72:75]
s_and_b32 m0, s47, 0xffff
ze v26,v26
zm v[52:55],a[152:155],a[232:235],v[52:55]
zt a[180:181],v205 offset:16384
zl v212,s[96:99],s47 offen offset:0 nt lds
zm v[52:55],a[148:151],a[228:231],v[52:55]
ze v27,v27
zm v[52:55],a[144:147],a[224:227],v[52:55]
zt a[182:183],v205 offset:20480
zc v109,v26,v27
zm v[60:63],a[144:147],a[240:243],v[76:79]
ze v28,v28
zm v[60:63],a[148:151],a[244:247],v[60:63]
zt a[184:185],v206 offset:16384
zm v[60:63],a[152:155],a[248:251],v[60:63]
s_waitcnt lgkmcnt(5)
ze v29,v29
zm v[60:63],a[156:159],a[252:255],v[60:63]
s_add_u32 s47, s47, 0x1000
zt a[186:187],v206 offset:20480
zc v110,v28,v29
zm v[56:59],a[140:143],a[252:255],v[76:79]
s_and_b32 m0, s47, 0xffff
ze v30,v30
zm v[56:59],a[136:139],a[248:251],v[56:59]
zt a[188:189],v207 offset:16384
zl v212,s[96:99],s47 offen offset:0 nt lds
zm v[56:59],a[132:135],a[244:247],v[56:59]
s_add_u32 s47, s47, 0x1000
ze v31,v31
zm v[56:59],a[128:131],a[240:243],v[56:59]
zt a[190:191],v207 offset:20480
zc v111,v30,v31
zm a[0:3],a[160:163],v[96:99],a[0:3]
ze v32,v32
zm a[16:19],a[164:167],v[96:99],a[16:19]
zk a[128:131],v196 offset:32768
zm a[20:23],a[164:167],v[100:103],a[20:23]
ze v33,v33
zm a[4:7],a[160:163],v[100:103],a[4:7]
zk a[132:135],v197 offset:32768
zc v112,v32,v33
zm a[8:11],a[160:163],v[104:107],a[8:11]
ze v34,v34
zm a[24:27],a[164:167],v[104:107],a[24:27]
zk a[136:139],v198 offset:32768
zm a[28:31],a[164:167],v[108:111],a[28:31]
ze v35,v35
zm a[12:15],a[160:163],v[108:111],a[12:15]
zk a[140:143],v199 offset:32768
zc v113,v34,v35
v_smfmac_f32_16x16x64_bf16 v[80:83], v[192:195], v[96:103], v253
ze v36,v36
v_smfmac_f32_16x16x64_bf16 v[84:87], v[192:195], v[104:111], v253
zk a[144:147],v196 offset:36864
zm a[44:47],a[168:171],v[108:111],a[44:47]
ze v37,v37
zm a[60:63],a[172:175],v[108:111],a[60:63]
zk a[148:151],v197 offset:36864
zc v114,v36,v37
zm a[56:59],a[172:175],v[104:107],a[56:59]
s_waitcnt lgkmcnt(6)
ze v38,v38
zm a[40:43],a[168:171],v[104:107],a[40:43]
zk a[152:155],v198 offset:36864
zm a[36:39],a[168:171],v[100:103],a[36:39]
ze v39,v39
zm a[52:55],a[172:175],v[100:103],a[52:55]
s_and_b32 m0, s48, 0xffff
zk a[156:159],v199 offset:36864
zc v115,v38,v39
zm a[48:51],a[172:175],v[96:99],a[48:51]
s_bitset1_b32 m0, 16
ze v40,v40
zm a[32:35],a[168:171],v[96:99],a[32:35]
zl v212,s[36:39],s48 offen offset:0 nt lds
zm a[64:67],a[176:179],v[96:99],a[64:67]
s_add_u32 s48, s48, 0x1000
ze v41,v41
zm a[80:83],a[180:183],v[96:99],a[80:83]
s_and_b32 m0, s48, 0xffff
zc v116,v40,v41
zm a[84:87],a[180:183],v[100:103],a[84:87]
s_bitset1_b32 m0, 16
ze v42,v42
zm a[68:71],a[176:179],v[100:103],a[68:71]
zl v212,s[36:39],s48 offen offset:0 nt lds
zm a[72:75],a[176:179],v[104:107],a[72:75]
s_add_u32 s48, s48, 0x1000
ze v43,v43
zm a[88:91],a[180:183],v[104:107],a[88:91]
s_and_b32 m0, s48, 0xffff
zc v117,v42,v43
zm a[92:95],a[180:183],v[108:111],a[92:95]
s_bitset1_b32 m0, 16
ze v44,v44
zm a[76:79],a[176:179],v[108:111],a[76:79]
zl v212,s[36:39],s48 offen offset:0 nt lds
zm a[108:111],a[184:187],v[108:111],a[108:111]
s_add_u32 s48, s48, 0x1000
ze v45,v45
zm a[124:127],a[188:191],v[108:111],a[124:127]
s_and_b32 m0, s48, 0xffff
zc v118,v44,v45
zm a[120:123],a[188:191],v[104:107],a[120:123]
s_bitset1_b32 m0, 16
ze v46,v46
zm a[104:107],a[184:187],v[104:107],a[104:107]
zl v212,s[36:39],s48 offen offset:0 nt lds
zm a[100:103],a[184:187],v[100:103],a[100:103]
s_add_u32 s48, s48, 0x1000
ze v47,v47
zm a[116:119],a[188:191],v[100:103],a[116:119]
zc v119,v46,v47
zm a[112:115],a[188:191],v[96:99],a[112:115]
s_waitcnt lgkmcnt(0)
zm a[96:99],a[184:187],v[96:99],a[96:99]
zm v[0:3],a[128:131],a[192:195],v[64:67]
ze v48,v48
zm v[0:3],a[132:135],a[196:199],v[0:3]
zt a[160:161],v200 offset:24576
global_load_dword v254, v255, s[52:53]
zm v[0:3],a[136:139],a[200:203],v[0:3]
ze v49,v49
zm v[0:3],a[140:143],a[204:207],v[0:3]
zt a[162:163],v200 offset:28672
zc v120,v48,v49
zm v[4:7],a[156:159],a[204:207],v[64:67]
ze v50,v50
zm v[4:7],a[152:155],a[200:203],v[4:7]
zt a[164:165],v201 offset:24576
zm v[4:7],a[148:151],a[196:199],v[4:7]
ze v51,v51
zm v[4:7],a[144:147],a[192:195],v[4:7]
zt a[166:167],v201 offset:28672
zc v121,v50,v51
zm v[12:15],a[144:147],a[208:211],v[68:71]
ze v52,v52
zm v[12:15],a[148:151],a[212:215],v[12:15]
zt a[168:169],v202 offset:24576
zm v[12:15],a[152:155],a[216:219],v[12:15]
ze v53,v53
zm v[12:15],a[156:159],a[220:223],v[12:15]
zt a[170:171],v202 offset:28672
zc v122,v52,v53
zm v[8:11],a[140:143],a[220:223],v[68:71]
ze v54,v54
zm v[8:11],a[136:139],a[216:219],v[8:11]
zt a[172:173],v203 offset:24576
zm v[8:11],a[132:135],a[212:215],v[8:11]
ze v55,v55
zm v[8:11],a[128:131],a[208:211],v[8:11]
zt a[174:175],v203 offset:28672
zc v123,v54,v55
zm v[16:19],a[128:131],a[224:227],v[72:75]
ze v56,v56
zm v[16:19],a[132:135],a[228:231],v[16:19]
zt a[176:177],v204 offset:24576
zm v[16:19],a[136:139],a[232:235],v[16:19]
ze v57,v57
zm v[16:19],a[140:143],a[236:239],v[16:19]
zt a[178:179],v204 offset:28672
zc v124,v56,v57
zm v[20:23],a[156:159],a[236:239],v[72:75]
ze v58,v58
zm v[20:23],a[152:155],a[232:235],v[20:23]
zt a[180:181],v205 offset:24576
zm v[20:23],a[148:151],a[228:231],v[20:23]
ze v59,v59
zm v[20:23],a[144:147],a[224:227],v[20:23]
zt a[182:183],v205 offset:28672
zc v125,v58,v59
zm v[28:31],a[144:147],a[240:243],v[76:79]
ze v60,v60
zm v[28:31],a[148:151],a[244:247],v[28:31]
zt a[184:185],v206 offset:24576
zm v[28:31],a[152:155],a[248:251],v[28:31]
s_waitcnt lgkmcnt(5)
ze v61,v61
zm v[28:31],a[156:159],a[252:255],v[28:31]
zt a[186:187],v206 offset:28672
zc v126,v60,v61
zm v[24:27],a[140:143],a[252:255],v[76:79]
ze v62,v62
zm v[24:27],a[136:139],a[248:251],v[24:27]
zt a[188:189],v207 offset:24576
zm v[24:27],a[132:135],a[244:247],v[24:27]
ze v63,v63
zm v[24:27],a[128:131],a[240:243],v[24:27]
zt a[190:191],v207 offset:28672
zc v127,v62,v63
zm a[0:3],a[160:163],v[112:115],a[0:3]
ze v0,v0
zm a[16:19],a[164:167],v[112:115],a[16:19]
zk a[128:131],v196 offset:40960
zm a[20:23],a[164:167],v[116:119],a[20:23]
ze v1,v1
zm a[4:7],a[160:163],v[116:119],a[4:7]
zk a[132:135],v197 offset:40960
zc v96,v0,v1
zm a[8:11],a[160:163],v[120:123],a[8:11]
ze v2,v2
zm a[24:27],a[164:167],v[120:123],a[24:27]
zk a[136:139],v198 offset:40960
zm a[28:31],a[164:167],v[124:127],a[28:31]
ze v3,v3
zm a[12:15],a[160:163],v[124:127],a[12:15]
zk a[140:143],v199 offset:40960
zc v97,v2,v3
v_smfmac_f32_16x16x64_bf16 v[80:83], v[192:195], v[112:119], v253
ze v4,v4
v_smfmac_f32_16x16x64_bf16 v[84:87], v[192:195], v[120:127], v253
zk a[144:147],v196 offset:45056
zm a[44:47],a[168:171],v[124:127],a[44:47]
ze v5,v5
zm a[60:63],a[172:175],v[124:127],a[60:63]
zk a[148:151],v197 offset:45056
zc v98,v4,v5
zm a[56:59],a[172:175],v[120:123],a[56:59]
s_waitcnt lgkmcnt(6)
ze v6,v6
zm a[40:43],a[168:171],v[120:123],a[40:43]
zk a[152:155],v198 offset:45056
zm a[36:39],a[168:171],v[116:119],a[36:39]
ze v7,v7
zm a[52:55],a[172:175],v[116:119],a[52:55]
zk a[156:159],v199 offset:45056
zc v99,v6,v7
zm a[48:51],a[172:175],v[112:115],a[48:51]
ze v8,v8
zm a[32:35],a[168:171],v[112:115],a[32:35]
zm a[64:67],a[176:179],v[112:115],a[64:67]
ze v9,v9
zm a[80:83],a[180:183],v[112:115],a[80:83]
zc v100,v8,v9
zm a[84:87],a[180:183],v[116:119],a[84:87]
ze v10,v10
zm a[68:71],a[176:179],v[116:119],a[68:71]
zm a[72:75],a[176:179],v[120:123],a[72:75]
ze v11,v11
zm a[88:91],a[180:183],v[120:123],a[88:91]
zc v101,v10,v11
zm a[92:95],a[180:183],v[124:127],a[92:95]
ze v12,v12
zm a[76:79],a[176:179],v[124:127],a[76:79]
zm a[108:111],a[184:187],v[124:127],a[108:111]
ze v13,v13
zm a[124:127],a[188:191],v[124:127],a[124:127]
zc v102,v12,v13
zm a[120:123],a[188:191],v[120:123],a[120:123]
ze v14,v14
zm a[104:107],a[184:187],v[120:123],a[104:107]
zm a[100:103],a[184:187],v[116:119],a[100:103]
ze v15,v15
zm a[116:119],a[188:191],v[116:119],a[116:119]
zc v103,v14,v15
zm a[112:115],a[188:191],v[112:115],a[112:115]
s_waitcnt vmcnt(6) lgkmcnt(0)
zm a[96:99],a[184:187],v[112:115],a[96:99]
s_barrier
zm v[32:35],a[128:131],a[192:195],v[64:67]
ze v16,v16
zm v[32:35],a[132:135],a[196:199],v[32:35]
zt a[160:161],v200 offset:32768
zm v[32:35],a[136:139],a[200:203],v[32:35]
ze v17,v17
zm v[32:35],a[140:143],a[204:207],v[32:35]
zt a[162:163],v200 offset:36864
zc v104,v16,v17
zm v[36:39],a[156:159],a[204:207],v[64:67]
s_and_b32 m0, s47, 0xffff
ze v18,v18
zm v[36:39],a[152:155],a[200:203],v[36:39]
zt a[164:165],v201 offset:32768
zl v212,s[96:99],s47 offen offset:0 lds
zm v[36:39],a[148:151],a[196:199],v[36:39]
ze v19,v19
zm v[36:39],a[144:147],a[192:195],v[36:39]
zt a[166:167],v201 offset:36864
zc v105,v18,v19
zm v[44:47],a[144:147],a[208:211],v[68:71]
ze v20,v20
zm v[44:47],a[148:151],a[212:215],v[44:47]
zt a[168:169],v202 offset:32768
zm v[44:47],a[152:155],a[216:219],v[44:47]
ze v21,v21
zm v[44:47],a[156:159],a[220:223],v[44:47]
s_add_u32 s47, s47, 0x1000
zt a[170:171],v202 offset:36864
zc v106,v20,v21
zm v[40:43],a[140:143],a[220:223],v[68:71]
s_and_b32 m0, s47, 0xffff
ze v22,v22
zm v[40:43],a[136:139],a[216:219],v[40:43]
zt a[172:173],v203 offset:32768
zl v212,s[96:99],s47 offen offset:0 lds
zm v[40:43],a[132:135],a[212:215],v[40:43]
ze v23,v23
zm v[40:43],a[128:131],a[208:211],v[40:43]
zt a[174:175],v203 offset:36864
zc v107,v22,v23
zm v[48:51],a[128:131],a[224:227],v[72:75]
ze v24,v24
zm v[48:51],a[132:135],a[228:231],v[48:51]
zt a[176:177],v204 offset:32768
zm v[48:51],a[136:139],a[232:235],v[48:51]
ze v25,v25
zm v[48:51],a[140:143],a[236:239],v[48:51]
s_add_u32 s47, s47, 0x1000
zt a[178:179],v204 offset:36864
zc v108,v24,v25
zm v[52:55],a[156:159],a[236:239],v[72:75]
s_and_b32 m0, s47, 0xffff
ze v26,v26
zm v[52:55],a[152:155],a[232:235],v[52:55]
zt a[180:181],v205 offset:32768
zl v212,s[96:99],s47 offen offset:0 nt lds
zm v[52:55],a[148:151],a[228:231],v[52:55]
ze v27,v27
zm v[52:55],a[144:147],a[224:227],v[52:55]
zt a[182:183],v205 offset:36864
zc v109,v26,v27
zm v[60:63],a[144:147],a[240:243],v[76:79]
ze v28,v28
zm v[60:63],a[148:151],a[244:247],v[60:63]
zt a[184:185],v206 offset:32768
zm v[60:63],a[152:155],a[248:251],v[60:63]
s_waitcnt lgkmcnt(5)
ze v29,v29
zm v[60:63],a[156:159],a[252:255],v[60:63]
s_add_u32 s47, s47, 0x1000
zt a[186:187],v206 offset:36864
zc v110,v28,v29
zm v[56:59],a[140:143],a[252:255],v[76:79]
s_and_b32 m0, s47, 0xffff
ze v30,v30
zm v[56:59],a[136:139],a[248:251],v[56:59]
zt a[188:189],v207 offset:32768
zl v212,s[96:99],s47 offen offset:0 nt lds
zm v[56:59],a[132:135],a[244:247],v[56:59]
s_add_u32 s47, s47, 0x1000
ze v31,v31
zm v[56:59],a[128:131],a[240:243],v[56:59]
zt a[190:191],v207 offset:36864
zc v111,v30,v31
zm a[0:3],a[160:163],v[96:99],a[0:3]
ze v32,v32
zm a[16:19],a[164:167],v[96:99],a[16:19]
zk a[128:131],v196 offset:49152
zm a[20:23],a[164:167],v[100:103],a[20:23]
ze v33,v33
zm a[4:7],a[160:163],v[100:103],a[4:7]
zk a[132:135],v197 offset:49152
zc v112,v32,v33
zm a[8:11],a[160:163],v[104:107],a[8:11]
ze v34,v34
zm a[24:27],a[164:167],v[104:107],a[24:27]
zk a[136:139],v198 offset:49152
zm a[28:31],a[164:167],v[108:111],a[28:31]
ze v35,v35
zm a[12:15],a[160:163],v[108:111],a[12:15]
zk a[140:143],v199 offset:49152
zc v113,v34,v35
v_smfmac_f32_16x16x64_bf16 v[80:83], v[192:195], v[96:103], v253
ze v36,v36
v_smfmac_f32_16x16x64_bf16 v[84:87], v[192:195], v[104:111], v253
zk a[144:147],v196 offset:53248
zm a[44:47],a[168:171],v[108:111],a[44:47]
ze v37,v37
zm a[60:63],a[172:175],v[108:111],a[60:63]
zk a[148:151],v197 offset:53248
zc v114,v36,v37
zm a[56:59],a[172:175],v[104:107],a[56:59]
s_waitcnt lgkmcnt(6)
ze v38,v38
zm a[40:43],a[168:171],v[104:107],a[40:43]
zk a[152:155],v198 offset:53248
zm a[36:39],a[168:171],v[100:103],a[36:39]
ze v39,v39
zm a[52:55],a[172:175],v[100:103],a[52:55]
s_and_b32 m0, s48, 0xffff
zk a[156:159],v199 offset:53248
zc v115,v38,v39
zm a[48:51],a[172:175],v[96:99],a[48:51]
s_bitset1_b32 m0, 16
ze v40,v40
zm a[32:35],a[168:171],v[96:99],a[32:35]
zl v212,s[36:39],s48 offen offset:0 nt lds
zm a[64:67],a[176:179],v[96:99],a[64:67]
s_add_u32 s48, s48, 0x1000
ze v41,v41
zm a[80:83],a[180:183],v[96:99],a[80:83]
s_and_b32 m0, s48, 0xffff
zc v116,v40,v41
zm a[84:87],a[180:183],v[100:103],a[84:87]
s_bitset1_b32 m0, 16
ze v42,v42
zm a[68:71],a[176:179],v[100:103],a[68:71]
zl v212,s[36:39],s48 offen offset:0 nt lds
zm a[72:75],a[176:179],v[104:107],a[72:75]
s_add_u32 s48, s48, 0x1000
ze v43,v43
zm a[88:91],a[180:183],v[104:107],a[88:91]
s_and_b32 m0, s48, 0xffff
zc v117,v42,v43
zm a[92:95],a[180:183],v[108:111],a[92:95]
s_bitset1_b32 m0, 16
ze v44,v44
zm a[76:79],a[176:179],v[108:111],a[76:79]
zl v212,s[36:39],s48 offen offset:0 nt lds
zm a[108:111],a[184:187],v[108:111],a[108:111]
s_add_u32 s48, s48, 0x1000
ze v45,v45
zm a[124:127],a[188:191],v[108:111],a[124:127]
s_and_b32 m0, s48, 0xffff
zc v118,v44,v45
zm a[120:123],a[188:191],v[104:107],a[120:123]
s_bitset1_b32 m0, 16
ze v46,v46
zm a[104:107],a[184:187],v[104:107],a[104:107]
zl v212,s[36:39],s48 offen offset:0 nt lds
zm a[100:103],a[184:187],v[100:103],a[100:103]
s_add_u32 s48, s48, 0x1000
ze v47,v47
zm a[116:119],a[188:191],v[100:103],a[116:119]
zc v119,v46,v47
zm a[112:115],a[188:191],v[96:99],a[112:115]
s_waitcnt lgkmcnt(0)
zm a[96:99],a[184:187],v[96:99],a[96:99]
zm v[0:3],a[128:131],a[192:195],v[64:67]
ze v48,v48
zm v[0:3],a[132:135],a[196:199],v[0:3]
zt a[160:161],v200 offset:40960
zm v[0:3],a[136:139],a[200:203],v[0:3]
ze v49,v49
zm v[0:3],a[140:143],a[204:207],v[0:3]
zt a[162:163],v200 offset:45056
zc v120,v48,v49
zm v[4:7],a[156:159],a[204:207],v[64:67]
ze v50,v50
zm v[4:7],a[152:155],a[200:203],v[4:7]
zt a[164:165],v201 offset:40960
zm v[4:7],a[148:151],a[196:199],v[4:7]
ze v51,v51
zm v[4:7],a[144:147],a[192:195],v[4:7]
zt a[166:167],v201 offset:45056
zc v121,v50,v51
zm v[12:15],a[144:147],a[208:211],v[68:71]
ze v52,v52
zm v[12:15],a[148:151],a[212:215],v[12:15]
zt a[168:169],v202 offset:40960
zm v[12:15],a[152:155],a[216:219],v[12:15]
ze v53,v53
zm v[12:15],a[156:159],a[220:223],v[12:15]
zt a[170:171],v202 offset:45056
zc v122,v52,v53
zm v[8:11],a[140:143],a[220:223],v[68:71]
ze v54,v54
zm v[8:11],a[136:139],a[216:219],v[8:11]
zt a[172:173],v203 offset:40960
zm v[8:11],a[132:135],a[212:215],v[8:11]
ze v55,v55
zm v[8:11],a[128:131],a[208:211],v[8:11]
zt a[174:175],v203 offset:45056
zc v123,v54,v55
zm v[16:19],a[128:131],a[224:227],v[72:75]
ze v56,v56
zm v[16:19],a[132:135],a[228:231],v[16:19]
zt a[176:177],v204 offset:40960
zm v[16:19],a[136:139],a[232:235],v[16:19]
ze v57,v57
zm v[16:19],a[140:143],a[236:239],v[16:19]
zt a[178:179],v204 offset:45056
zc v124,v56,v57
zm v[20:23],a[156:159],a[236:239],v[72:75]
ze v58,v58
zm v[20:23],a[152:155],a[232:235],v[20:23]
zt a[180:181],v205 offset:40960
zm v[20:23],a[148:151],a[228:231],v[20:23]
ze v59,v59
zm v[20:23],a[144:147],a[224:227],v[20:23]
zt a[182:183],v205 offset:45056
zc v125,v58,v59
zm v[28:31],a[144:147],a[240:243],v[76:79]
ze v60,v60
zm v[28:31],a[148:151],a[244:247],v[28:31]
zt a[184:185],v206 offset:40960
zm v[28:31],a[152:155],a[248:251],v[28:31]
s_waitcnt lgkmcnt(5)
ze v61,v61
zm v[28:31],a[156:159],a[252:255],v[28:31]
zt a[186:187],v206 offset:45056
zc v126,v60,v61
zm v[24:27],a[140:143],a[252:255],v[76:79]
ze v62,v62
zm v[24:27],a[136:139],a[248:251],v[24:27]
zt a[188:189],v207 offset:40960
zm v[24:27],a[132:135],a[244:247],v[24:27]
)"
R"(ze v63,v63
zm v[24:27],a[128:131],a[240:243],v[24:27]
zt a[190:191],v207 offset:45056
zc v127,v62,v63
zm a[0:3],a[160:163],v[112:115],a[0:3]
ze v0,v0
zm a[16:19],a[164:167],v[112:115],a[16:19]
zk a[128:131],v196 offset:57344
zm a[20:23],a[164:167],v[116:119],a[20:23]
ze v1,v1
zm a[4:7],a[160:163],v[116:119],a[4:7]
zk a[132:135],v197 offset:57344
zc v96,v0,v1
zm a[8:11],a[160:163],v[120:123],a[8:11]
ze v2,v2
zm a[24:27],a[164:167],v[120:123],a[24:27]
zk a[136:139],v198 offset:57344
zm a[28:31],a[164:167],v[124:127],a[28:31]
ze v3,v3
zm a[12:15],a[160:163],v[124:127],a[12:15]
zk a[140:143],v199 offset:57344
zc v97,v2,v3
v_smfmac_f32_16x16x64_bf16 v[80:83], v[192:195], v[112:119], v253
ze v4,v4
v_smfmac_f32_16x16x64_bf16 v[84:87], v[192:195], v[120:127], v253
zk a[144:147],v196 offset:61440
zm a[44:47],a[168:171],v[124:127],a[44:47]
ze v5,v5
zm a[60:63],a[172:175],v[124:127],a[60:63]
zk a[148:151],v197 offset:61440
zc v98,v4,v5
zm a[56:59],a[172:175],v[120:123],a[56:59]
s_waitcnt lgkmcnt(6)
ze v6,v6
zm a[40:43],a[168:171],v[120:123],a[40:43]
zk a[152:155],v198 offset:61440
zm a[36:39],a[168:171],v[116:119],a[36:39]
ze v7,v7
zm a[52:55],a[172:175],v[116:119],a[52:55]
zk a[156:159],v199 offset:61440
zc v99,v6,v7
zm a[48:51],a[172:175],v[112:115],a[48:51]
ze v8,v8
zm a[32:35],a[168:171],v[112:115],a[32:35]
zm a[64:67],a[176:179],v[112:115],a[64:67]
ze v9,v9
zm a[80:83],a[180:183],v[112:115],a[80:83]
zc v100,v8,v9
zm a[84:87],a[180:183],v[116:119],a[84:87]
ze v10,v10
zm a[68:71],a[176:179],v[116:119],a[68:71]
zm a[72:75],a[176:179],v[120:123],a[72:75]
ze v11,v11
zm a[88:91],a[180:183],v[120:123],a[88:91]
zc v101,v10,v11
zm a[92:95],a[180:183],v[124:127],a[92:95]
ze v12,v12
zm a[76:79],a[176:179],v[124:127],a[76:79]
zm a[108:111],a[184:187],v[124:127],a[108:111]
ze v13,v13
zm a[124:127],a[188:191],v[124:127],a[124:127]
zc v102,v12,v13
zm a[120:123],a[188:191],v[120:123],a[120:123]
ze v14,v14
zm a[104:107],a[184:187],v[120:123],a[104:107]
zm a[100:103],a[184:187],v[116:119],a[100:103]
ze v15,v15
zm a[116:119],a[188:191],v[116:119],a[116:119]
zc v103,v14,v15
zm a[112:115],a[188:191],v[112:115],a[112:115]
s_waitcnt vmcnt(6) lgkmcnt(0)
zm a[96:99],a[184:187],v[112:115],a[96:99]
s_barrier
zm v[32:35],a[128:131],a[192:195],v[64:67]
ze v16,v16
zm v[32:35],a[132:135],a[196:199],v[32:35]
zt a[160:161],v200 offset:49152
zm v[32:35],a[136:139],a[200:203],v[32:35]
ze v17,v17
zm v[32:35],a[140:143],a[204:207],v[32:35]
zt a[162:163],v200 offset:53248
zc v104,v16,v17
zm v[36:39],a[156:159],a[204:207],v[64:67]
s_and_b32 m0, s47, 0xffff
ze v18,v18
zm v[36:39],a[152:155],a[200:203],v[36:39]
zt a[164:165],v201 offset:49152
zl v212,s[96:99],s47 offen offset:0 lds
zm v[36:39],a[148:151],a[196:199],v[36:39]
ze v19,v19
zm v[36:39],a[144:147],a[192:195],v[36:39]
zt a[166:167],v201 offset:53248
zc v105,v18,v19
zm v[44:47],a[144:147],a[208:211],v[68:71]
ze v20,v20
zm v[44:47],a[148:151],a[212:215],v[44:47]
zt a[168:169],v202 offset:49152
zm v[44:47],a[152:155],a[216:219],v[44:47]
ze v21,v21
zm v[44:47],a[156:159],a[220:223],v[44:47]
s_add_u32 s47, s47, 0x1000
zt a[170:171],v202 offset:53248
zc v106,v20,v21
zm v[40:43],a[140:143],a[220:223],v[68:71]
s_and_b32 m0, s47, 0xffff
ze v22,v22
zm v[40:43],a[136:139],a[216:219],v[40:43]
zt a[172:173],v203 offset:49152
zl v212,s[96:99],s47 offen offset:0 lds
zm v[40:43],a[132:135],a[212:215],v[40:43]
ze v23,v23
zm v[40:43],a[128:131],a[208:211],v[40:43]
zt a[174:175],v203 offset:53248
zc v107,v22,v23
zm v[48:51],a[128:131],a[224:227],v[72:75]
ze v24,v24
zm v[48:51],a[132:135],a[228:231],v[48:51]
zt a[176:177],v204 offset:49152
zm v[48:51],a[136:139],a[232:235],v[48:51]
ze v25,v25
zm v[48:51],a[140:143],a[236:239],v[48:51]
s_add_u32 s47, s47, 0x1000
zt a[178:179],v204 offset:53248
zc v108,v24,v25
zm v[52:55],a[156:159],a[236:239],v[72:75]
s_and_b32 m0, s47, 0xffff
ze v26,v26
zm v[52:55],a[152:155],a[232:235],v[52:55]
zt a[180:181],v205 offset:49152
zl v212,s[96:99],s47 offen offset:0 nt lds
zm v[52:55],a[148:151],a[228:231],v[52:55]
ze v27,v27
zm v[52:55],a[144:147],a[224:227],v[52:55]
zt a[182:183],v205 offset:53248
zc v109,v26,v27
zm v[60:63],a[144:147],a[240:243],v[76:79]
ze v28,v28
zm v[60:63],a[148:151],a[244:247],v[60:63]
zt a[184:185],v206 offset:49152
zm v[60:63],a[152:155],a[248:251],v[60:63]
s_waitcnt lgkmcnt(5)
ze v29,v29
zm v[60:63],a[156:159],a[252:255],v[60:63]
s_add_u32 s47, s47, 0x1000
zt a[186:187],v206 offset:53248
zc v110,v28,v29
zm v[56:59],a[140:143],a[252:255],v[76:79]
s_and_b32 m0, s47, 0xffff
ze v30,v30
zm v[56:59],a[136:139],a[248:251],v[56:59]
zt a[188:189],v207 offset:49152
zl v212,s[96:99],s47 offen offset:0 nt lds
zm v[56:59],a[132:135],a[244:247],v[56:59]
s_add_u32 s47, s47, 0x1000
ze v31,v31
zm v[56:59],a[128:131],a[240:243],v[56:59]
zt a[190:191],v207 offset:53248
zc v111,v30,v31
zm a[0:3],a[160:163],v[96:99],a[0:3]
ze v32,v32
zm a[16:19],a[164:167],v[96:99],a[16:19]
zk a[128:131],v196 offset:0
zm a[20:23],a[164:167],v[100:103],a[20:23]
s_add_u32 s51, s51, 1
ze v33,v33
zm a[4:7],a[160:163],v[100:103],a[4:7]
zk a[132:135],v197 offset:0
zc v112,v32,v33
zm a[8:11],a[160:163],v[104:107],a[8:11]
ze v34,v34
zm a[24:27],a[164:167],v[104:107],a[24:27]
zk a[136:139],v198 offset:0
zm a[28:31],a[164:167],v[108:111],a[28:31]
ze v35,v35
zm a[12:15],a[160:163],v[108:111],a[12:15]
zk a[140:143],v199 offset:0
zc v113,v34,v35
v_smfmac_f32_16x16x64_bf16 v[80:83], v[192:195], v[96:103], v253
ze v36,v36
v_smfmac_f32_16x16x64_bf16 v[84:87], v[192:195], v[104:111], v253
zk a[144:147],v196 offset:4096
zm a[44:47],a[168:171],v[108:111],a[44:47]
ze v37,v37
zm a[60:63],a[172:175],v[108:111],a[60:63]
zk a[148:151],v197 offset:4096
zc v114,v36,v37
zm a[56:59],a[172:175],v[104:107],a[56:59]
s_waitcnt lgkmcnt(6)
ze v38,v38
zm a[40:43],a[168:171],v[104:107],a[40:43]
zk a[152:155],v198 offset:4096
zm a[36:39],a[168:171],v[100:103],a[36:39]
ze v39,v39
zm a[52:55],a[172:175],v[100:103],a[52:55]
s_and_b32 m0, s48, 0xffff
zk a[156:159],v199 offset:4096
zc v115,v38,v39
zm a[48:51],a[172:175],v[96:99],a[48:51]
s_bitset1_b32 m0, 16
ze v40,v40
zm a[32:35],a[168:171],v[96:99],a[32:35]
zl v212,s[36:39],s48 offen offset:0 nt lds
zm a[64:67],a[176:179],v[96:99],a[64:67]
s_add_u32 s48, s48, 0x1000
ze v41,v41
zm a[80:83],a[180:183],v[96:99],a[80:83]
s_and_b32 m0, s48, 0xffff
zc v116,v40,v41
zm a[84:87],a[180:183],v[100:103],a[84:87]
s_bitset1_b32 m0, 16
ze v42,v42
zm a[68:71],a[176:179],v[100:103],a[68:71]
zl v212,s[36:39],s48 offen offset:0 nt lds
zm a[72:75],a[176:179],v[104:107],a[72:75]
s_add_u32 s48, s48, 0x1000
ze v43,v43
zm a[88:91],a[180:183],v[104:107],a[88:91]
s_and_b32 m0, s48, 0xffff
zc v117,v42,v43
zm a[92:95],a[180:183],v[108:111],a[92:95]
s_bitset1_b32 m0, 16
ze v44,v44
zm a[76:79],a[176:179],v[108:111],a[76:79]
zl v212,s[36:39],s48 offen offset:0 nt lds
zm a[108:111],a[184:187],v[108:111],a[108:111]
s_add_u32 s48, s48, 0x1000
ze v45,v45
zm a[124:127],a[188:191],v[108:111],a[124:127]
s_and_b32 m0, s48, 0xffff
zc v118,v44,v45
zm a[120:123],a[188:191],v[104:107],a[120:123]
s_bitset1_b32 m0, 16
ze v46,v46
zm a[104:107],a[184:187],v[104:107],a[104:107]
zl v212,s[36:39],s48 offen offset:0 nt lds
zm a[100:103],a[184:187],v[100:103],a[100:103]
s_add_u32 s48, s48, 0x1000
ze v47,v47
zm a[116:119],a[188:191],v[100:103],a[116:119]
zc v119,v46,v47
zm a[112:115],a[188:191],v[96:99],a[112:115]
s_cmp_eq_u32 s51, 4
s_waitcnt lgkmcnt(0)
zm a[96:99],a[184:187],v[96:99],a[96:99]
s_cbranch_scc1 L_seam%=
zm v[0:3],a[128:131],a[192:195],v[64:67]
ze v48,v48
zm v[0:3],a[132:135],a[196:199],v[0:3]
zt a[160:161],v200 offset:57344
zm v[0:3],a[136:139],a[200:203],v[0:3]
ze v49,v49
zm v[0:3],a[140:143],a[204:207],v[0:3]
zt a[162:163],v200 offset:61440
zc v120,v48,v49
zm v[4:7],a[156:159],a[204:207],v[64:67]
ze v50,v50
zm v[4:7],a[152:155],a[200:203],v[4:7]
zt a[164:165],v201 offset:57344
zm v[4:7],a[148:151],a[196:199],v[4:7]
ze v51,v51
zm v[4:7],a[144:147],a[192:195],v[4:7]
zt a[166:167],v201 offset:61440
zc v121,v50,v51
zm v[12:15],a[144:147],a[208:211],v[68:71]
ze v52,v52
zm v[12:15],a[148:151],a[212:215],v[12:15]
zt a[168:169],v202 offset:57344
zm v[12:15],a[152:155],a[216:219],v[12:15]
ze v53,v53
zm v[12:15],a[156:159],a[220:223],v[12:15]
zt a[170:171],v202 offset:61440
zc v122,v52,v53
zm v[8:11],a[140:143],a[220:223],v[68:71]
ze v54,v54
zm v[8:11],a[136:139],a[216:219],v[8:11]
zt a[172:173],v203 offset:57344
zm v[8:11],a[132:135],a[212:215],v[8:11]
ze v55,v55
zm v[8:11],a[128:131],a[208:211],v[8:11]
zt a[174:175],v203 offset:61440
zc v123,v54,v55
zm v[16:19],a[128:131],a[224:227],v[72:75]
ze v56,v56
zm v[16:19],a[132:135],a[228:231],v[16:19]
zt a[176:177],v204 offset:57344
zm v[16:19],a[136:139],a[232:235],v[16:19]
ze v57,v57
zm v[16:19],a[140:143],a[236:239],v[16:19]
zt a[178:179],v204 offset:61440
zc v124,v56,v57
zm v[20:23],a[156:159],a[236:239],v[72:75]
ze v58,v58
zm v[20:23],a[152:155],a[232:235],v[20:23]
zt a[180:181],v205 offset:57344
zm v[20:23],a[148:151],a[228:231],v[20:23]
ze v59,v59
zm v[20:23],a[144:147],a[224:227],v[20:23]
zt a[182:183],v205 offset:61440
zc v125,v58,v59
zm v[28:31],a[144:147],a[240:243],v[76:79]
ze v60,v60
zm v[28:31],a[148:151],a[244:247],v[28:31]
zt a[184:185],v206 offset:57344
zm v[28:31],a[152:155],a[248:251],v[28:31]
s_waitcnt lgkmcnt(5)
ze v61,v61
zm v[28:31],a[156:159],a[252:255],v[28:31]
zt a[186:187],v206 offset:61440
zc v126,v60,v61
zm v[24:27],a[140:143],a[252:255],v[76:79]
ze v62,v62
zm v[24:27],a[136:139],a[248:251],v[24:27]
zt a[188:189],v207 offset:57344
zm v[24:27],a[132:135],a[244:247],v[24:27]
ze v63,v63
zm v[24:27],a[128:131],a[240:243],v[24:27]
zt a[190:191],v207 offset:61440
zc v127,v62,v63
zm a[0:3],a[160:163],v[112:115],a[0:3]
ze v0,v0
zm a[16:19],a[164:167],v[112:115],a[16:19]
zk a[128:131],v196 offset:8192
zm a[20:23],a[164:167],v[116:119],a[20:23]
ze v1,v1
zm a[4:7],a[160:163],v[116:119],a[4:7]
zk a[132:135],v197 offset:8192
zc v96,v0,v1
zm a[8:11],a[160:163],v[120:123],a[8:11]
ze v2,v2
zm a[24:27],a[164:167],v[120:123],a[24:27]
zk a[136:139],v198 offset:8192
zm a[28:31],a[164:167],v[124:127],a[28:31]
ze v3,v3
zm a[12:15],a[160:163],v[124:127],a[12:15]
zk a[140:143],v199 offset:8192
zc v97,v2,v3
v_smfmac_f32_16x16x64_bf16 v[80:83], v[192:195], v[112:119], v253
ze v4,v4
v_smfmac_f32_16x16x64_bf16 v[84:87], v[192:195], v[120:127], v253
zk a[144:147],v196 offset:12288
zm a[44:47],a[168:171],v[124:127],a[44:47]
ze v5,v5
zm a[60:63],a[172:175],v[124:127],a[60:63]
zk a[148:151],v197 offset:12288
zc v98,v4,v5
zm a[56:59],a[172:175],v[120:123],a[56:59]
s_waitcnt lgkmcnt(6)
ze v6,v6
zm a[40:43],a[168:171],v[120:123],a[40:43]
zk a[152:155],v198 offset:12288
zm a[36:39],a[168:171],v[116:119],a[36:39]
ze v7,v7
zm a[52:55],a[172:175],v[116:119],a[52:55]
zk a[156:159],v199 offset:12288
zc v99,v6,v7
zm a[48:51],a[172:175],v[112:115],a[48:51]
ze v8,v8
zm a[32:35],a[168:171],v[112:115],a[32:35]
zm a[64:67],a[176:179],v[112:115],a[64:67]
ze v9,v9
zm a[80:83],a[180:183],v[112:115],a[80:83]
zc v100,v8,v9
zm a[84:87],a[180:183],v[116:119],a[84:87]
ze v10,v10
zm a[68:71],a[176:179],v[116:119],a[68:71]
zm a[72:75],a[176:179],v[120:123],a[72:75]
ze v11,v11
zm a[88:91],a[180:183],v[120:123],a[88:91]
zc v101,v10,v11
zm a[92:95],a[180:183],v[124:127],a[92:95]
ze v12,v12
zm a[76:79],a[176:179],v[124:127],a[76:79]
zm a[108:111],a[184:187],v[124:127],a[108:111]
ze v13,v13
zm a[124:127],a[188:191],v[124:127],a[124:127]
zc v102,v12,v13
zm a[120:123],a[188:191],v[120:123],a[120:123]
ze v14,v14
zm a[104:107],a[184:187],v[120:123],a[104:107]
zm a[100:103],a[184:187],v[116:119],a[100:103]
ze v15,v15
zm a[116:119],a[188:191],v[116:119],a[116:119]
s_cmp_eq_u32 s51, 7
zc v103,v14,v15
zm a[112:115],a[188:191],v[112:115],a[112:115]
s_waitcnt vmcnt(6) lgkmcnt(0)
zm a[96:99],a[184:187],v[112:115],a[96:99]
s_barrier
s_cbranch_scc1 L_last%=
zm v[32:35],a[128:131],a[192:195],v[64:67]
ze v16,v16
zm v[32:35],a[132:135],a[196:199],v[32:35]
zt a[160:161],v200 offset:0
zm v[32:35],a[136:139],a[200:203],v[32:35]
ze v17,v17
zm v[32:35],a[140:143],a[204:207],v[32:35]
zt a[162:163],v200 offset:4096
zc v104,v16,v17
zm v[36:39],a[156:159],a[204:207],v[64:67]
s_and_b32 m0, s47, 0xffff
ze v18,v18
zm v[36:39],a[152:155],a[200:203],v[36:39]
zt a[164:165],v201 offset:0
zl v212,s[96:99],s47 offen offset:0 lds
zm v[36:39],a[148:151],a[196:199],v[36:39]
ze v19,v19
zm v[36:39],a[144:147],a[192:195],v[36:39]
zt a[166:167],v201 offset:4096
zc v105,v18,v19
zm v[44:47],a[144:147],a[208:211],v[68:71]
ze v20,v20
zm v[44:47],a[148:151],a[212:215],v[44:47]
zt a[168:169],v202 offset:0
zm v[44:47],a[152:155],a[216:219],v[44:47]
ze v21,v21
zm v[44:47],a[156:159],a[220:223],v[44:47]
s_add_u32 s47, s47, 0x1000
zt a[170:171],v202 offset:4096
zc v106,v20,v21
zm v[40:43],a[140:143],a[220:223],v[68:71]
s_and_b32 m0, s47, 0xffff
ze v22,v22
zm v[40:43],a[136:139],a[216:219],v[40:43]
zt a[172:173],v203 offset:0
zl v212,s[96:99],s47 offen offset:0 lds
zm v[40:43],a[132:135],a[212:215],v[40:43]
ze v23,v23
zm v[40:43],a[128:131],a[208:211],v[40:43]
zt a[174:175],v203 offset:4096
zc v107,v22,v23
zm v[48:51],a[128:131],a[224:227],v[72:75]
ze v24,v24
zm v[48:51],a[132:135],a[228:231],v[48:51]
zt a[176:177],v204 offset:0
zm v[48:51],a[136:139],a[232:235],v[48:51]
ze v25,v25
zm v[48:51],a[140:143],a[236:239],v[48:51]
s_add_u32 s47, s47, 0x1000
zt a[178:179],v204 offset:4096
zc v108,v24,v25
zm v[52:55],a[156:159],a[236:239],v[72:75]
s_and_b32 m0, s47, 0xffff
ze v26,v26
zm v[52:55],a[152:155],a[232:235],v[52:55]
zt a[180:181],v205 offset:0
zl v212,s[96:99],s47 offen offset:0 nt lds
zm v[52:55],a[148:151],a[228:231],v[52:55]
ze v27,v27
zm v[52:55],a[144:147],a[224:227],v[52:55]
zt a[182:183],v205 offset:4096
zc v109,v26,v27
zm v[60:63],a[144:147],a[240:243],v[76:79]
ze v28,v28
zm v[60:63],a[148:151],a[244:247],v[60:63]
zt a[184:185],v206 offset:0
zm v[60:63],a[152:155],a[248:251],v[60:63]
s_waitcnt lgkmcnt(5)
ze v29,v29
zm v[60:63],a[156:159],a[252:255],v[60:63]
s_add_u32 s47, s47, 0x1000
zt a[186:187],v206 offset:4096
zc v110,v28,v29
zm v[56:59],a[140:143],a[252:255],v[76:79]
s_and_b32 m0, s47, 0xffff
ze v30,v30
zm v[56:59],a[136:139],a[248:251],v[56:59]
zt a[188:189],v207 offset:0
zl v212,s[96:99],s47 offen offset:0 nt lds
zm v[56:59],a[132:135],a[244:247],v[56:59]
s_add_u32 s47, s47, 0x1000
ze v31,v31
zm v[56:59],a[128:131],a[240:243],v[56:59]
zt a[190:191],v207 offset:4096
zc v111,v30,v31
zm a[0:3],a[160:163],v[96:99],a[0:3]
ze v32,v32
zm a[16:19],a[164:167],v[96:99],a[16:19]
zk a[128:131],v196 offset:16384
zm a[20:23],a[164:167],v[100:103],a[20:23]
ze v33,v33
zm a[4:7],a[160:163],v[100:103],a[4:7]
zk a[132:135],v197 offset:16384
zc v112,v32,v33
zm a[8:11],a[160:163],v[104:107],a[8:11]
ze v34,v34
zm a[24:27],a[164:167],v[104:107],a[24:27]
zk a[136:139],v198 offset:16384
zm a[28:31],a[164:167],v[108:111],a[28:31]
ze v35,v35
zm a[12:15],a[160:163],v[108:111],a[12:15]
zk a[140:143],v199 offset:16384
zc v113,v34,v35
v_smfmac_f32_16x16x64_bf16 v[80:83], v[192:195], v[96:103], v253
ze v36,v36
v_smfmac_f32_16x16x64_bf16 v[84:87], v[192:195], v[104:111], v253
zk a[144:147],v196 offset:20480
zm a[44:47],a[168:171],v[108:111],a[44:47]
ze v37,v37
zm a[60:63],a[172:175],v[108:111],a[60:63]
zk a[148:151],v197 offset:20480
zc v114,v36,v37
zm a[56:59],a[172:175],v[104:107],a[56:59]
s_waitcnt lgkmcnt(6)
ze v38,v38
zm a[40:43],a[168:171],v[104:107],a[40:43]
zk a[152:155],v198 offset:20480
zm a[36:39],a[168:171],v[100:103],a[36:39]
ze v39,v39
zm a[52:55],a[172:175],v[100:103],a[52:55]
s_and_b32 m0, s48, 0xffff
zk a[156:159],v199 offset:20480
zc v115,v38,v39
zm a[48:51],a[172:175],v[96:99],a[48:51]
s_bitset1_b32 m0, 16
ze v40,v40
zm a[32:35],a[168:171],v[96:99],a[32:35]
zl v212,s[36:39],s48 offen offset:0 nt lds
zm a[64:67],a[176:179],v[96:99],a[64:67]
s_add_u32 s48, s48, 0x1000
ze v41,v41
zm a[80:83],a[180:183],v[96:99],a[80:83]
s_and_b32 m0, s48, 0xffff
zc v116,v40,v41
zm a[84:87],a[180:183],v[100:103],a[84:87]
s_bitset1_b32 m0, 16
ze v42,v42
zm a[68:71],a[176:179],v[100:103],a[68:71]
zl v212,s[36:39],s48 offen offset:0 nt lds
zm a[72:75],a[176:179],v[104:107],a[72:75]
s_add_u32 s48, s48, 0x1000
ze v43,v43
zm a[88:91],a[180:183],v[104:107],a[88:91]
s_and_b32 m0, s48, 0xffff
zc v117,v42,v43
zm a[92:95],a[180:183],v[108:111],a[92:95]
s_bitset1_b32 m0, 16
ze v44,v44
zm a[76:79],a[176:179],v[108:111],a[76:79]
zl v212,s[36:39],s48 offen offset:0 nt lds
zm a[108:111],a[184:187],v[108:111],a[108:111]
s_add_u32 s48, s48, 0x1000
ze v45,v45
zm a[124:127],a[188:191],v[108:111],a[124:127]
s_and_b32 m0, s48, 0xffff
zc v118,v44,v45
zm a[120:123],a[188:191],v[104:107],a[120:123]
s_bitset1_b32 m0, 16
ze v46,v46
zm a[104:107],a[184:187],v[104:107],a[104:107]
zl v212,s[36:39],s48 offen offset:0 nt lds
zm a[100:103],a[184:187],v[100:103],a[100:103]
s_add_u32 s48, s48, 0x1000
ze v47,v47
zm a[116:119],a[188:191],v[100:103],a[116:119]
zc v119,v46,v47
zm a[112:115],a[188:191],v[96:99],a[112:115]
s_waitcnt lgkmcnt(0)
zm a[96:99],a[184:187],v[96:99],a[96:99]
s_branch L_A1%=
L_seam%=:
zm v[0:3],a[128:131],v[128:131],0
ze v48,v48
zm v[0:3],a[132:135],v[144:147],v[0:3]
zt a[160:161],v200 offset:57344
zw a192,v128
zm v[0:3],a[136:139],v[160:163],v[0:3]
ze v49,v49
zm v[0:3],a[140:143],v[176:179],v[0:3]
zt a[162:163],v200 offset:61440
zc v120,v48,v49
zm v[4:7],a[156:159],v[176:179],0
ze v50,v50
zm v[4:7],a[152:155],v[160:163],v[4:7]
zt a[164:165],v201 offset:57344
zw a193,v129
zm v[4:7],a[148:151],v[144:147],v[4:7]
ze v51,v51
zm v[4:7],a[144:147],v[128:131],v[4:7]
zt a[166:167],v201 offset:61440
zc v121,v50,v51
zm v[12:15],a[144:147],v[132:135],0
ze v52,v52
zm v[12:15],a[148:151],v[148:151],v[12:15]
zt a[168:169],v202 offset:57344
zw a194,v130
zm v[12:15],a[152:155],v[164:167],v[12:15]
ze v53,v53
zm v[12:15],a[156:159],v[180:183],v[12:15]
zt a[170:171],v202 offset:61440
zc v122,v52,v53
zm v[8:11],a[140:143],v[180:183],0
ze v54,v54
zm v[8:11],a[136:139],v[164:167],v[8:11]
zt a[172:173],v203 offset:57344
zw a195,v131
zm v[8:11],a[132:135],v[148:151],v[8:11]
ze v55,v55
zm v[8:11],a[128:131],v[132:135],v[8:11]
zt a[174:175],v203 offset:61440
zc v123,v54,v55
zm v[16:19],a[128:131],v[136:139],0
ze v56,v56
zm v[16:19],a[132:135],v[152:155],v[16:19]
zt a[176:177],v204 offset:57344
zw a208,v132
zm v[16:19],a[136:139],v[168:171],v[16:19]
ze v57,v57
zm v[16:19],a[140:143],v[184:187],v[16:19]
zt a[178:179],v204 offset:61440
zc v124,v56,v57
zm v[20:23],a[156:159],v[184:187],0
ze v58,v58
zm v[20:23],a[152:155],v[168:171],v[20:23]
zt a[180:181],v205 offset:57344
zw a209,v133
zm v[20:23],a[148:151],v[152:155],v[20:23]
ze v59,v59
zm v[20:23],a[144:147],v[136:139],v[20:23]
zt a[182:183],v205 offset:61440
zc v125,v58,v59
zm v[28:31],a[144:147],v[140:143],0
ze v60,v60
zm v[28:31],a[148:151],v[156:159],v[28:31]
zt a[184:185],v206 offset:57344
zw a210,v134
zm v[28:31],a[152:155],v[172:175],v[28:31]
s_waitcnt lgkmcnt(5)
ze v61,v61
zm v[28:31],a[156:159],v[188:191],v[28:31]
zt a[186:187],v206 offset:61440
zc v126,v60,v61
zm v[24:27],a[140:143],v[188:191],0
ze v62,v62
zm v[24:27],a[136:139],v[172:175],v[24:27]
zt a[188:189],v207 offset:57344
zw a211,v135
zm v[24:27],a[132:135],v[156:159],v[24:27]
ze v63,v63
zm v[24:27],a[128:131],v[140:143],v[24:27]
zt a[190:191],v207 offset:61440
zc v127,v62,v63
zm a[0:3],a[160:163],v[112:115],a[0:3]
zw a224,v136
zw a225,v137
zm a[16:19],a[164:167],v[112:115],a[16:19]
zk a[128:131],v196 offset:8192
zw a226,v138
zm a[20:23],a[164:167],v[116:119],a[20:23]
zw a227,v139
zw a240,v140
zm a[4:7],a[160:163],v[116:119],a[4:7]
zk a[132:135],v197 offset:8192
zw a241,v141
zm a[8:11],a[160:163],v[120:123],a[8:11]
zw a242,v142
zw a243,v143
zm a[24:27],a[164:167],v[120:123],a[24:27]
zk a[136:139],v198 offset:8192
zw a196,v144
zm a[28:31],a[164:167],v[124:127],a[28:31]
zw a197,v145
zw a198,v146
zm a[12:15],a[160:163],v[124:127],a[12:15]
zk a[140:143],v199 offset:8192
zw a199,v147
v_smfmac_f32_16x16x64_bf16 v[80:83], v[192:195], v[112:119], v253
zw a212,v148
zw a213,v149
v_smfmac_f32_16x16x64_bf16 v[84:87], v[192:195], v[120:127], v253
zk a[144:147],v196 offset:12288
zw a214,v150
zm a[44:47],a[168:171],v[124:127],a[44:47]
zw a215,v151
zw a228,v152
zm a[60:63],a[172:175],v[124:127],a[60:63]
zk a[148:151],v197 offset:12288
zw a229,v153
zm a[56:59],a[172:175],v[120:123],a[56:59]
s_waitcnt lgkmcnt(6)
zw a230,v154
zw a231,v155
zm a[40:43],a[168:171],v[120:123],a[40:43]
zk a[152:155],v198 offset:12288
zw a244,v156
zm a[36:39],a[168:171],v[116:119],a[36:39]
zw a245,v157
zw a246,v158
zm a[52:55],a[172:175],v[116:119],a[52:55]
zk a[156:159],v199 offset:12288
zw a247,v159
zm a[48:51],a[172:175],v[112:115],a[48:51]
v_add_f32 v228, v80, v81
v_add_f32 v230, v82, v83
zm a[32:35],a[168:171],v[112:115],a[32:35]
v_add_f32 v232, v84, v85
zm a[64:67],a[176:179],v[112:115],a[64:67]
v_add_f32 v234, v86, v87
v_add_f32 v236, v228, v230
zm a[80:83],a[180:183],v[112:115],a[80:83]
v_add_f32 v237, v232, v234
zm a[84:87],a[180:183],v[116:119],a[84:87]
v_add_f32 v236, v236, v237
v_cmp_nge_f32 s[56:57], s55, v236
zm a[68:71],a[176:179],v[116:119],a[68:71]
v_mov_b32 v238, s64
zm a[72:75],a[176:179],v[120:123],a[72:75]
v_lshlrev_b32 v238, 2, v238
v_add_u32 v238, 0x26000, v238
zm a[88:91],a[180:183],v[120:123],a[88:91]
zw a200,v160
zm a[92:95],a[180:183],v[124:127],a[92:95]
zw a201,v161
zw a202,v162
zm a[76:79],a[176:179],v[124:127],a[76:79]
zw a203,v163
zm a[108:111],a[184:187],v[124:127],a[108:111]
zw a216,v164
zw a217,v165
zm a[124:127],a[188:191],v[124:127],a[124:127]
zw a218,v166
zm a[120:123],a[188:191],v[120:123],a[120:123]
s_or_b32 s71, s56, s57
v_mov_b32 v237, s71
zm a[104:107],a[184:187],v[120:123],a[104:107]
ds_write_b32 v238, v237 offset:0
zm a[100:103],a[184:187],v[116:119],a[100:103]
zw a219,v167
zw a232,v168
zm a[116:119],a[188:191],v[116:119],a[116:119]
zw a233,v169
zm a[112:115],a[188:191],v[112:115],a[112:115]
zw a234,v170
zw a235,v171
s_waitcnt vmcnt(6) lgkmcnt(0)
zm a[96:99],a[184:187],v[112:115],a[96:99]
s_barrier
.p2align 3
zw a248,v172
zw a249,v173
zw a250,v174
zw a251,v175
zw a204,v176
zw a205,v177
zw a206,v178
zw a207,v179
zw a220,v180
zw a221,v181
zw a222,v182
zw a223,v183
zw a236,v184
zw a237,v185
zw a238,v186
zw a239,v187
zw a252,v188
zw a253,v189
zw a254,v190
zw a255,v191
v_mov_b32 v248, 0x26000
zk v[244:247],v248 offset:0
v_max_f32 v228, 0.5, v228
v_max_f32 v230, 0.5, v230
v_max_f32 v232, 0.5, v232
v_max_f32 v234, 0.5, v234
v_rcp_f32 v229, v228
v_rcp_f32 v231, v230
v_rcp_f32 v233, v232
v_rcp_f32 v235, v234
v_rcp_f32 v228, v228
v_rcp_f32 v230, v230
v_rcp_f32 v232, v232
v_rcp_f32 v234, v234
.p2align 3
zr v236,a0
zr v237,a1
zr v238,a2
zr v239,a3
zr v240,a16
zr v241,a17
zr v242,a18
zr v243,a19
s_waitcnt lgkmcnt(0)
v_or3_b32 v244, v244, v245, v246
v_or_b32 v244, v244, v247
zp v[236:237],v[236:237],v[228:229]
zp v[238:239],v[238:239],v[228:229]
zp v[240:241],v[240:241],v[228:229]
zp v[242:243],v[242:243],v[228:229]
v_readfirstlane_b32 s71, v244
zc v128,v236,v237
zc v129,v238,v239
zc v130,v240,v241
zc v131,v242,v243
s_andn2_b32 s71, s71, s76
s_cbranch_scc1 L_fail%=
.p2align 3
zr v236,a4
zr v237,a5
zr v238,a6
zr v239,a7
zr v240,a20
zr v241,a21
zr v242,a22
zr v243,a23
zp v[236:237],v[236:237],v[230:231]
zp v[238:239],v[238:239],v[230:231]
zp v[240:241],v[240:241],v[230:231]
zp v[242:243],v[242:243],v[230:231]
zc v132,v236,v237
zc v133,v238,v239
zc v134,v240,v241
zc v135,v242,v243
.p2align 3
zr v236,a8
zr v237,a9
zr v238,a10
zr v239,a11
zr v240,a24
zr v241,a25
zr v242,a26
zr v243,a27
zp v[236:237],v[236:237],v[232:233]
zp v[238:239],v[238:239],v[232:233]
zp v[240:241],v[240:241],v[232:233]
zp v[242:243],v[242:243],v[232:233]
zc v136,v236,v237
zc v137,v238,v239
zc v138,v240,v241
zc v139,v242,v243
.p2align 3
zr v236,a12
zr v237,a13
zr v238,a14
zr v239,a15
zr v240,a28
zr v241,a29
zr v242,a30
zr v243,a31
zp v[236:237],v[236:237],v[234:235]
zp v[238:239],v[238:239],v[234:235]
zp v[240:241],v[240:241],v[234:235]
zp v[242:243],v[242:243],v[234:235]
zc v140,v236,v237
zc v141,v238,v239
zc v142,v240,v241
zc v143,v242,v243
.p2align 3
zr v236,a32
zr v237,a33
zr v238,a34
zr v239,a35
zr v240,a48
zr v241,a49
zr v242,a50
zr v243,a51
zp v[236:237],v[236:237],v[228:229]
zp v[238:239],v[238:239],v[228:229]
zp v[240:241],v[240:241],v[228:229]
zp v[242:243],v[242:243],v[228:229]
zc v144,v236,v237
zc v145,v238,v239
zc v146,v240,v241
zc v147,v242,v243
.p2align 3
zr v236,a36
zr v237,a37
zr v238,a38
zr v239,a39
zr v240,a52
zr v241,a53
zr v242,a54
zr v243,a55
zp v[236:237],v[236:237],v[230:231]
zp v[238:239],v[238:239],v[230:231]
zp v[240:241],v[240:241],v[230:231]
zp v[242:243],v[242:243],v[230:231]
zc v148,v236,v237
zc v149,v238,v239
zc v150,v240,v241
zc v151,v242,v243
.p2align 3
zr v236,a40
zr v237,a41
zr v238,a42
zr v239,a43
zr v240,a56
zr v241,a57
zr v242,a58
zr v243,a59
zp v[236:237],v[236:237],v[232:233]
zp v[238:239],v[238:239],v[232:233]
zp v[240:241],v[240:241],v[232:233]
zp v[242:243],v[242:243],v[232:233]
zc v152,v236,v237
zc v153,v238,v239
zc v154,v240,v241
zc v155,v242,v243
.p2align 3
zr v236,a44
zr v237,a45
zr v238,a46
zr v239,a47
zr v240,a60
zr v241,a61
zr v242,a62
zr v243,a63
zp v[236:237],v[236:237],v[234:235]
zp v[238:239],v[238:239],v[234:235]
zp v[240:241],v[240:241],v[234:235]
zp v[242:243],v[242:243],v[234:235]
zc v156,v236,v237
zc v157,v238,v239
zc v158,v240,v241
zc v159,v242,v243
.p2align 3
zr v236,a64
zr v237,a65
zr v238,a66
zr v239,a67
zr v240,a80
zr v241,a81
zr v242,a82
zr v243,a83
zp v[236:237],v[236:237],v[228:229]
zp v[238:239],v[238:239],v[228:229]
zp v[240:241],v[240:241],v[228:229]
zp v[242:243],v[242:243],v[228:229]
zc v160,v236,v237
zc v161,v238,v239
zc v162,v240,v241
zc v163,v242,v243
.p2align 3
zr v236,a68
zr v237,a69
zr v238,a70
zr v239,a71
zr v240,a84
zr v241,a85
zr v242,a86
zr v243,a87
zp v[236:237],v[236:237],v[230:231]
zp v[238:239],v[238:239],v[230:231]
zp v[240:241],v[240:241],v[230:231]
zp v[242:243],v[242:243],v[230:231]
zc v164,v236,v237
zc v165,v238,v239
zc v166,v240,v241
zc v167,v242,v243
.p2align 3
zr v236,a72
zr v237,a73
zr v238,a74
zr v239,a75
zr v240,a88
zr v241,a89
zr v242,a90
zr v243,a91
zp v[236:237],v[236:237],v[232:233]
zp v[238:239],v[238:239],v[232:233]
zp v[240:241],v[240:241],v[232:233]
zp v[242:243],v[242:243],v[232:233]
zc v168,v236,v237
zc v169,v238,v239
zc v170,v240,v241
zc v171,v242,v243
.p2align 3
zr v236,a76
zr v237,a77
zr v238,a78
zr v239,a79
zr v240,a92
zr v241,a93
zr v242,a94
zr v243,a95
zp v[236:237],v[236:237],v[234:235]
zp v[238:239],v[238:239],v[234:235]
zp v[240:241],v[240:241],v[234:235]
zp v[242:243],v[242:243],v[234:235]
zc v172,v236,v237
zc v173,v238,v239
zc v174,v240,v241
zc v175,v242,v243
.p2align 3
zr v236,a96
zr v237,a97
zr v238,a98
zr v239,a99
zr v240,a112
zr v241,a113
zr v242,a114
zr v243,a115
zp v[236:237],v[236:237],v[228:229]
zp v[238:239],v[238:239],v[228:229]
zp v[240:241],v[240:241],v[228:229]
zp v[242:243],v[242:243],v[228:229]
zc v176,v236,v237
zc v177,v238,v239
zc v178,v240,v241
zc v179,v242,v243
.p2align 3
zr v236,a100
)"
R"(zr v237,a101
zr v238,a102
zr v239,a103
zr v240,a116
zr v241,a117
zr v242,a118
zr v243,a119
zp v[236:237],v[236:237],v[230:231]
zp v[238:239],v[238:239],v[230:231]
zp v[240:241],v[240:241],v[230:231]
zp v[242:243],v[242:243],v[230:231]
zc v180,v236,v237
zc v181,v238,v239
zc v182,v240,v241
zc v183,v242,v243
.p2align 3
zr v236,a104
zr v237,a105
zr v238,a106
zr v239,a107
zr v240,a120
zr v241,a121
zr v242,a122
zr v243,a123
zp v[236:237],v[236:237],v[232:233]
zp v[238:239],v[238:239],v[232:233]
zp v[240:241],v[240:241],v[232:233]
zp v[242:243],v[242:243],v[232:233]
zc v184,v236,v237
zc v185,v238,v239
zc v186,v240,v241
zc v187,v242,v243
.p2align 3
zr v236,a108
zr v237,a109
zr v238,a110
zr v239,a111
zr v240,a124
zr v241,a125
zr v242,a126
zr v243,a127
zp v[236:237],v[236:237],v[234:235]
zp v[238:239],v[238:239],v[234:235]
zp v[240:241],v[240:241],v[234:235]
zp v[242:243],v[242:243],v[234:235]
zc v188,v236,v237
zc v189,v238,v239
zc v190,v240,v241
zc v191,v242,v243
s_branch L_start%=
L_last%=:
zm v[32:35],a[128:131],a[192:195],v[64:67]
s_mov_b32 s42, s78
ze v16,v16
zm v[32:35],a[132:135],a[196:199],v[32:35]
s_mov_b32 s51, 8
zt a[160:161],v200 offset:0
zm v[32:35],a[136:139],a[200:203],v[32:35]
ze v17,v17
zm v[32:35],a[140:143],a[204:207],v[32:35]
s_and_b32 m0, s48, 0xffff
zt a[162:163],v200 offset:4096
zc v104,v16,v17
zm v[36:39],a[156:159],a[204:207],v[64:67]
s_bitset1_b32 m0, 16
ze v18,v18
zm v[36:39],a[152:155],a[200:203],v[36:39]
zt a[164:165],v201 offset:0
zl v212,s[36:39],s48 offen offset:0 nt lds
zm v[36:39],a[148:151],a[196:199],v[36:39]
ze v19,v19
zm v[36:39],a[144:147],a[192:195],v[36:39]
zt a[166:167],v201 offset:4096
zc v105,v18,v19
zm v[44:47],a[144:147],a[208:211],v[68:71]
ze v20,v20
zm v[44:47],a[148:151],a[212:215],v[44:47]
zt a[168:169],v202 offset:0
zm v[44:47],a[152:155],a[216:219],v[44:47]
s_add_u32 s48, s48, 0x1000
ze v21,v21
zm v[44:47],a[156:159],a[220:223],v[44:47]
s_and_b32 m0, s48, 0xffff
zt a[170:171],v202 offset:4096
zc v106,v20,v21
zm v[40:43],a[140:143],a[220:223],v[68:71]
s_bitset1_b32 m0, 16
ze v22,v22
zm v[40:43],a[136:139],a[216:219],v[40:43]
zt a[172:173],v203 offset:0
zl v212,s[36:39],s48 offen offset:0 nt lds
zm v[40:43],a[132:135],a[212:215],v[40:43]
ze v23,v23
zm v[40:43],a[128:131],a[208:211],v[40:43]
zt a[174:175],v203 offset:4096
zc v107,v22,v23
zm v[48:51],a[128:131],a[224:227],v[72:75]
ze v24,v24
zm v[48:51],a[132:135],a[228:231],v[48:51]
zt a[176:177],v204 offset:0
zm v[48:51],a[136:139],a[232:235],v[48:51]
s_add_u32 s48, s48, 0x1000
ze v25,v25
zm v[48:51],a[140:143],a[236:239],v[48:51]
s_and_b32 m0, s48, 0xffff
zt a[178:179],v204 offset:4096
zc v108,v24,v25
zm v[52:55],a[156:159],a[236:239],v[72:75]
s_bitset1_b32 m0, 16
ze v26,v26
zm v[52:55],a[152:155],a[232:235],v[52:55]
zt a[180:181],v205 offset:0
zl v212,s[36:39],s48 offen offset:0 nt lds
zm v[52:55],a[148:151],a[228:231],v[52:55]
ze v27,v27
zm v[52:55],a[144:147],a[224:227],v[52:55]
zt a[182:183],v205 offset:4096
zc v109,v26,v27
zm v[60:63],a[144:147],a[240:243],v[76:79]
ze v28,v28
zm v[60:63],a[148:151],a[244:247],v[60:63]
zt a[184:185],v206 offset:0
zm v[60:63],a[152:155],a[248:251],v[60:63]
s_add_u32 s48, s48, 0x1000
s_waitcnt lgkmcnt(5)
ze v29,v29
zm v[60:63],a[156:159],a[252:255],v[60:63]
s_and_b32 m0, s48, 0xffff
zt a[186:187],v206 offset:4096
zc v110,v28,v29
zm v[56:59],a[140:143],a[252:255],v[76:79]
s_bitset1_b32 m0, 16
ze v30,v30
zm v[56:59],a[136:139],a[248:251],v[56:59]
zt a[188:189],v207 offset:0
zl v212,s[36:39],s48 offen offset:0 nt lds
zm v[56:59],a[132:135],a[244:247],v[56:59]
s_add_u32 s48, s48, 0x1000
ze v31,v31
zm v[56:59],a[128:131],a[240:243],v[56:59]
zt a[190:191],v207 offset:4096
zc v111,v30,v31
zm a[0:3],a[160:163],v[96:99],a[0:3]
ze v32,v32
zm a[16:19],a[164:167],v[96:99],a[16:19]
zk a[128:131],v196 offset:16384
zm a[20:23],a[164:167],v[100:103],a[20:23]
ze v33,v33
zm a[4:7],a[160:163],v[100:103],a[4:7]
zk a[132:135],v197 offset:16384
zc v112,v32,v33
zm a[8:11],a[160:163],v[104:107],a[8:11]
ze v34,v34
zm a[24:27],a[164:167],v[104:107],a[24:27]
zk a[136:139],v198 offset:16384
zm a[28:31],a[164:167],v[108:111],a[28:31]
ze v35,v35
zm a[12:15],a[160:163],v[108:111],a[12:15]
zk a[140:143],v199 offset:16384
zc v113,v34,v35
v_smfmac_f32_16x16x64_bf16 v[80:83], v[192:195], v[96:103], v253
ze v36,v36
v_smfmac_f32_16x16x64_bf16 v[84:87], v[192:195], v[104:111], v253
zk a[144:147],v196 offset:20480
zm a[44:47],a[168:171],v[108:111],a[44:47]
ze v37,v37
zm a[60:63],a[172:175],v[108:111],a[60:63]
zk a[148:151],v197 offset:20480
zc v114,v36,v37
zm a[56:59],a[172:175],v[104:107],a[56:59]
s_waitcnt lgkmcnt(6)
ze v38,v38
zm a[40:43],a[168:171],v[104:107],a[40:43]
zk a[152:155],v198 offset:20480
zm a[36:39],a[168:171],v[100:103],a[36:39]
ze v39,v39
zm a[52:55],a[172:175],v[100:103],a[52:55]
zk a[156:159],v199 offset:20480
zc v115,v38,v39
zm a[48:51],a[172:175],v[96:99],a[48:51]
ze v40,v40
zm a[32:35],a[168:171],v[96:99],a[32:35]
zm a[64:67],a[176:179],v[96:99],a[64:67]
ze v41,v41
zm a[80:83],a[180:183],v[96:99],a[80:83]
zc v116,v40,v41
zm a[84:87],a[180:183],v[100:103],a[84:87]
ze v42,v42
zm a[68:71],a[176:179],v[100:103],a[68:71]
zm a[72:75],a[176:179],v[104:107],a[72:75]
ze v43,v43
zm a[88:91],a[180:183],v[104:107],a[88:91]
zc v117,v42,v43
zm a[92:95],a[180:183],v[108:111],a[92:95]
ze v44,v44
zm a[76:79],a[176:179],v[108:111],a[76:79]
zm a[108:111],a[184:187],v[108:111],a[108:111]
ze v45,v45
zm a[124:127],a[188:191],v[108:111],a[124:127]
zc v118,v44,v45
zm a[120:123],a[188:191],v[104:107],a[120:123]
ze v46,v46
zm a[104:107],a[184:187],v[104:107],a[104:107]
zm a[100:103],a[184:187],v[100:103],a[100:103]
ze v47,v47
zm a[116:119],a[188:191],v[100:103],a[116:119]
zc v119,v46,v47
zm a[112:115],a[188:191],v[96:99],a[112:115]
s_waitcnt lgkmcnt(0)
zm a[96:99],a[184:187],v[96:99],a[96:99]
zm v[0:3],a[128:131],a[192:195],v[64:67]
ze v48,v48
zm v[0:3],a[132:135],a[196:199],v[0:3]
zt a[160:161],v200 offset:8192
zm v[0:3],a[136:139],a[200:203],v[0:3]
ze v49,v49
zm v[0:3],a[140:143],a[204:207],v[0:3]
zt a[162:163],v200 offset:12288
zc v120,v48,v49
zm v[4:7],a[156:159],a[204:207],v[64:67]
ze v50,v50
zm v[4:7],a[152:155],a[200:203],v[4:7]
zt a[164:165],v201 offset:8192
zm v[4:7],a[148:151],a[196:199],v[4:7]
ze v51,v51
zm v[4:7],a[144:147],a[192:195],v[4:7]
zt a[166:167],v201 offset:12288
zc v121,v50,v51
zm v[12:15],a[144:147],a[208:211],v[68:71]
ze v52,v52
zm v[12:15],a[148:151],a[212:215],v[12:15]
zt a[168:169],v202 offset:8192
zm v[12:15],a[152:155],a[216:219],v[12:15]
ze v53,v53
zm v[12:15],a[156:159],a[220:223],v[12:15]
zt a[170:171],v202 offset:12288
zc v122,v52,v53
zm v[8:11],a[140:143],a[220:223],v[68:71]
ze v54,v54
zm v[8:11],a[136:139],a[216:219],v[8:11]
zt a[172:173],v203 offset:8192
zm v[8:11],a[132:135],a[212:215],v[8:11]
ze v55,v55
zm v[8:11],a[128:131],a[208:211],v[8:11]
zt a[174:175],v203 offset:12288
zc v123,v54,v55
zm v[16:19],a[128:131],a[224:227],v[72:75]
ze v56,v56
zm v[16:19],a[132:135],a[228:231],v[16:19]
zt a[176:177],v204 offset:8192
zm v[16:19],a[136:139],a[232:235],v[16:19]
ze v57,v57
zm v[16:19],a[140:143],a[236:239],v[16:19]
zt a[178:179],v204 offset:12288
zc v124,v56,v57
zm v[20:23],a[156:159],a[236:239],v[72:75]
ze v58,v58
zm v[20:23],a[152:155],a[232:235],v[20:23]
zt a[180:181],v205 offset:8192
zm v[20:23],a[148:151],a[228:231],v[20:23]
ze v59,v59
zm v[20:23],a[144:147],a[224:227],v[20:23]
zt a[182:183],v205 offset:12288
zc v125,v58,v59
zm v[28:31],a[144:147],a[240:243],v[76:79]
ze v60,v60
zm v[28:31],a[148:151],a[244:247],v[28:31]
zt a[184:185],v206 offset:8192
zm v[28:31],a[152:155],a[248:251],v[28:31]
s_waitcnt lgkmcnt(5)
ze v61,v61
zm v[28:31],a[156:159],a[252:255],v[28:31]
zt a[186:187],v206 offset:12288
zc v126,v60,v61
zm v[24:27],a[140:143],a[252:255],v[76:79]
ze v62,v62
zm v[24:27],a[136:139],a[248:251],v[24:27]
zt a[188:189],v207 offset:8192
zm v[24:27],a[132:135],a[244:247],v[24:27]
ze v63,v63
zm v[24:27],a[128:131],a[240:243],v[24:27]
zt a[190:191],v207 offset:12288
zc v127,v62,v63
zm a[0:3],a[160:163],v[112:115],a[0:3]
ze v0,v0
zm a[16:19],a[164:167],v[112:115],a[16:19]
zk a[128:131],v196 offset:24576
zm a[20:23],a[164:167],v[116:119],a[20:23]
ze v1,v1
zm a[4:7],a[160:163],v[116:119],a[4:7]
zk a[132:135],v197 offset:24576
zc v96,v0,v1
zm a[8:11],a[160:163],v[120:123],a[8:11]
ze v2,v2
zm a[24:27],a[164:167],v[120:123],a[24:27]
zk a[136:139],v198 offset:24576
zm a[28:31],a[164:167],v[124:127],a[28:31]
ze v3,v3
zm a[12:15],a[160:163],v[124:127],a[12:15]
zk a[140:143],v199 offset:24576
zc v97,v2,v3
v_smfmac_f32_16x16x64_bf16 v[80:83], v[192:195], v[112:119], v253
ze v4,v4
v_smfmac_f32_16x16x64_bf16 v[84:87], v[192:195], v[120:127], v253
zk a[144:147],v196 offset:28672
zm a[44:47],a[168:171],v[124:127],a[44:47]
ze v5,v5
zm a[60:63],a[172:175],v[124:127],a[60:63]
zk a[148:151],v197 offset:28672
zc v98,v4,v5
zm a[56:59],a[172:175],v[120:123],a[56:59]
s_waitcnt lgkmcnt(6)
ze v6,v6
zm a[40:43],a[168:171],v[120:123],a[40:43]
zk a[152:155],v198 offset:28672
zm a[36:39],a[168:171],v[116:119],a[36:39]
ze v7,v7
zm a[52:55],a[172:175],v[116:119],a[52:55]
zk a[156:159],v199 offset:28672
zc v99,v6,v7
zm a[48:51],a[172:175],v[112:115],a[48:51]
ze v8,v8
zm a[32:35],a[168:171],v[112:115],a[32:35]
zm a[64:67],a[176:179],v[112:115],a[64:67]
ze v9,v9
zm a[80:83],a[180:183],v[112:115],a[80:83]
zc v100,v8,v9
zm a[84:87],a[180:183],v[116:119],a[84:87]
ze v10,v10
zm a[68:71],a[176:179],v[116:119],a[68:71]
zm a[72:75],a[176:179],v[120:123],a[72:75]
ze v11,v11
zm a[88:91],a[180:183],v[120:123],a[88:91]
zc v101,v10,v11
zm a[92:95],a[180:183],v[124:127],a[92:95]
ze v12,v12
zm a[76:79],a[176:179],v[124:127],a[76:79]
zm a[108:111],a[184:187],v[124:127],a[108:111]
ze v13,v13
zm a[124:127],a[188:191],v[124:127],a[124:127]
zc v102,v12,v13
zm a[120:123],a[188:191],v[120:123],a[120:123]
ze v14,v14
zm a[104:107],a[184:187],v[120:123],a[104:107]
zm a[100:103],a[184:187],v[116:119],a[100:103]
ze v15,v15
zm a[116:119],a[188:191],v[116:119],a[116:119]
zc v103,v14,v15
zm a[112:115],a[188:191],v[112:115],a[112:115]
s_waitcnt vmcnt(12) lgkmcnt(0)
zm a[96:99],a[184:187],v[112:115],a[96:99]
s_barrier
zm v[32:35],a[128:131],a[192:195],v[64:67]
ze v16,v16
zm v[32:35],a[132:135],a[196:199],v[32:35]
zt a[160:161],v200 offset:16384
zm v[32:35],a[136:139],a[200:203],v[32:35]
ze v17,v17
zm v[32:35],a[140:143],a[204:207],v[32:35]
zt a[162:163],v200 offset:20480
zc v104,v16,v17
zm v[36:39],a[156:159],a[204:207],v[64:67]
ze v18,v18
zm v[36:39],a[152:155],a[200:203],v[36:39]
zt a[164:165],v201 offset:16384
zm v[36:39],a[148:151],a[196:199],v[36:39]
ze v19,v19
zm v[36:39],a[144:147],a[192:195],v[36:39]
zt a[166:167],v201 offset:20480
zc v105,v18,v19
zm v[44:47],a[144:147],a[208:211],v[68:71]
s_add_u32 s60, s88, 0
ze v20,v20
zm v[44:47],a[148:151],a[212:215],v[44:47]
zt a[168:169],v202 offset:16384
buffer_store_dwordx4 v[128:131], v214, s[40:43], s60 offen offset:0 sc1
zm v[44:47],a[152:155],a[216:219],v[44:47]
ze v21,v21
zm v[44:47],a[156:159],a[220:223],v[44:47]
zt a[170:171],v202 offset:20480
zc v106,v20,v21
zm v[40:43],a[140:143],a[220:223],v[68:71]
ze v22,v22
zm v[40:43],a[136:139],a[216:219],v[40:43]
zt a[172:173],v203 offset:16384
zm v[40:43],a[132:135],a[212:215],v[40:43]
ze v23,v23
zm v[40:43],a[128:131],a[208:211],v[40:43]
zt a[174:175],v203 offset:20480
zc v107,v22,v23
zm v[48:51],a[128:131],a[224:227],v[72:75]
ze v24,v24
zm v[48:51],a[132:135],a[228:231],v[48:51]
zt a[176:177],v204 offset:16384
zm v[48:51],a[136:139],a[232:235],v[48:51]
ze v25,v25
zm v[48:51],a[140:143],a[236:239],v[48:51]
zt a[178:179],v204 offset:20480
zc v108,v24,v25
zm v[52:55],a[156:159],a[236:239],v[72:75]
ze v26,v26
zm v[52:55],a[152:155],a[232:235],v[52:55]
zt a[180:181],v205 offset:16384
zm v[52:55],a[148:151],a[228:231],v[52:55]
ze v27,v27
zm v[52:55],a[144:147],a[224:227],v[52:55]
zt a[182:183],v205 offset:20480
zc v109,v26,v27
zm v[60:63],a[144:147],a[240:243],v[76:79]
ze v28,v28
zm v[60:63],a[148:151],a[244:247],v[60:63]
zt a[184:185],v206 offset:16384
buffer_store_dwordx4 v[132:135], v214, s[40:43], s60 offen offset:1024 sc1
zm v[60:63],a[152:155],a[248:251],v[60:63]
s_waitcnt lgkmcnt(5)
ze v29,v29
zm v[60:63],a[156:159],a[252:255],v[60:63]
zt a[186:187],v206 offset:20480
zc v110,v28,v29
zm v[56:59],a[140:143],a[252:255],v[76:79]
ze v30,v30
zm v[56:59],a[136:139],a[248:251],v[56:59]
zt a[188:189],v207 offset:16384
zm v[56:59],a[132:135],a[244:247],v[56:59]
ze v31,v31
zm v[56:59],a[128:131],a[240:243],v[56:59]
zt a[190:191],v207 offset:20480
zc v111,v30,v31
zm a[0:3],a[160:163],v[96:99],a[0:3]
ze v32,v32
zm a[16:19],a[164:167],v[96:99],a[16:19]
zk a[128:131],v196 offset:32768
zm a[20:23],a[164:167],v[100:103],a[20:23]
ze v33,v33
zm a[4:7],a[160:163],v[100:103],a[4:7]
zk a[132:135],v197 offset:32768
zc v112,v32,v33
zm a[8:11],a[160:163],v[104:107],a[8:11]
ze v34,v34
zm a[24:27],a[164:167],v[104:107],a[24:27]
zk a[136:139],v198 offset:32768
zm a[28:31],a[164:167],v[108:111],a[28:31]
ze v35,v35
zm a[12:15],a[160:163],v[108:111],a[12:15]
zk a[140:143],v199 offset:32768
zc v113,v34,v35
v_smfmac_f32_16x16x64_bf16 v[80:83], v[192:195], v[96:103], v253
ze v36,v36
v_smfmac_f32_16x16x64_bf16 v[84:87], v[192:195], v[104:111], v253
zk a[144:147],v196 offset:36864
zm a[44:47],a[168:171],v[108:111],a[44:47]
ze v37,v37
zm a[60:63],a[172:175],v[108:111],a[60:63]
zk a[148:151],v197 offset:36864
zc v114,v36,v37
zm a[56:59],a[172:175],v[104:107],a[56:59]
s_waitcnt lgkmcnt(6)
ze v38,v38
zm a[40:43],a[168:171],v[104:107],a[40:43]
zk a[152:155],v198 offset:36864
zm a[36:39],a[168:171],v[100:103],a[36:39]
ze v39,v39
zm a[52:55],a[172:175],v[100:103],a[52:55]
zk a[156:159],v199 offset:36864
zc v115,v38,v39
zm a[48:51],a[172:175],v[96:99],a[48:51]
ze v40,v40
zm a[32:35],a[168:171],v[96:99],a[32:35]
zm a[64:67],a[176:179],v[96:99],a[64:67]
ze v41,v41
zm a[80:83],a[180:183],v[96:99],a[80:83]
zc v116,v40,v41
zm a[84:87],a[180:183],v[100:103],a[84:87]
ze v42,v42
zm a[68:71],a[176:179],v[100:103],a[68:71]
zm a[72:75],a[176:179],v[104:107],a[72:75]
ze v43,v43
zm a[88:91],a[180:183],v[104:107],a[88:91]
zc v117,v42,v43
zm a[92:95],a[180:183],v[108:111],a[92:95]
ze v44,v44
zm a[76:79],a[176:179],v[108:111],a[76:79]
zm a[108:111],a[184:187],v[108:111],a[108:111]
ze v45,v45
zm a[124:127],a[188:191],v[108:111],a[124:127]
zc v118,v44,v45
zm a[120:123],a[188:191],v[104:107],a[120:123]
ze v46,v46
zm a[104:107],a[184:187],v[104:107],a[104:107]
zm a[100:103],a[184:187],v[100:103],a[100:103]
ze v47,v47
zm a[116:119],a[188:191],v[100:103],a[116:119]
zc v119,v46,v47
zm a[112:115],a[188:191],v[96:99],a[112:115]
s_waitcnt lgkmcnt(0)
zm a[96:99],a[184:187],v[96:99],a[96:99]
buffer_store_dwordx4 v[136:139], v214, s[40:43], s60 offen offset:2048 sc1
zm v[0:3],a[128:131],a[192:195],v[64:67]
ze v48,v48
zm v[0:3],a[132:135],a[196:199],v[0:3]
zt a[160:161],v200 offset:24576
buffer_store_dwordx4 v[140:143], v214, s[40:43], s60 offen offset:3072 sc1
zm v[0:3],a[136:139],a[200:203],v[0:3]
ze v49,v49
zm v[0:3],a[140:143],a[204:207],v[0:3]
zt a[162:163],v200 offset:28672
zc v120,v48,v49
zm v[4:7],a[156:159],a[204:207],v[64:67]
ze v50,v50
zm v[4:7],a[152:155],a[200:203],v[4:7]
zt a[164:165],v201 offset:24576
zm v[4:7],a[148:151],a[196:199],v[4:7]
ze v51,v51
zm v[4:7],a[144:147],a[192:195],v[4:7]
zt a[166:167],v201 offset:28672
zc v121,v50,v51
zm v[12:15],a[144:147],a[208:211],v[68:71]
ze v52,v52
zm v[12:15],a[148:151],a[212:215],v[12:15]
zt a[168:169],v202 offset:24576
zm v[12:15],a[152:155],a[216:219],v[12:15]
ze v53,v53
zm v[12:15],a[156:159],a[220:223],v[12:15]
zt a[170:171],v202 offset:28672
zc v122,v52,v53
zm v[8:11],a[140:143],a[220:223],v[68:71]
ze v54,v54
zm v[8:11],a[136:139],a[216:219],v[8:11]
zt a[172:173],v203 offset:24576
zm v[8:11],a[132:135],a[212:215],v[8:11]
ze v55,v55
zm v[8:11],a[128:131],a[208:211],v[8:11]
zt a[174:175],v203 offset:28672
zc v123,v54,v55
zm v[16:19],a[128:131],a[224:227],v[72:75]
s_add_u32 s60, s88, 0x1000
ze v56,v56
zm v[16:19],a[132:135],a[228:231],v[16:19]
zt a[176:177],v204 offset:24576
buffer_store_dwordx4 v[144:147], v214, s[40:43], s60 offen offset:0 sc1
zm v[16:19],a[136:139],a[232:235],v[16:19]
ze v57,v57
zm v[16:19],a[140:143],a[236:239],v[16:19]
zt a[178:179],v204 offset:28672
zc v124,v56,v57
zm v[20:23],a[156:159],a[236:239],v[72:75]
ze v58,v58
zm v[20:23],a[152:155],a[232:235],v[20:23]
zt a[180:181],v205 offset:24576
zm v[20:23],a[148:151],a[228:231],v[20:23]
ze v59,v59
zm v[20:23],a[144:147],a[224:227],v[20:23]
zt a[182:183],v205 offset:28672
zc v125,v58,v59
zm v[28:31],a[144:147],a[240:243],v[76:79]
ze v60,v60
zm v[28:31],a[148:151],a[244:247],v[28:31]
zt a[184:185],v206 offset:24576
buffer_store_dwordx4 v[148:151], v214, s[40:43], s60 offen offset:1024 sc1
zm v[28:31],a[152:155],a[248:251],v[28:31]
s_waitcnt lgkmcnt(5)
ze v61,v61
zm v[28:31],a[156:159],a[252:255],v[28:31]
zt a[186:187],v206 offset:28672
zc v126,v60,v61
zm v[24:27],a[140:143],a[252:255],v[76:79]
ze v62,v62
zm v[24:27],a[136:139],a[248:251],v[24:27]
zt a[188:189],v207 offset:24576
zm v[24:27],a[132:135],a[244:247],v[24:27]
ze v63,v63
zm v[24:27],a[128:131],a[240:243],v[24:27]
zt a[190:191],v207 offset:28672
zc v127,v62,v63
zm a[0:3],a[160:163],v[112:115],a[0:3]
ze v0,v0
zm a[16:19],a[164:167],v[112:115],a[16:19]
zk a[128:131],v196 offset:40960
zm a[20:23],a[164:167],v[116:119],a[20:23]
ze v1,v1
zm a[4:7],a[160:163],v[116:119],a[4:7]
zk a[132:135],v197 offset:40960
zc v96,v0,v1
zm a[8:11],a[160:163],v[120:123],a[8:11]
ze v2,v2
zm a[24:27],a[164:167],v[120:123],a[24:27]
zk a[136:139],v198 offset:40960
zm a[28:31],a[164:167],v[124:127],a[28:31]
ze v3,v3
zm a[12:15],a[160:163],v[124:127],a[12:15]
zk a[140:143],v199 offset:40960
zc v97,v2,v3
v_smfmac_f32_16x16x64_bf16 v[80:83], v[192:195], v[112:119], v253
ze v4,v4
v_smfmac_f32_16x16x64_bf16 v[84:87], v[192:195], v[120:127], v253
zk a[144:147],v196 offset:45056
zm a[44:47],a[168:171],v[124:127],a[44:47]
ze v5,v5
zm a[60:63],a[172:175],v[124:127],a[60:63]
zk a[148:151],v197 offset:45056
zc v98,v4,v5
zm a[56:59],a[172:175],v[120:123],a[56:59]
s_waitcnt lgkmcnt(6)
ze v6,v6
zm a[40:43],a[168:171],v[120:123],a[40:43]
zk a[152:155],v198 offset:45056
zm a[36:39],a[168:171],v[116:119],a[36:39]
ze v7,v7
zm a[52:55],a[172:175],v[116:119],a[52:55]
zk a[156:159],v199 offset:45056
zc v99,v6,v7
zm a[48:51],a[172:175],v[112:115],a[48:51]
ze v8,v8
zm a[32:35],a[168:171],v[112:115],a[32:35]
buffer_store_dwordx4 v[152:155], v214, s[40:43], s60 offen offset:2048 sc1
zm a[64:67],a[176:179],v[112:115],a[64:67]
ze v9,v9
zm a[80:83],a[180:183],v[112:115],a[80:83]
zc v100,v8,v9
zm a[84:87],a[180:183],v[116:119],a[84:87]
ze v10,v10
zm a[68:71],a[176:179],v[116:119],a[68:71]
zm a[72:75],a[176:179],v[120:123],a[72:75]
ze v11,v11
zm a[88:91],a[180:183],v[120:123],a[88:91]
zc v101,v10,v11
zm a[92:95],a[180:183],v[124:127],a[92:95]
ze v12,v12
zm a[76:79],a[176:179],v[124:127],a[76:79]
zm a[108:111],a[184:187],v[124:127],a[108:111]
ze v13,v13
zm a[124:127],a[188:191],v[124:127],a[124:127]
zc v102,v12,v13
zm a[120:123],a[188:191],v[120:123],a[120:123]
ze v14,v14
zm a[104:107],a[184:187],v[120:123],a[104:107]
buffer_store_dwordx4 v[156:159], v214, s[40:43], s60 offen offset:3072 sc1
zm a[100:103],a[184:187],v[116:119],a[100:103]
ze v15,v15
zm a[116:119],a[188:191],v[116:119],a[116:119]
zc v103,v14,v15
zm a[112:115],a[188:191],v[112:115],a[112:115]
s_waitcnt vmcnt(12) lgkmcnt(0)
zm a[96:99],a[184:187],v[112:115],a[96:99]
s_barrier
zm v[32:35],a[128:131],a[192:195],v[64:67]
ze v16,v16
zm v[32:35],a[132:135],a[196:199],v[32:35]
zt a[160:161],v200 offset:32768
zm v[32:35],a[136:139],a[200:203],v[32:35]
ze v17,v17
zm v[32:35],a[140:143],a[204:207],v[32:35]
s_add_u32 s50, s84, 0
zt a[162:163],v200 offset:36864
zc v104,v16,v17
zm v[36:39],a[156:159],a[204:207],v[64:67]
s_add_u32 m0, s44, 0
ze v18,v18
zm v[36:39],a[152:155],a[200:203],v[36:39]
zt a[164:165],v201 offset:32768
zl v208,s[24:27],s50 offen offset:0 lds
zm v[36:39],a[148:151],a[196:199],v[36:39]
ze v19,v19
zm v[36:39],a[144:147],a[192:195],v[36:39]
zt a[166:167],v201 offset:36864
zc v105,v18,v19
zm v[44:47],a[144:147],a[208:211],v[68:71]
s_add_u32 s60, s88, 0x2000
ze v20,v20
zm v[44:47],a[148:151],a[212:215],v[44:47]
zt a[168:169],v202 offset:32768
buffer_store_dwordx4 v[160:163], v214, s[40:43], s60 offen offset:0 sc1
zm v[44:47],a[152:155],a[216:219],v[44:47]
ze v21,v21
zm v[44:47],a[156:159],a[220:223],v[44:47]
zt a[170:171],v202 offset:36864
zc v106,v20,v21
zm v[40:43],a[140:143],a[220:223],v[68:71]
ze v22,v22
zm v[40:43],a[136:139],a[216:219],v[40:43]
zt a[172:173],v203 offset:32768
zl v209,s[24:27],s50 offen offset:1024 lds
zm v[40:43],a[132:135],a[212:215],v[40:43]
ze v23,v23
zm v[40:43],a[128:131],a[208:211],v[40:43]
zt a[174:175],v203 offset:36864
zc v107,v22,v23
zm v[48:51],a[128:131],a[224:227],v[72:75]
ze v24,v24
zm v[48:51],a[132:135],a[228:231],v[48:51]
zt a[176:177],v204 offset:32768
zm v[48:51],a[136:139],a[232:235],v[48:51]
ze v25,v25
zm v[48:51],a[140:143],a[236:239],v[48:51]
zt a[178:179],v204 offset:36864
zc v108,v24,v25
zm v[52:55],a[156:159],a[236:239],v[72:75]
ze v26,v26
zm v[52:55],a[152:155],a[232:235],v[52:55]
zt a[180:181],v205 offset:32768
zl v210,s[24:27],s50 offen offset:2048 lds
zm v[52:55],a[148:151],a[228:231],v[52:55]
ze v27,v27
zm v[52:55],a[144:147],a[224:227],v[52:55]
zt a[182:183],v205 offset:36864
zc v109,v26,v27
zm v[60:63],a[144:147],a[240:243],v[76:79]
ze v28,v28
zm v[60:63],a[148:151],a[244:247],v[60:63]
zt a[184:185],v206 offset:32768
buffer_store_dwordx4 v[164:167], v214, s[40:43], s60 offen offset:1024 sc1
zm v[60:63],a[152:155],a[248:251],v[60:63]
s_waitcnt lgkmcnt(5)
ze v29,v29
zm v[60:63],a[156:159],a[252:255],v[60:63]
zt a[186:187],v206 offset:36864
zc v110,v28,v29
zm v[56:59],a[140:143],a[252:255],v[76:79]
ze v30,v30
zm v[56:59],a[136:139],a[248:251],v[56:59]
zt a[188:189],v207 offset:32768
zl v211,s[24:27],s50 offen offset:3072 lds
zm v[56:59],a[132:135],a[244:247],v[56:59]
ze v31,v31
zm v[56:59],a[128:131],a[240:243],v[56:59]
zt a[190:191],v207 offset:36864
zc v111,v30,v31
zm a[0:3],a[160:163],v[96:99],a[0:3]
ze v32,v32
zm a[16:19],a[164:167],v[96:99],a[16:19]
zk a[128:131],v196 offset:49152
zm a[20:23],a[164:167],v[100:103],a[20:23]
ze v33,v33
zm a[4:7],a[160:163],v[100:103],a[4:7]
zk a[132:135],v197 offset:49152
zc v112,v32,v33
zm a[8:11],a[160:163],v[104:107],a[8:11]
ze v34,v34
zm a[24:27],a[164:167],v[104:107],a[24:27]
zk a[136:139],v198 offset:49152
zm a[28:31],a[164:167],v[108:111],a[28:31]
ze v35,v35
zm a[12:15],a[160:163],v[108:111],a[12:15]
zk a[140:143],v199 offset:49152
zc v113,v34,v35
v_smfmac_f32_16x16x64_bf16 v[80:83], v[192:195], v[96:103], v253
ze v36,v36
v_smfmac_f32_16x16x64_bf16 v[84:87], v[192:195], v[104:111], v253
zk a[144:147],v196 offset:53248
zm a[44:47],a[168:171],v[108:111],a[44:47]
ze v37,v37
zm a[60:63],a[172:175],v[108:111],a[60:63]
zk a[148:151],v197 offset:53248
zc v114,v36,v37
zm a[56:59],a[172:175],v[104:107],a[56:59]
s_waitcnt lgkmcnt(6)
ze v38,v38
zm a[40:43],a[168:171],v[104:107],a[40:43]
zk a[152:155],v198 offset:53248
zm a[36:39],a[168:171],v[100:103],a[36:39]
ze v39,v39
zm a[52:55],a[172:175],v[100:103],a[52:55]
zk a[156:159],v199 offset:53248
zc v115,v38,v39
zm a[48:51],a[172:175],v[96:99],a[48:51]
ze v40,v40
zm a[32:35],a[168:171],v[96:99],a[32:35]
zm a[64:67],a[176:179],v[96:99],a[64:67]
ze v41,v41
zm a[80:83],a[180:183],v[96:99],a[80:83]
zc v116,v40,v41
zm a[84:87],a[180:183],v[100:103],a[84:87]
ze v42,v42
zm a[68:71],a[176:179],v[100:103],a[68:71]
zm a[72:75],a[176:179],v[104:107],a[72:75]
ze v43,v43
zm a[88:91],a[180:183],v[104:107],a[88:91]
zc v117,v42,v43
zm a[92:95],a[180:183],v[108:111],a[92:95]
ze v44,v44
zm a[76:79],a[176:179],v[108:111],a[76:79]
zm a[108:111],a[184:187],v[108:111],a[108:111]
ze v45,v45
zm a[124:127],a[188:191],v[108:111],a[124:127]
zc v118,v44,v45
zm a[120:123],a[188:191],v[104:107],a[120:123]
ze v46,v46
zm a[104:107],a[184:187],v[104:107],a[104:107]
zm a[100:103],a[184:187],v[100:103],a[100:103]
ze v47,v47
zm a[116:119],a[188:191],v[100:103],a[116:119]
zc v119,v46,v47
zm a[112:115],a[188:191],v[96:99],a[112:115]
s_waitcnt lgkmcnt(0)
zm a[96:99],a[184:187],v[96:99],a[96:99]
buffer_store_dwordx4 v[168:171], v214, s[40:43], s60 offen offset:2048 sc1
zm v[0:3],a[128:131],a[192:195],v[64:67]
ze v48,v48
zm v[0:3],a[132:135],a[196:199],v[0:3]
zt a[160:161],v200 offset:40960
buffer_store_dwordx4 v[172:175], v214, s[40:43], s60 offen offset:3072 sc1
zm v[0:3],a[136:139],a[200:203],v[0:3]
ze v49,v49
zm v[0:3],a[140:143],a[204:207],v[0:3]
s_add_u32 s50, s84, 0x10000
zt a[162:163],v200 offset:45056
zc v120,v48,v49
zm v[4:7],a[156:159],a[204:207],v[64:67]
s_add_u32 m0, s44, 0x4000
ze v50,v50
zm v[4:7],a[152:155],a[200:203],v[4:7]
zt a[164:165],v201 offset:40960
zl v208,s[24:27],s50 offen offset:0 lds
zm v[4:7],a[148:151],a[196:199],v[4:7]
ze v51,v51
zm v[4:7],a[144:147],a[192:195],v[4:7]
zt a[166:167],v201 offset:45056
zc v121,v50,v51
zm v[12:15],a[144:147],a[208:211],v[68:71]
ze v52,v52
zm v[12:15],a[148:151],a[212:215],v[12:15]
zt a[168:169],v202 offset:40960
zm v[12:15],a[152:155],a[216:219],v[12:15]
ze v53,v53
zm v[12:15],a[156:159],a[220:223],v[12:15]
zt a[170:171],v202 offset:45056
zc v122,v52,v53
zm v[8:11],a[140:143],a[220:223],v[68:71]
ze v54,v54
zm v[8:11],a[136:139],a[216:219],v[8:11]
zt a[172:173],v203 offset:40960
zl v209,s[24:27],s50 offen offset:1024 lds
zm v[8:11],a[132:135],a[212:215],v[8:11]
ze v55,v55
zm v[8:11],a[128:131],a[208:211],v[8:11]
zt a[174:175],v203 offset:45056
zc v123,v54,v55
zm v[16:19],a[128:131],a[224:227],v[72:75]
s_add_u32 s60, s88, 0x3000
ze v56,v56
zm v[16:19],a[132:135],a[228:231],v[16:19]
zt a[176:177],v204 offset:40960
buffer_store_dwordx4 v[176:179], v214, s[40:43], s60 offen offset:0 sc1
zm v[16:19],a[136:139],a[232:235],v[16:19]
ze v57,v57
zm v[16:19],a[140:143],a[236:239],v[16:19]
zt a[178:179],v204 offset:45056
zc v124,v56,v57
zm v[20:23],a[156:159],a[236:239],v[72:75]
ze v58,v58
zm v[20:23],a[152:155],a[232:235],v[20:23]
zt a[180:181],v205 offset:40960
zl v210,s[24:27],s50 offen offset:2048 lds
zm v[20:23],a[148:151],a[228:231],v[20:23]
ze v59,v59
zm v[20:23],a[144:147],a[224:227],v[20:23]
zt a[182:183],v205 offset:45056
zc v125,v58,v59
zm v[28:31],a[144:147],a[240:243],v[76:79]
ze v60,v60
zm v[28:31],a[148:151],a[244:247],v[28:31]
zt a[184:185],v206 offset:40960
buffer_store_dwordx4 v[180:183], v214, s[40:43], s60 offen offset:1024 sc1
zm v[28:31],a[152:155],a[248:251],v[28:31]
s_waitcnt lgkmcnt(5)
ze v61,v61
zm v[28:31],a[156:159],a[252:255],v[28:31]
zt a[186:187],v206 offset:45056
zc v126,v60,v61
zm v[24:27],a[140:143],a[252:255],v[76:79]
ze v62,v62
zm v[24:27],a[136:139],a[248:251],v[24:27]
zt a[188:189],v207 offset:40960
zl v211,s[24:27],s50 offen offset:3072 lds
zm v[24:27],a[132:135],a[244:247],v[24:27]
ze v63,v63
zm v[24:27],a[128:131],a[240:243],v[24:27]
zt a[190:191],v207 offset:45056
zc v127,v62,v63
zm a[0:3],a[160:163],v[112:115],a[0:3]
ze v0,v0
zm a[16:19],a[164:167],v[112:115],a[16:19]
zk a[128:131],v196 offset:57344
zm a[20:23],a[164:167],v[116:119],a[20:23]
ze v1,v1
zm a[4:7],a[160:163],v[116:119],a[4:7]
zk a[132:135],v197 offset:57344
zc v96,v0,v1
zm a[8:11],a[160:163],v[120:123],a[8:11]
ze v2,v2
zm a[24:27],a[164:167],v[120:123],a[24:27]
zk a[136:139],v198 offset:57344
zm a[28:31],a[164:167],v[124:127],a[28:31]
ze v3,v3
zm a[12:15],a[160:163],v[124:127],a[12:15]
zk a[140:143],v199 offset:57344
zc v97,v2,v3
v_smfmac_f32_16x16x64_bf16 v[80:83], v[192:195], v[112:119], v253
ze v4,v4
v_smfmac_f32_16x16x64_bf16 v[84:87], v[192:195], v[120:127], v253
zk a[144:147],v196 offset:61440
zm a[44:47],a[168:171],v[124:127],a[44:47]
ze v5,v5
zm a[60:63],a[172:175],v[124:127],a[60:63]
zk a[148:151],v197 offset:61440
zc v98,v4,v5
zm a[56:59],a[172:175],v[120:123],a[56:59]
s_waitcnt lgkmcnt(6)
ze v6,v6
zm a[40:43],a[168:171],v[120:123],a[40:43]
zk a[152:155],v198 offset:61440
zm a[36:39],a[168:171],v[116:119],a[36:39]
ze v7,v7
zm a[52:55],a[172:175],v[116:119],a[52:55]
zk a[156:159],v199 offset:61440
zc v99,v6,v7
zm a[48:51],a[172:175],v[112:115],a[48:51]
ze v8,v8
zm a[32:35],a[168:171],v[112:115],a[32:35]
buffer_store_dwordx4 v[184:187], v214, s[40:43], s60 offen offset:2048 sc1
zm a[64:67],a[176:179],v[112:115],a[64:67]
ze v9,v9
zm a[80:83],a[180:183],v[112:115],a[80:83]
zc v100,v8,v9
zm a[84:87],a[180:183],v[116:119],a[84:87]
ze v10,v10
zm a[68:71],a[176:179],v[116:119],a[68:71]
zm a[72:75],a[176:179],v[120:123],a[72:75]
ze v11,v11
zm a[88:91],a[180:183],v[120:123],a[88:91]
zc v101,v10,v11
zm a[92:95],a[180:183],v[124:127],a[92:95]
ze v12,v12
zm a[76:79],a[176:179],v[124:127],a[76:79]
zm a[108:111],a[184:187],v[124:127],a[108:111]
ze v13,v13
zm a[124:127],a[188:191],v[124:127],a[124:127]
zc v102,v12,v13
zm a[120:123],a[188:191],v[120:123],a[120:123]
ze v14,v14
zm a[104:107],a[184:187],v[120:123],a[104:107]
buffer_store_dwordx4 v[188:191], v214, s[40:43], s60 offen offset:3072 sc1
zm a[100:103],a[184:187],v[116:119],a[100:103]
ze v15,v15
zm a[116:119],a[188:191],v[116:119],a[116:119]
zc v103,v14,v15
zm a[112:115],a[188:191],v[112:115],a[112:115]
s_waitcnt vmcnt(24) lgkmcnt(0)
zm a[96:99],a[184:187],v[112:115],a[96:99]
s_barrier
zm v[32:35],a[128:131],a[192:195],v[64:67]
ze v16,v16
zm v[32:35],a[132:135],a[196:199],v[32:35]
zt a[160:161],v200 offset:49152
zm v[32:35],a[136:139],a[200:203],v[32:35]
ze v17,v17
zm v[32:35],a[140:143],a[204:207],v[32:35]
s_add_u32 s50, s84, 0x20000
zt a[162:163],v200 offset:53248
zc v104,v16,v17
zm v[36:39],a[156:159],a[204:207],v[64:67]
s_add_u32 m0, s44, 0x8000
ze v18,v18
zm v[36:39],a[152:155],a[200:203],v[36:39]
zt a[164:165],v201 offset:49152
zl v208,s[24:27],s50 offen offset:0 lds
zm v[36:39],a[148:151],a[196:199],v[36:39]
ze v19,v19
zm v[36:39],a[144:147],a[192:195],v[36:39]
zt a[166:167],v201 offset:53248
zc v105,v18,v19
zm v[44:47],a[144:147],a[208:211],v[68:71]
ze v20,v20
zm v[44:47],a[148:151],a[212:215],v[44:47]
zt a[168:169],v202 offset:49152
global_load_dword v254, v255, s[52:53]
zm v[44:47],a[152:155],a[216:219],v[44:47]
ze v21,v21
zm v[44:47],a[156:159],a[220:223],v[44:47]
zt a[170:171],v202 offset:53248
zc v106,v20,v21
zm v[40:43],a[140:143],a[220:223],v[68:71]
ze v22,v22
zm v[40:43],a[136:139],a[216:219],v[40:43]
zt a[172:173],v203 offset:49152
zl v209,s[24:27],s50 offen offset:1024 lds
zm v[40:43],a[132:135],a[212:215],v[40:43]
ze v23,v23
zm v[40:43],a[128:131],a[208:211],v[40:43]
zt a[174:175],v203 offset:53248
zc v107,v22,v23
zm v[48:51],a[128:131],a[224:227],v[72:75]
ze v24,v24
zm v[48:51],a[132:135],a[228:231],v[48:51]
zt a[176:177],v204 offset:49152
zm v[48:51],a[136:139],a[232:235],v[48:51]
ze v25,v25
zm v[48:51],a[140:143],a[236:239],v[48:51]
zt a[178:179],v204 offset:53248
zc v108,v24,v25
)"
R"(zm v[52:55],a[156:159],a[236:239],v[72:75]
ze v26,v26
zm v[52:55],a[152:155],a[232:235],v[52:55]
zt a[180:181],v205 offset:49152
zl v210,s[24:27],s50 offen offset:2048 lds
zm v[52:55],a[148:151],a[228:231],v[52:55]
ze v27,v27
zm v[52:55],a[144:147],a[224:227],v[52:55]
zt a[182:183],v205 offset:53248
zc v109,v26,v27
zm v[60:63],a[144:147],a[240:243],v[76:79]
ze v28,v28
zm v[60:63],a[148:151],a[244:247],v[60:63]
zt a[184:185],v206 offset:49152
zm v[60:63],a[152:155],a[248:251],v[60:63]
s_waitcnt lgkmcnt(5)
ze v29,v29
zm v[60:63],a[156:159],a[252:255],v[60:63]
zt a[186:187],v206 offset:53248
zc v110,v28,v29
zm v[56:59],a[140:143],a[252:255],v[76:79]
ze v30,v30
zm v[56:59],a[136:139],a[248:251],v[56:59]
zt a[188:189],v207 offset:49152
zl v211,s[24:27],s50 offen offset:3072 lds
zm v[56:59],a[132:135],a[244:247],v[56:59]
ze v31,v31
zm v[56:59],a[128:131],a[240:243],v[56:59]
zt a[190:191],v207 offset:53248
zc v111,v30,v31
zm a[0:3],a[160:163],v[96:99],a[0:3]
ze v32,v32
zm a[16:19],a[164:167],v[96:99],a[16:19]
zm a[20:23],a[164:167],v[100:103],a[20:23]
ze v33,v33
zm a[4:7],a[160:163],v[100:103],a[4:7]
zc v112,v32,v33
zm a[8:11],a[160:163],v[104:107],a[8:11]
ze v34,v34
zm a[24:27],a[164:167],v[104:107],a[24:27]
zm a[28:31],a[164:167],v[108:111],a[28:31]
ze v35,v35
zm a[12:15],a[160:163],v[108:111],a[12:15]
zc v113,v34,v35
v_smfmac_f32_16x16x64_bf16 v[80:83], v[192:195], v[96:103], v253
ze v36,v36
v_smfmac_f32_16x16x64_bf16 v[84:87], v[192:195], v[104:111], v253
zm a[44:47],a[168:171],v[108:111],a[44:47]
ze v37,v37
zm a[60:63],a[172:175],v[108:111],a[60:63]
zc v114,v36,v37
zm a[56:59],a[172:175],v[104:107],a[56:59]
s_waitcnt lgkmcnt(0)
ze v38,v38
zm a[40:43],a[168:171],v[104:107],a[40:43]
zm a[36:39],a[168:171],v[100:103],a[36:39]
ze v39,v39
zm a[52:55],a[172:175],v[100:103],a[52:55]
s_add_u32 s50, s84, 0x30000
zc v115,v38,v39
zm a[48:51],a[172:175],v[96:99],a[48:51]
s_add_u32 m0, s44, 0xc000
ze v40,v40
zm a[32:35],a[168:171],v[96:99],a[32:35]
zl v208,s[24:27],s50 offen offset:0 lds
zm a[64:67],a[176:179],v[96:99],a[64:67]
ze v41,v41
zm a[80:83],a[180:183],v[96:99],a[80:83]
zc v116,v40,v41
zm a[84:87],a[180:183],v[100:103],a[84:87]
ze v42,v42
zm a[68:71],a[176:179],v[100:103],a[68:71]
zl v209,s[24:27],s50 offen offset:1024 lds
zm a[72:75],a[176:179],v[104:107],a[72:75]
ze v43,v43
zm a[88:91],a[180:183],v[104:107],a[88:91]
zc v117,v42,v43
zm a[92:95],a[180:183],v[108:111],a[92:95]
ze v44,v44
zm a[76:79],a[176:179],v[108:111],a[76:79]
zl v210,s[24:27],s50 offen offset:2048 lds
zm a[108:111],a[184:187],v[108:111],a[108:111]
ze v45,v45
zm a[124:127],a[188:191],v[108:111],a[124:127]
zc v118,v44,v45
zm a[120:123],a[188:191],v[104:107],a[120:123]
ze v46,v46
zm a[104:107],a[184:187],v[104:107],a[104:107]
zl v211,s[24:27],s50 offen offset:3072 lds
zm a[100:103],a[184:187],v[100:103],a[100:103]
ze v47,v47
zm a[116:119],a[188:191],v[100:103],a[116:119]
zc v119,v46,v47
zm a[112:115],a[188:191],v[96:99],a[112:115]
s_waitcnt vmcnt(9) lgkmcnt(0)
zm a[96:99],a[184:187],v[96:99],a[96:99]
s_mov_b64 exec, 1
buffer_store_dwordx2 v[216:217], off, s[92:95], s77 offset:0 sc1
s_mov_b64 exec, -1
ze v48,v48
zt a[160:161],v200 offset:57344
ze v49,v49
zt a[162:163],v200 offset:61440
zc v120,v48,v49
ze v50,v50
zt a[164:165],v201 offset:57344
ze v51,v51
zt a[166:167],v201 offset:61440
zc v121,v50,v51
ze v52,v52
zt a[168:169],v202 offset:57344
ze v53,v53
zt a[170:171],v202 offset:61440
zc v122,v52,v53
ze v54,v54
zt a[172:173],v203 offset:57344
ze v55,v55
zt a[174:175],v203 offset:61440
zc v123,v54,v55
ze v56,v56
zt a[176:177],v204 offset:57344
ze v57,v57
zt a[178:179],v204 offset:61440
zc v124,v56,v57
ze v58,v58
zt a[180:181],v205 offset:57344
ze v59,v59
zt a[182:183],v205 offset:61440
zc v125,v58,v59
ze v60,v60
zt a[184:185],v206 offset:57344
s_waitcnt lgkmcnt(5)
ze v61,v61
zt a[186:187],v206 offset:61440
zc v126,v60,v61
ze v62,v62
zt a[188:189],v207 offset:57344
ze v63,v63
zt a[190:191],v207 offset:61440
zc v127,v62,v63
s_nop 1
zm a[0:3],a[160:163],v[112:115],a[0:3]
s_lshl_b32 s74, s75, 1
v_xor_b32 v239, 0x10000, v200
zm a[16:19],a[164:167],v[112:115],a[16:19]
zm a[20:23],a[164:167],v[116:119],a[20:23]
s_mov_b32 s42, 0x20000
zm a[4:7],a[160:163],v[116:119],a[4:7]
s_add_u32 s54, s74, 2
zm a[8:11],a[160:163],v[120:123],a[8:11]
s_and_b32 s54, s54, 7
zm a[24:27],a[164:167],v[120:123],a[24:27]
s_lshl_b32 s54, s54, 5
v_xor_b32 v200, s54, v239
zm a[28:31],a[164:167],v[124:127],a[28:31]
s_add_u32 s54, s74, 3
zm a[12:15],a[160:163],v[124:127],a[12:15]
s_and_b32 s54, s54, 7
v_smfmac_f32_16x16x64_bf16 v[80:83], v[192:195], v[112:119], v253
s_lshl_b32 s54, s54, 5
v_xor_b32 v201, s54, v239
v_smfmac_f32_16x16x64_bf16 v[84:87], v[192:195], v[120:127], v253
s_add_u32 s54, s74, 4
zm a[44:47],a[168:171],v[124:127],a[44:47]
s_and_b32 s54, s54, 7
zm a[60:63],a[172:175],v[124:127],a[60:63]
s_lshl_b32 s54, s54, 5
v_xor_b32 v202, s54, v239
zm a[56:59],a[172:175],v[120:123],a[56:59]
s_waitcnt lgkmcnt(0)
s_add_u32 s54, s74, 5
zm a[40:43],a[168:171],v[120:123],a[40:43]
s_and_b32 s54, s54, 7
zm a[36:39],a[168:171],v[116:119],a[36:39]
s_lshl_b32 s54, s54, 5
v_xor_b32 v203, s54, v239
zm a[52:55],a[172:175],v[116:119],a[52:55]
s_add_u32 s54, s74, 6
zm a[48:51],a[172:175],v[112:115],a[48:51]
s_and_b32 s54, s54, 7
v_add_f32 v228, v80, v81
v_add_f32 v230, v82, v83
zm a[32:35],a[168:171],v[112:115],a[32:35]
s_lshl_b32 s54, s54, 5
v_xor_b32 v204, s54, v239
zm a[64:67],a[176:179],v[112:115],a[64:67]
s_add_u32 s54, s74, 7
v_add_f32 v232, v84, v85
v_add_f32 v234, v86, v87
zm a[80:83],a[180:183],v[112:115],a[80:83]
s_and_b32 s54, s54, 7
v_add_f32 v236, v228, v230
zm a[84:87],a[180:183],v[116:119],a[84:87]
s_lshl_b32 s54, s54, 5
v_xor_b32 v205, s54, v239
v_add_f32 v237, v232, v234
zm a[68:71],a[176:179],v[116:119],a[68:71]
s_add_u32 s54, s74, 8
v_add_f32 v236, v236, v237
zm a[72:75],a[176:179],v[120:123],a[72:75]
s_and_b32 s54, s54, 7
v_cmp_nge_f32 s[56:57], s55, v236
v_mov_b32 v238, s64
zm a[88:91],a[180:183],v[120:123],a[88:91]
s_lshl_b32 s54, s54, 5
v_xor_b32 v206, s54, v239
zm a[92:95],a[180:183],v[124:127],a[92:95]
s_add_u32 s54, s74, 9
v_lshlrev_b32 v238, 2, v238
v_add_u32 v238, 0x26000, v238
zm a[76:79],a[176:179],v[124:127],a[76:79]
s_and_b32 s54, s54, 7
zm a[108:111],a[184:187],v[124:127],a[108:111]
s_lshl_b32 s54, s54, 5
v_xor_b32 v207, s54, v239
zm a[124:127],a[188:191],v[124:127],a[124:127]
zm a[120:123],a[188:191],v[120:123],a[120:123]
s_or_b32 s71, s56, s57
v_mov_b32 v237, s71
zm a[104:107],a[184:187],v[120:123],a[104:107]
ds_write_b32 v238, v237 offset:0
zm a[100:103],a[184:187],v[116:119],a[100:103]
zm a[116:119],a[188:191],v[116:119],a[116:119]
zm a[112:115],a[188:191],v[112:115],a[112:115]
s_waitcnt vmcnt(12) lgkmcnt(0)
zm a[96:99],a[184:187],v[112:115],a[96:99]
s_barrier
v_mov_b32 v248, 0x26000
zk v[244:247],v248 offset:0
v_max_f32 v228, 0.5, v228
v_max_f32 v230, 0.5, v230
v_max_f32 v232, 0.5, v232
v_max_f32 v234, 0.5, v234
v_rcp_f32 v229, v228
v_rcp_f32 v231, v230
v_rcp_f32 v233, v232
v_rcp_f32 v235, v234
v_rcp_f32 v228, v228
v_rcp_f32 v230, v230
v_rcp_f32 v232, v232
v_rcp_f32 v234, v234
.p2align 3
zr v236,a0
zr v237,a1
zr v238,a2
zr v239,a3
zr v240,a16
zr v241,a17
zr v242,a18
zr v243,a19
s_waitcnt lgkmcnt(0)
v_or3_b32 v244, v244, v245, v246
v_or_b32 v244, v244, v247
zp v[236:237],v[236:237],v[228:229]
zp v[238:239],v[238:239],v[228:229]
zp v[240:241],v[240:241],v[228:229]
zp v[242:243],v[242:243],v[228:229]
v_readfirstlane_b32 s71, v244
zc v32,v236,v237
zc v33,v238,v239
zc v34,v240,v241
zc v35,v242,v243
s_andn2_b32 s71, s71, s76
s_cbranch_scc1 L_fail%=
.p2align 3
zr v236,a4
zr v237,a5
zr v238,a6
zr v239,a7
zr v240,a20
zr v241,a21
zr v242,a22
zr v243,a23
zp v[236:237],v[236:237],v[230:231]
zp v[238:239],v[238:239],v[230:231]
zp v[240:241],v[240:241],v[230:231]
zp v[242:243],v[242:243],v[230:231]
zc v36,v236,v237
zc v37,v238,v239
zc v38,v240,v241
zc v39,v242,v243
.p2align 3
zr v236,a8
zr v237,a9
zr v238,a10
zr v239,a11
zr v240,a24
zr v241,a25
zr v242,a26
zr v243,a27
zp v[236:237],v[236:237],v[232:233]
zp v[238:239],v[238:239],v[232:233]
zp v[240:241],v[240:241],v[232:233]
zp v[242:243],v[242:243],v[232:233]
zc v40,v236,v237
zc v41,v238,v239
zc v42,v240,v241
zc v43,v242,v243
s_add_u32 s60, s88, 0x4000
buffer_store_dwordx4 v[32:35], v214, s[40:43], s60 offen offset:0 sc1
.p2align 3
zr v236,a12
zr v237,a13
zr v238,a14
zr v239,a15
zr v240,a28
zr v241,a29
zr v242,a30
zr v243,a31
zp v[236:237],v[236:237],v[234:235]
zp v[238:239],v[238:239],v[234:235]
zp v[240:241],v[240:241],v[234:235]
zp v[242:243],v[242:243],v[234:235]
zc v44,v236,v237
zc v45,v238,v239
zc v46,v240,v241
zc v47,v242,v243
zt a[128:129],v200 offset:0
zt a[130:131],v200 offset:4096
zt a[132:133],v200 offset:8192
zt a[134:135],v200 offset:12288
zt a[136:137],v200 offset:16384
zt a[138:139],v200 offset:20480
zt a[140:141],v200 offset:24576
zt a[142:143],v200 offset:28672
zt a[144:145],v201 offset:0
zt a[146:147],v201 offset:4096
zt a[148:149],v201 offset:8192
zt a[150:151],v201 offset:12288
.p2align 3
zr v236,a32
zr v237,a33
zr v238,a34
zr v239,a35
zr v240,a48
zr v241,a49
zr v242,a50
zr v243,a51
zp v[236:237],v[236:237],v[228:229]
zp v[238:239],v[238:239],v[228:229]
zp v[240:241],v[240:241],v[228:229]
zp v[242:243],v[242:243],v[228:229]
zc v48,v236,v237
zc v49,v238,v239
zc v50,v240,v241
zc v51,v242,v243
.p2align 3
zr v236,a36
zr v237,a37
zr v238,a38
zr v239,a39
zr v240,a52
zr v241,a53
zr v242,a54
zr v243,a55
zp v[236:237],v[236:237],v[230:231]
zp v[238:239],v[238:239],v[230:231]
zp v[240:241],v[240:241],v[230:231]
zp v[242:243],v[242:243],v[230:231]
zc v52,v236,v237
zc v53,v238,v239
zc v54,v240,v241
zc v55,v242,v243
buffer_store_dwordx4 v[36:39], v214, s[40:43], s60 offen offset:1024 sc1
s_waitcnt lgkmcnt(10)
zm a[192:195],a[128:131],v[128:131],0
s_waitcnt lgkmcnt(8)
s_add_u32 s50, s85, 0
zt a[152:153],v201 offset:16384
zr v236,a40
zm a[192:195],a[132:135],v[144:147],a[192:195]
s_waitcnt lgkmcnt(7)
s_add_u32 m0, s45, 0
zr v237,a41
zr v238,a42
zm a[192:195],a[136:139],v[160:163],a[192:195]
s_waitcnt lgkmcnt(5)
zt a[154:155],v201 offset:20480
zl v208,s[24:27],s50 offen offset:0 lds
zm a[192:195],a[140:143],v[176:179],a[192:195]
zt a[156:157],v201 offset:24576
buffer_store_dwordx4 v[40:43], v214, s[40:43], s60 offen offset:2048 sc1
zm a[196:199],a[140:143],v[180:183],0
zr v239,a43
zr v240,a56
zm a[196:199],a[136:139],v[164:167],a[196:199]
zt a[158:159],v201 offset:28672
zr v241,a57
zm a[196:199],a[132:135],v[148:151],a[196:199]
zr v242,a58
zr v243,a59
zm a[196:199],a[128:131],v[132:135],a[196:199]
zt a[160:161],v202 offset:0
zl v209,s[24:27],s50 offen offset:1024 lds
zm a[200:203],a[128:131],v[136:139],0
zt a[162:163],v202 offset:4096
v_mul_f32 v236, v232, v236
zm a[200:203],a[132:135],v[152:155],a[200:203]
v_mul_f32 v237, v232, v237
v_mul_f32 v238, v232, v238
zm a[200:203],a[136:139],v[168:171],a[200:203]
zt a[164:165],v202 offset:8192
v_mul_f32 v239, v232, v239
zm a[200:203],a[140:143],v[184:187],a[200:203]
zt a[166:167],v202 offset:12288
v_mul_f32 v240, v232, v240
zm a[204:207],a[140:143],v[188:191],0
v_mul_f32 v241, v232, v241
v_mul_f32 v242, v232, v242
zm a[204:207],a[136:139],v[172:175],a[204:207]
zt a[168:169],v202 offset:16384
v_mul_f32 v243, v232, v243
zm a[204:207],a[132:135],v[156:159],a[204:207]
zt a[170:171],v202 offset:20480
zc v56,v236,v237
zm a[204:207],a[128:131],v[140:143],a[204:207]
s_waitcnt lgkmcnt(12)
zl v210,s[24:27],s50 offen offset:2048 lds
buffer_store_dwordx4 v[44:47], v214, s[40:43], s60 offen offset:3072 sc1
zm a[220:223],a[144:147],v[140:143],0
s_waitcnt lgkmcnt(10)
zt a[172:173],v202 offset:24576
zc v57,v238,v239
zm a[220:223],a[148:151],v[156:159],a[220:223]
s_waitcnt lgkmcnt(9)
zt a[174:175],v202 offset:28672
zc v58,v240,v241
zm a[220:223],a[152:155],v[172:175],a[220:223]
s_waitcnt lgkmcnt(8)
zc v59,v242,v243
zr v236,a44
zm a[220:223],a[156:159],v[188:191],a[220:223]
zt a[176:177],v203 offset:0
zr v237,a45
zm a[216:219],a[156:159],v[184:187],0
zr v238,a46
zr v239,a47
zm a[216:219],a[152:155],v[168:171],a[216:219]
zt a[178:179],v203 offset:4096
zr v240,a60
zm a[216:219],a[148:151],v[152:155],a[216:219]
zt a[180:181],v203 offset:8192
zr v241,a61
zm a[216:219],a[144:147],v[136:139],a[216:219]
zl v211,s[24:27],s50 offen offset:3072 lds
zr v242,a62
zm a[212:215],a[144:147],v[132:135],0
zt a[182:183],v203 offset:12288
zr v243,a63
zm a[212:215],a[148:151],v[148:151],a[212:215]
zt a[184:185],v203 offset:16384
v_mul_f32 v236, v234, v236
zm a[212:215],a[152:155],v[164:167],a[212:215]
s_add_u32 s60, s88, 0x5000
v_mul_f32 v237, v234, v237
v_mul_f32 v238, v234, v238
zm a[212:215],a[156:159],v[180:183],a[212:215]
zt a[186:187],v203 offset:20480
buffer_store_dwordx4 v[48:51], v214, s[40:43], s60 offen offset:0 sc1
zm a[208:211],a[156:159],v[176:179],0
zt a[188:189],v203 offset:24576
v_mul_f32 v239, v234, v239
zm a[208:211],a[152:155],v[160:163],a[208:211]
s_add_u32 s50, s85, 0x10000
v_mul_f32 v240, v234, v240
v_mul_f32 v241, v234, v241
zm a[208:211],a[148:151],v[144:147],a[208:211]
s_add_u32 m0, s45, 0x4000
zt a[190:191],v203 offset:28672
v_mul_f32 v242, v234, v242
zm a[208:211],a[144:147],v[128:131],a[208:211]
s_waitcnt lgkmcnt(14)
zl v208,s[24:27],s50 offen offset:0 lds
v_mul_f32 v243, v234, v243
zm a[224:227],a[160:163],v[128:131],0
s_waitcnt lgkmcnt(12)
zc v60,v236,v237
zc v61,v238,v239
zm a[224:227],a[164:167],v[144:147],a[224:227]
s_waitcnt lgkmcnt(10)
zc v62,v240,v241
zm a[224:227],a[168:171],v[160:163],a[224:227]
s_waitcnt lgkmcnt(8)
zc v63,v242,v243
zr v236,a64
zm a[224:227],a[172:175],v[176:179],a[224:227]
zr v237,a65
zm a[228:231],a[172:175],v[180:183],0
zr v238,a66
zr v239,a67
zm a[228:231],a[168:171],v[164:167],a[228:231]
zr v240,a80
zm a[228:231],a[164:167],v[148:151],a[228:231]
zl v209,s[24:27],s50 offen offset:1024 lds
zr v241,a81
zm a[228:231],a[160:163],v[132:135],a[228:231]
buffer_store_dwordx4 v[52:55], v214, s[40:43], s60 offen offset:1024 sc1
zr v242,a82
zm a[232:235],a[160:163],v[136:139],0
zr v243,a83
v_mul_f32 v236, v228, v236
zm a[232:235],a[164:167],v[152:155],a[232:235]
v_mul_f32 v237, v228, v237
zm a[232:235],a[168:171],v[168:171],a[232:235]
v_mul_f32 v238, v228, v238
v_mul_f32 v239, v228, v239
zm a[232:235],a[172:175],v[184:187],a[232:235]
v_mul_f32 v240, v228, v240
zm a[236:239],a[172:175],v[188:191],0
v_mul_f32 v241, v228, v241
v_mul_f32 v242, v228, v242
zm a[236:239],a[168:171],v[172:175],a[236:239]
v_mul_f32 v243, v228, v243
zm a[236:239],a[164:167],v[156:159],a[236:239]
zt a[128:129],v204 offset:0
zl v210,s[24:27],s50 offen offset:2048 lds
zm a[236:239],a[160:163],v[140:143],a[236:239]
s_waitcnt lgkmcnt(7)
zt a[130:131],v204 offset:4096
zc v96,v236,v237
zm a[252:255],a[176:179],v[140:143],0
s_waitcnt lgkmcnt(6)
zc v97,v238,v239
zc v98,v240,v241
zm a[252:255],a[180:183],v[156:159],a[252:255]
s_waitcnt lgkmcnt(4)
zt a[132:133],v204 offset:8192
zc v99,v242,v243
zm a[252:255],a[184:187],v[172:175],a[252:255]
s_waitcnt lgkmcnt(3)
zt a[134:135],v204 offset:12288
zr v236,a68
zm a[252:255],a[188:191],v[188:191],a[252:255]
buffer_store_dwordx4 v[56:59], v214, s[40:43], s60 offen offset:2048 sc1
zr v237,a69
zm a[248:251],a[188:191],v[184:187],0
zt a[136:137],v204 offset:16384
zr v238,a70
zm a[248:251],a[184:187],v[168:171],a[248:251]
zt a[138:139],v204 offset:20480
zr v239,a71
zm a[248:251],a[180:183],v[152:155],a[248:251]
zl v211,s[24:27],s50 offen offset:3072 lds
zr v240,a84
zm a[248:251],a[176:179],v[136:139],a[248:251]
zt a[140:141],v204 offset:24576
zr v241,a85
zm a[244:247],a[176:179],v[132:135],0
zt a[142:143],v204 offset:28672
zr v242,a86
zm a[244:247],a[180:183],v[148:151],a[244:247]
zr v243,a87
v_mul_f32 v236, v230, v236
zm a[244:247],a[184:187],v[164:167],a[244:247]
zt a[144:145],v205 offset:0
v_mul_f32 v237, v230, v237
zm a[244:247],a[188:191],v[180:183],a[244:247]
v_mul_f32 v238, v230, v238
v_mul_f32 v239, v230, v239
zm a[240:243],a[188:191],v[176:179],0
zt a[146:147],v205 offset:4096
v_mul_f32 v240, v230, v240
zm a[240:243],a[184:187],v[160:163],a[240:243]
zt a[148:149],v205 offset:8192
v_mul_f32 v241, v230, v241
zm a[240:243],a[180:183],v[144:147],a[240:243]
v_mul_f32 v242, v230, v242
v_mul_f32 v243, v230, v243
zm a[240:243],a[176:179],v[128:131],a[240:243]
s_waitcnt lgkmcnt(9)
zt a[150:151],v205 offset:12288
buffer_store_dwordx4 v[60:63], v214, s[40:43], s60 offen offset:3072 sc1
zm a[0:3],a[128:131],v[128:131],0
s_waitcnt lgkmcnt(8)
zt a[152:153],v205 offset:16384
buffer_load_dwordx2 v[226:227], v215, s[92:95], 0 offen offset:0 sc1
zm a[0:3],a[132:135],v[144:147],a[0:3]
s_waitcnt lgkmcnt(7)
s_mov_b32 s96, s22
zc v100,v236,v237
zc v101,v238,v239
zm a[0:3],a[136:139],v[160:163],a[0:3]
s_waitcnt lgkmcnt(5)
s_and_b32 s97, s23, 0xffff
zt a[154:155],v205 offset:20480
zc v102,v240,v241
zm a[0:3],a[140:143],v[176:179],a[0:3]
s_mov_b32 s98, 0x7a000
zt a[156:157],v205 offset:24576
zc v103,v242,v243
zm a[4:7],a[140:143],v[180:183],0
s_mov_b32 s99, 0x20000
zr v236,a72
zr v237,a73
zm a[4:7],a[136:139],v[164:167],a[4:7]
s_add_u32 s50, s86, 0x10000
zt a[158:159],v205 offset:28672
zr v238,a74
zm a[4:7],a[132:135],v[148:151],a[4:7]
s_add_u32 m0, s45, 0xc000
zr v239,a75
zr v240,a88
zm a[4:7],a[128:131],v[132:135],a[4:7]
zt a[160:161],v206 offset:0
zl v208,s[24:27],s50 offen offset:0 lds
zm a[8:11],a[128:131],v[136:139],0
zt a[162:163],v206 offset:4096
zr v241,a89
zm a[8:11],a[132:135],v[152:155],a[8:11]
zr v242,a90
zr v243,a91
zm a[8:11],a[136:139],v[168:171],a[8:11]
s_add_u32 s60, s88, 0x6000
zt a[164:165],v206 offset:8192
v_mul_f32 v236, v232, v236
zm a[8:11],a[140:143],v[184:187],a[8:11]
zt a[166:167],v206 offset:12288
buffer_store_dwordx4 v[96:99], v214, s[40:43], s60 offen offset:0 sc1
zm a[12:15],a[140:143],v[188:191],0
v_mul_f32 v237, v232, v237
v_mul_f32 v238, v232, v238
zm a[12:15],a[136:139],v[172:175],a[12:15]
zt a[168:169],v206 offset:16384
zl v209,s[24:27],s50 offen offset:1024 lds
zm a[12:15],a[132:135],v[156:159],a[12:15]
zt a[170:171],v206 offset:20480
v_mul_f32 v239, v232, v239
zm a[12:15],a[128:131],v[140:143],a[12:15]
s_waitcnt lgkmcnt(12)
v_mul_f32 v240, v232, v240
v_mul_f32 v241, v232, v241
zm a[28:31],a[144:147],v[140:143],0
s_waitcnt lgkmcnt(10)
zt a[172:173],v206 offset:24576
v_mul_f32 v242, v232, v242
zm a[28:31],a[148:151],v[156:159],a[28:31]
s_waitcnt lgkmcnt(9)
zt a[174:175],v206 offset:28672
v_mul_f32 v243, v232, v243
zm a[28:31],a[152:155],v[172:175],a[28:31]
s_waitcnt lgkmcnt(8)
zc v104,v236,v237
zc v105,v238,v239
zm a[28:31],a[156:159],v[188:191],a[28:31]
zt a[176:177],v207 offset:0
zc v106,v240,v241
zm a[24:27],a[156:159],v[184:187],0
zl v210,s[24:27],s50 offen offset:2048 lds
zc v107,v242,v243
zm a[24:27],a[152:155],v[168:171],a[24:27]
zt a[178:179],v207 offset:4096
zr v236,a76
zm a[24:27],a[148:151],v[152:155],a[24:27]
zt a[180:181],v207 offset:8192
zr v237,a77
zm a[24:27],a[144:147],v[136:139],a[24:27]
buffer_store_dwordx4 v[100:103], v214, s[40:43], s60 offen offset:1024 sc1
zr v238,a78
zm a[20:23],a[144:147],v[132:135],0
zt a[182:183],v207 offset:12288
zr v239,a79
zm a[20:23],a[148:151],v[148:151],a[20:23]
zt a[184:185],v207 offset:16384
zr v240,a92
zm a[20:23],a[152:155],v[164:167],a[20:23]
zr v241,a93
zr v242,a94
zm a[20:23],a[156:159],v[180:183],a[20:23]
zt a[186:187],v207 offset:20480
zl v211,s[24:27],s50 offen offset:3072 lds
zm a[16:19],a[156:159],v[176:179],0
zt a[188:189],v207 offset:24576
zr v243,a95
zm a[16:19],a[152:155],v[160:163],a[16:19]
v_mul_f32 v236, v234, v236
v_mul_f32 v237, v234, v237
zm a[16:19],a[148:151],v[144:147],a[16:19]
zt a[190:191],v207 offset:28672
v_mul_f32 v238, v234, v238
zm a[16:19],a[144:147],v[128:131],a[16:19]
s_waitcnt lgkmcnt(14)
v_mul_f32 v239, v234, v239
v_mul_f32 v240, v234, v240
zm a[32:35],a[160:163],v[128:131],0
s_waitcnt lgkmcnt(12)
s_add_u32 s50, s86, 0
v_mul_f32 v241, v234, v241
zm a[32:35],a[164:167],v[144:147],a[32:35]
s_waitcnt lgkmcnt(10)
s_add_u32 m0, s45, 0x8000
v_mul_f32 v242, v234, v242
v_mul_f32 v243, v234, v243
zm a[32:35],a[168:171],v[160:163],a[32:35]
s_waitcnt lgkmcnt(8)
zl v208,s[24:27],s50 offen offset:0 lds
zc v108,v236,v237
zm a[32:35],a[172:175],v[176:179],a[32:35]
buffer_store_dwordx4 v[104:107], v214, s[40:43], s60 offen offset:2048 sc1
zc v109,v238,v239
zm a[36:39],a[172:175],v[180:183],0
zc v110,v240,v241
zc v111,v242,v243
zm a[36:39],a[168:171],v[164:167],a[36:39]
zr v236,a96
zm a[36:39],a[164:167],v[148:151],a[36:39]
zr v237,a97
zr v238,a98
zm a[36:39],a[160:163],v[132:135],a[36:39]
zr v239,a99
zm a[40:43],a[160:163],v[136:139],0
zr v240,a112
zr v241,a113
zm a[40:43],a[164:167],v[152:155],a[40:43]
zl v209,s[24:27],s50 offen offset:1024 lds
zr v242,a114
zm a[40:43],a[168:171],v[168:171],a[40:43]
zr v243,a115
v_mul_f32 v236, v228, v236
zm a[40:43],a[172:175],v[184:187],a[40:43]
s_waitcnt vmcnt(27) lgkmcnt(0)
v_mul_f32 v237, v228, v237
zm a[44:47],a[172:175],v[188:191],0
s_barrier
v_mul_f32 v238, v228, v238
v_mul_f32 v239, v228, v239
zm a[44:47],a[168:171],v[172:175],a[44:47]
v_mul_f32 v240, v228, v240
zm a[44:47],a[164:167],v[156:159],a[44:47]
zt a[128:129],v200 offset:32768
v_mul_f32 v241, v228, v241
zm a[44:47],a[160:163],v[140:143],a[44:47]
zt a[130:131],v200 offset:36864
buffer_store_dwordx4 v[108:111], v214, s[40:43], s60 offen offset:3072 sc1
zm a[60:63],a[176:179],v[140:143],0
zl v210,s[24:27],s50 offen offset:2048 lds
v_mul_f32 v242, v228, v242
zm a[60:63],a[180:183],v[156:159],a[60:63]
zt a[132:133],v200 offset:40960
v_mul_f32 v243, v228, v243
zm a[60:63],a[184:187],v[172:175],a[60:63]
zt a[134:135],v200 offset:45056
zc v112,v236,v237
zm a[60:63],a[188:191],v[188:191],a[60:63]
zc v113,v238,v239
zc v114,v240,v241
zm a[56:59],a[188:191],v[184:187],0
zt a[136:137],v201 offset:32768
zc v115,v242,v243
zm a[56:59],a[184:187],v[168:171],a[56:59]
zt a[138:139],v201 offset:36864
zr v236,a100
zm a[56:59],a[180:183],v[152:155],a[56:59]
zr v237,a101
zr v238,a102
zm a[56:59],a[176:179],v[136:139],a[56:59]
zt a[140:141],v201 offset:40960
zl v211,s[24:27],s50 offen offset:3072 lds
zm a[52:55],a[176:179],v[132:135],0
zt a[142:143],v201 offset:45056
zr v239,a103
zm a[52:55],a[180:183],v[148:151],a[52:55]
zr v240,a116
zr v241,a117
zm a[52:55],a[184:187],v[164:167],a[52:55]
s_add_u32 s60, s88, 0x7000
zt a[144:145],v202 offset:32768
zr v242,a118
zm a[52:55],a[188:191],v[180:183],a[52:55]
buffer_store_dwordx4 v[112:115], v214, s[40:43], s60 offen offset:0 sc1
zr v243,a119
zm a[48:51],a[188:191],v[176:179],0
zt a[146:147],v202 offset:36864
v_mul_f32 v236, v230, v236
s_waitcnt vmcnt(13)
L_pc0%=:
v_cmp_ne_u64 vcc, v[226:227], v[216:217]
s_nop 1
s_cbranch_vccnz L_ps0%=
L_pd0%=:
zm a[48:51],a[184:187],v[160:163],a[48:51]
zt a[148:149],v202 offset:40960
v_mul_f32 v237, v230, v237
zm a[48:51],a[180:183],v[144:147],a[48:51]
v_mul_f32 v238, v230, v238
v_mul_f32 v239, v230, v239
zm a[48:51],a[176:179],v[128:131],a[48:51]
s_waitcnt lgkmcnt(9)
zt a[150:151],v202 offset:45056
v_mul_f32 v240, v230, v240
zm a[192:195],a[128:131],v[32:35],a[192:195]
s_waitcnt lgkmcnt(8)
s_add_u32 s50, s87, 0
zt a[152:153],v203 offset:32768
v_mul_f32 v241, v230, v241
zm a[192:195],a[132:135],v[48:51],a[192:195]
s_add_u32 m0, s44, 0
v_mul_f32 v242, v230, v242
v_mul_f32 v243, v230, v243
zm a[196:199],a[132:135],v[52:55],a[196:199]
zt a[154:155],v203 offset:36864
zl v208,s[24:27],s50 offen offset:0 lds
zm a[196:199],a[128:131],v[36:39],a[196:199]
zt a[156:157],v203 offset:40960
zc v116,v236,v237
zm a[200:203],a[128:131],v[40:43],a[200:203]
zc v117,v238,v239
zc v118,v240,v241
zm a[200:203],a[132:135],v[56:59],a[200:203]
zt a[158:159],v203 offset:45056
zc v119,v242,v243
zm a[204:207],a[132:135],v[60:63],a[204:207]
zl v209,s[24:27],s50 offen offset:1024 lds
zr v236,a104
zm a[204:207],a[128:131],v[44:47],a[204:207]
s_waitcnt lgkmcnt(10)
zt a[160:161],v204 offset:32768
buffer_store_dwordx4 v[116:119], v214, s[40:43], s60 offen offset:1024 sc1
zm a[220:223],a[136:139],v[44:47],a[220:223]
s_waitcnt lgkmcnt(9)
zt a[162:163],v204 offset:36864
zr v237,a105
zm a[220:223],a[140:143],v[60:63],a[220:223]
zr v238,a106
zr v239,a107
zm a[216:219],a[140:143],v[56:59],a[216:219]
zt a[164:165],v204 offset:40960
zr v240,a120
zm a[216:219],a[136:139],v[40:43],a[216:219]
zt a[166:167],v204 offset:45056
zr v241,a121
zm a[212:215],a[136:139],v[36:39],a[212:215]
zl v210,s[24:27],s50 offen offset:2048 lds
zr v242,a122
zm a[212:215],a[140:143],v[52:55],a[212:215]
zt a[168:169],v205 offset:32768
zr v243,a123
zm a[208:211],a[140:143],v[48:51],a[208:211]
zt a[170:171],v205 offset:36864
v_mul_f32 v236, v232, v236
zm a[208:211],a[136:139],v[32:35],a[208:211]
s_waitcnt lgkmcnt(12)
v_mul_f32 v237, v232, v237
v_mul_f32 v238, v232, v238
zm a[224:227],a[144:147],v[32:35],a[224:227]
s_waitcnt lgkmcnt(10)
zt a[172:173],v205 offset:40960
v_mul_f32 v239, v232, v239
zm a[224:227],a[148:151],v[48:51],a[224:227]
zt a[174:175],v205 offset:45056
v_mul_f32 v240, v232, v240
zm a[228:231],a[148:151],v[52:55],a[228:231]
zl v211,s[24:27],s50 offen offset:3072 lds
v_mul_f32 v241, v232, v241
zm a[228:231],a[144:147],v[36:39],a[228:231]
zt a[176:177],v206 offset:32768
v_mul_f32 v242, v232, v242
zm a[232:235],a[144:147],v[40:43],a[232:235]
v_mul_f32 v243, v232, v243
zc v120,v236,v237
zm a[232:235],a[148:151],v[56:59],a[232:235]
zt a[178:179],v206 offset:36864
zc v121,v238,v239
zm a[236:239],a[148:151],v[60:63],a[236:239]
zt a[180:181],v206 offset:40960
zc v122,v240,v241
zm a[236:239],a[144:147],v[44:47],a[236:239]
s_waitcnt lgkmcnt(13)
s_add_u32 s72, s89, 0
zc v123,v242,v243
zr v236,a108
zm a[252:255],a[152:155],v[44:47],a[252:255]
s_waitcnt lgkmcnt(11)
zt a[182:183],v206 offset:45056
zl v[0:3],v214,s[40:43],s72 offen offset:0 sc1
zm a[252:255],a[156:159],v[60:63],a[252:255]
zt a[184:185],v207 offset:32768
buffer_store_dwordx4 v[120:123], v214, s[40:43], s60 offen offset:2048 sc1
zm a[248:251],a[156:159],v[56:59],a[248:251]
zr v237,a109
zr v238,a110
zm a[248:251],a[152:155],v[40:43],a[248:251]
zt a[186:187],v207 offset:36864
zr v239,a111
zm a[244:247],a[152:155],v[36:39],a[244:247]
zt a[188:189],v207 offset:40960
zr v240,a124
zm a[244:247],a[156:159],v[52:55],a[244:247]
zr v241,a125
zr v242,a126
zm a[240:243],a[156:159],v[48:51],a[240:243]
zt a[190:191],v207 offset:45056
zr v243,a127
zm a[240:243],a[152:155],v[32:35],a[240:243]
s_waitcnt lgkmcnt(14)
zl v[4:7],v214,s[40:43],s72 offen offset:1024 sc1
v_mul_f32 v236, v234, v236
zm a[0:3],a[160:163],v[32:35],a[0:3]
s_waitcnt lgkmcnt(12)
v_mul_f32 v237, v234, v237
v_mul_f32 v238, v234, v238
zm a[0:3],a[164:167],v[48:51],a[0:3]
v_mul_f32 v239, v234, v239
zm a[4:7],a[164:167],v[52:55],a[4:7]
v_mul_f32 v240, v234, v240
v_mul_f32 v241, v234, v241
zm a[4:7],a[160:163],v[36:39],a[4:7]
v_mul_f32 v242, v234, v242
zm a[8:11],a[160:163],v[40:43],a[8:11]
v_mul_f32 v243, v234, v243
zc v124,v236,v237
zm a[8:11],a[164:167],v[56:59],a[8:11]
zl v[8:11],v214,s[40:43],s72 offen offset:2048 sc1
zc v125,v238,v239
zm a[12:15],a[164:167],v[60:63],a[12:15]
zc v126,v240,v241
zc v127,v242,v243
zm a[12:15],a[160:163],v[44:47],a[12:15]
s_waitcnt lgkmcnt(10)
buffer_store_dwordx4 v[124:127], v214, s[40:43], s60 offen offset:3072 sc1
zm a[28:31],a[168:171],v[44:47],a[28:31]
s_waitcnt lgkmcnt(8)
zm a[28:31],a[172:175],v[60:63],a[28:31]
zm a[24:27],a[172:175],v[56:59],a[24:27]
zm a[24:27],a[168:171],v[40:43],a[24:27]
zl v[12:15],v214,s[40:43],s72 offen offset:3072 sc1
zm a[20:23],a[168:171],v[36:39],a[20:23]
zm a[20:23],a[172:175],v[52:55],a[20:23]
zm a[16:19],a[172:175],v[48:51],a[16:19]
zt a[128:129],v200 offset:49152
zm a[16:19],a[168:171],v[32:35],a[16:19]
s_waitcnt lgkmcnt(7)
zt a[130:131],v200 offset:53248
zm a[32:35],a[176:179],v[32:35],a[32:35]
s_waitcnt lgkmcnt(6)
s_add_u32 s72, s89, 0x1000
zm a[32:35],a[180:183],v[48:51],a[32:35]
zt a[132:133],v200 offset:57344
zl v[16:19],v214,s[40:43],s72 offen offset:0 sc1
zm a[36:39],a[180:183],v[52:55],a[36:39]
zt a[134:135],v200 offset:61440
zm a[36:39],a[176:179],v[36:39],a[36:39]
zm a[40:43],a[176:179],v[40:43],a[40:43]
zt a[136:137],v201 offset:49152
zm a[40:43],a[180:183],v[56:59],a[40:43]
zt a[138:139],v201 offset:53248
zm a[44:47],a[180:183],v[60:63],a[44:47]
zm a[44:47],a[176:179],v[44:47],a[44:47]
s_waitcnt lgkmcnt(8)
zt a[140:141],v201 offset:57344
zl v[20:23],v214,s[40:43],s72 offen offset:1024 sc1
zm a[60:63],a[184:187],v[44:47],a[60:63]
s_waitcnt lgkmcnt(7)
zt a[142:143],v201 offset:61440
zm a[60:63],a[188:191],v[60:63],a[60:63]
zm a[56:59],a[188:191],v[56:59],a[56:59]
zt a[144:145],v202 offset:49152
zm a[56:59],a[184:187],v[40:43],a[56:59]
zm a[52:55],a[184:187],v[36:39],a[52:55]
zt a[146:147],v202 offset:53248
zm a[52:55],a[188:191],v[52:55],a[52:55]
zt a[148:149],v202 offset:57344
zm a[48:51],a[188:191],v[48:51],a[48:51]
zm a[48:51],a[184:187],v[32:35],a[48:51]
s_waitcnt lgkmcnt(9)
zt a[150:151],v202 offset:61440
zm a[192:195],a[128:131],v[96:99],a[192:195]
s_waitcnt lgkmcnt(8)
s_add_u32 s50, s87, 0x10000
zt a[152:153],v203 offset:49152
zm a[192:195],a[132:135],v[112:115],a[192:195]
s_add_u32 m0, s44, 0x4000
zm a[196:199],a[132:135],v[116:119],a[196:199]
zt a[154:155],v203 offset:53248
zl v208,s[24:27],s50 offen offset:0 lds
zm a[196:199],a[128:131],v[100:103],a[196:199]
zt a[156:157],v203 offset:57344
zm a[200:203],a[128:131],v[104:107],a[200:203]
zl v209,s[24:27],s50 offen offset:1024 lds
zm a[200:203],a[132:135],v[120:123],a[200:203]
zt a[158:159],v203 offset:61440
zm a[204:207],a[132:135],v[124:127],a[204:207]
zm a[204:207],a[128:131],v[108:111],a[204:207]
s_waitcnt lgkmcnt(10)
zt a[160:161],v204 offset:49152
zm a[220:223],a[136:139],v[108:111],a[220:223]
s_waitcnt lgkmcnt(9)
zt a[162:163],v204 offset:53248
zl v210,s[24:27],s50 offen offset:2048 lds
zm a[220:223],a[140:143],v[124:127],a[220:223]
zm a[216:219],a[140:143],v[120:123],a[216:219]
zt a[164:165],v204 offset:57344
zm a[216:219],a[136:139],v[104:107],a[216:219]
zt a[166:167],v204 offset:61440
zm a[212:215],a[136:139],v[100:103],a[212:215]
zm a[212:215],a[140:143],v[116:119],a[212:215]
zt a[168:169],v205 offset:49152
zl v211,s[24:27],s50 offen offset:3072 lds
zm a[208:211],a[140:143],v[112:115],a[208:211]
zt a[170:171],v205 offset:53248
zm a[208:211],a[136:139],v[96:99],a[208:211]
)"
R"(s_waitcnt lgkmcnt(12)
zm a[224:227],a[144:147],v[96:99],a[224:227]
s_waitcnt lgkmcnt(10)
zt a[172:173],v205 offset:57344
zm a[224:227],a[148:151],v[112:115],a[224:227]
zt a[174:175],v205 offset:61440
zl v[24:27],v214,s[40:43],s72 offen offset:2048 sc1
zm a[228:231],a[148:151],v[116:119],a[228:231]
zm a[228:231],a[144:147],v[100:103],a[228:231]
zt a[176:177],v206 offset:49152
zm a[232:235],a[144:147],v[104:107],a[232:235]
zm a[232:235],a[148:151],v[120:123],a[232:235]
zt a[178:179],v206 offset:53248
zm a[236:239],a[148:151],v[124:127],a[236:239]
zt a[180:181],v206 offset:57344
zl v[28:31],v214,s[40:43],s72 offen offset:3072 sc1
zm a[236:239],a[144:147],v[108:111],a[236:239]
s_waitcnt lgkmcnt(13)
zm a[252:255],a[152:155],v[108:111],a[252:255]
s_waitcnt lgkmcnt(11)
zt a[182:183],v206 offset:61440
zm a[252:255],a[156:159],v[124:127],a[252:255]
s_add_u32 s72, s89, 0x2000
zt a[184:185],v207 offset:49152
zm a[248:251],a[156:159],v[120:123],a[248:251]
zl v[64:67],v214,s[40:43],s72 offen offset:0 sc1
zm a[248:251],a[152:155],v[104:107],a[248:251]
zt a[186:187],v207 offset:53248
zm a[244:247],a[152:155],v[100:103],a[244:247]
zt a[188:189],v207 offset:57344
zm a[244:247],a[156:159],v[116:119],a[244:247]
zm a[240:243],a[156:159],v[112:115],a[240:243]
zt a[190:191],v207 offset:61440
zm a[240:243],a[152:155],v[96:99],a[240:243]
s_waitcnt lgkmcnt(14)
zl v[68:71],v214,s[40:43],s72 offen offset:1024 sc1
zm a[0:3],a[160:163],v[96:99],a[0:3]
s_waitcnt lgkmcnt(12)
zm a[0:3],a[164:167],v[112:115],a[0:3]
zm a[4:7],a[164:167],v[116:119],a[4:7]
zm a[4:7],a[160:163],v[100:103],a[4:7]
zl v[72:75],v214,s[40:43],s72 offen offset:2048 sc1
v_xor_b32 v200, 0x10000, v200
zm a[8:11],a[160:163],v[104:107],a[8:11]
v_xor_b32 v201, 0x10000, v201
zm a[8:11],a[164:167],v[120:123],a[8:11]
v_xor_b32 v202, 0x10000, v202
zm a[12:15],a[164:167],v[124:127],a[12:15]
v_xor_b32 v203, 0x10000, v203
zm a[12:15],a[160:163],v[108:111],a[12:15]
s_waitcnt lgkmcnt(10)
zl v[76:79],v214,s[40:43],s72 offen offset:3072 sc1
v_xor_b32 v204, 0x10000, v204
zm a[28:31],a[168:171],v[108:111],a[28:31]
s_waitcnt lgkmcnt(8)
v_xor_b32 v205, 0x10000, v205
zm a[28:31],a[172:175],v[124:127],a[28:31]
v_xor_b32 v206, 0x10000, v206
zm a[24:27],a[172:175],v[120:123],a[24:27]
v_xor_b32 v207, 0x10000, v207
zm a[24:27],a[168:171],v[104:107],a[24:27]
s_waitcnt vmcnt(38) lgkmcnt(0)
zm a[20:23],a[168:171],v[100:103],a[20:23]
s_barrier
s_add_u32 s72, s89, 0x3000
zm a[20:23],a[172:175],v[116:119],a[20:23]
zl v[80:83],v214,s[40:43],s72 offen offset:0 sc1
zm a[16:19],a[172:175],v[112:115],a[16:19]
zt a[128:129],v200 offset:0
zm a[16:19],a[168:171],v[96:99],a[16:19]
zt a[130:131],v200 offset:4096
zm a[32:35],a[176:179],v[96:99],a[32:35]
zl v[84:87],v214,s[40:43],s72 offen offset:1024 sc1
zm a[32:35],a[180:183],v[112:115],a[32:35]
zt a[132:133],v200 offset:8192
zm a[36:39],a[180:183],v[116:119],a[36:39]
zt a[134:135],v200 offset:12288
zm a[36:39],a[176:179],v[100:103],a[36:39]
zm a[40:43],a[176:179],v[104:107],a[40:43]
zt a[136:137],v201 offset:0
s_waitcnt vmcnt(15)
s_mov_b64 exec, 1
buffer_store_dwordx2 v[216:217], off, s[92:95], s77 offset:1024 sc1
s_mov_b64 exec, -1
zm a[40:43],a[180:183],v[120:123],a[40:43]
zt a[138:139],v201 offset:4096
zl v[88:91],v214,s[40:43],s72 offen offset:2048 sc1
zm a[44:47],a[180:183],v[124:127],a[44:47]
zm a[44:47],a[176:179],v[108:111],a[44:47]
zt a[140:141],v201 offset:8192
zm a[60:63],a[184:187],v[108:111],a[60:63]
zt a[142:143],v201 offset:12288
zm a[60:63],a[188:191],v[124:127],a[60:63]
zl v[92:95],v214,s[40:43],s72 offen offset:3072 sc1
zm a[56:59],a[188:191],v[120:123],a[56:59]
zt a[144:145],v202 offset:0
zm a[56:59],a[184:187],v[104:107],a[56:59]
zm a[52:55],a[184:187],v[100:103],a[52:55]
zt a[146:147],v202 offset:4096
zm a[52:55],a[188:191],v[116:119],a[52:55]
zt a[148:149],v202 offset:8192
zm a[48:51],a[188:191],v[112:115],a[48:51]
zm a[48:51],a[184:187],v[96:99],a[48:51]
s_waitcnt lgkmcnt(9)
s_waitcnt vmcnt(9)
zt a[150:151],v202 offset:12288
zm a[192:195],a[128:131],v[0:3],a[192:195]
s_waitcnt lgkmcnt(8)
s_add_u32 s50, s85, 0x20000
zt a[152:153],v203 offset:0
zm a[192:195],a[132:135],v[16:19],a[192:195]
s_add_u32 m0, s44, 0x8000
zm a[196:199],a[132:135],v[20:23],a[196:199]
zt a[154:155],v203 offset:4096
zl v208,s[24:27],s50 offen offset:0 lds
zm a[196:199],a[128:131],v[4:7],a[196:199]
zt a[156:157],v203 offset:8192
zl v209,s[24:27],s50 offen offset:1024 lds
zm a[200:203],a[128:131],v[8:11],a[200:203]
zm a[200:203],a[132:135],v[24:27],a[200:203]
zt a[158:159],v203 offset:12288
zm a[204:207],a[132:135],v[28:31],a[204:207]
zm a[204:207],a[128:131],v[12:15],a[204:207]
s_waitcnt lgkmcnt(10)
zt a[160:161],v204 offset:0
zl v210,s[24:27],s50 offen offset:2048 lds
zm a[220:223],a[136:139],v[12:15],a[220:223]
s_waitcnt lgkmcnt(9)
zt a[162:163],v204 offset:4096
zm a[220:223],a[140:143],v[28:31],a[220:223]
zm a[216:219],a[140:143],v[24:27],a[216:219]
zt a[164:165],v204 offset:8192
zm a[216:219],a[136:139],v[8:11],a[216:219]
zt a[166:167],v204 offset:12288
zl v211,s[24:27],s50 offen offset:3072 lds
zm a[212:215],a[136:139],v[4:7],a[212:215]
zm a[212:215],a[140:143],v[20:23],a[212:215]
zt a[168:169],v205 offset:0
zm a[208:211],a[140:143],v[16:19],a[208:211]
s_add_u32 s72, s90, 0
zt a[170:171],v205 offset:4096
zm a[208:211],a[136:139],v[0:3],a[208:211]
s_waitcnt lgkmcnt(12)
zl a[64:67],v214,s[40:43],s72 offen offset:0 sc1
zm a[224:227],a[144:147],v[0:3],a[224:227]
s_waitcnt lgkmcnt(10)
zt a[172:173],v205 offset:8192
zm a[224:227],a[148:151],v[16:19],a[224:227]
zt a[174:175],v205 offset:12288
zm a[228:231],a[148:151],v[20:23],a[228:231]
zm a[228:231],a[144:147],v[4:7],a[228:231]
zt a[176:177],v206 offset:0
zl a[68:71],v214,s[40:43],s72 offen offset:1024 sc1
zm a[232:235],a[144:147],v[8:11],a[232:235]
zm a[232:235],a[148:151],v[24:27],a[232:235]
zt a[178:179],v206 offset:4096
zm a[236:239],a[148:151],v[28:31],a[236:239]
zt a[180:181],v206 offset:8192
zm a[236:239],a[144:147],v[12:15],a[236:239]
s_waitcnt lgkmcnt(13)
zl a[72:75],v214,s[40:43],s72 offen offset:2048 sc1
zm a[252:255],a[152:155],v[12:15],a[252:255]
s_waitcnt lgkmcnt(11)
zt a[182:183],v206 offset:12288
zm a[252:255],a[156:159],v[28:31],a[252:255]
zt a[184:185],v207 offset:0
zm a[248:251],a[156:159],v[24:27],a[248:251]
zm a[248:251],a[152:155],v[8:11],a[248:251]
zt a[186:187],v207 offset:4096
zl a[76:79],v214,s[40:43],s72 offen offset:3072 sc1
zm a[244:247],a[152:155],v[4:7],a[244:247]
zt a[188:189],v207 offset:8192
zm a[244:247],a[156:159],v[20:23],a[244:247]
zm a[240:243],a[156:159],v[16:19],a[240:243]
s_add_u32 s72, s90, 0x1000
zt a[190:191],v207 offset:12288
zm a[240:243],a[152:155],v[0:3],a[240:243]
s_waitcnt lgkmcnt(14)
zl a[80:83],v214,s[40:43],s72 offen offset:0 sc1
zm a[0:3],a[160:163],v[0:3],a[0:3]
s_waitcnt lgkmcnt(12)
zm a[0:3],a[164:167],v[16:19],a[0:3]
zm a[4:7],a[164:167],v[20:23],a[4:7]
zl a[84:87],v214,s[40:43],s72 offen offset:1024 sc1
zm a[4:7],a[160:163],v[4:7],a[4:7]
zm a[8:11],a[160:163],v[8:11],a[8:11]
zm a[8:11],a[164:167],v[24:27],a[8:11]
zm a[12:15],a[164:167],v[28:31],a[12:15]
zl a[88:91],v214,s[40:43],s72 offen offset:2048 sc1
zm a[12:15],a[160:163],v[12:15],a[12:15]
s_waitcnt lgkmcnt(10)
zm a[28:31],a[168:171],v[12:15],a[28:31]
s_waitcnt lgkmcnt(8)
zm a[28:31],a[172:175],v[28:31],a[28:31]
zm a[24:27],a[172:175],v[24:27],a[24:27]
zl a[92:95],v214,s[40:43],s72 offen offset:3072 sc1
zm a[24:27],a[168:171],v[8:11],a[24:27]
zm a[20:23],a[168:171],v[4:7],a[20:23]
zm a[20:23],a[172:175],v[20:23],a[20:23]
s_add_u32 s72, s90, 0x2000
zm a[16:19],a[172:175],v[16:19],a[16:19]
zt a[128:129],v200 offset:16384
zl a[96:99],v214,s[40:43],s72 offen offset:0 sc1
zm a[16:19],a[168:171],v[0:3],a[16:19]
s_waitcnt lgkmcnt(7)
zt a[130:131],v200 offset:20480
zm a[32:35],a[176:179],v[0:3],a[32:35]
s_waitcnt lgkmcnt(6)
zm a[32:35],a[180:183],v[16:19],a[32:35]
zt a[132:133],v200 offset:24576
zm a[36:39],a[180:183],v[20:23],a[36:39]
zt a[134:135],v200 offset:28672
zl a[100:103],v214,s[40:43],s72 offen offset:1024 sc1
zm a[36:39],a[176:179],v[4:7],a[36:39]
zm a[40:43],a[176:179],v[8:11],a[40:43]
zt a[136:137],v201 offset:16384
zm a[40:43],a[180:183],v[24:27],a[40:43]
zt a[138:139],v201 offset:20480
zm a[44:47],a[180:183],v[28:31],a[44:47]
zl a[104:107],v214,s[40:43],s72 offen offset:2048 sc1
zm a[44:47],a[176:179],v[12:15],a[44:47]
s_waitcnt lgkmcnt(8)
zt a[140:141],v201 offset:24576
zm a[60:63],a[184:187],v[12:15],a[60:63]
s_waitcnt lgkmcnt(7)
zt a[142:143],v201 offset:28672
zm a[60:63],a[188:191],v[28:31],a[60:63]
zm a[56:59],a[188:191],v[24:27],a[56:59]
zt a[144:145],v202 offset:16384
zl a[108:111],v214,s[40:43],s72 offen offset:3072 sc1
zm a[56:59],a[184:187],v[8:11],a[56:59]
zm a[52:55],a[184:187],v[4:7],a[52:55]
zt a[146:147],v202 offset:20480
zm a[52:55],a[188:191],v[20:23],a[52:55]
zt a[148:149],v202 offset:24576
zm a[48:51],a[188:191],v[16:19],a[48:51]
zm a[48:51],a[184:187],v[0:3],a[48:51]
s_waitcnt lgkmcnt(9)
s_waitcnt vmcnt(16)
zt a[150:151],v202 offset:28672
zm a[192:195],a[128:131],v[64:67],a[192:195]
s_waitcnt lgkmcnt(8)
s_add_u32 s50, s85, 0x30000
zt a[152:153],v203 offset:16384
zm a[192:195],a[132:135],v[80:83],a[192:195]
s_add_u32 m0, s44, 0xc000
zm a[196:199],a[132:135],v[84:87],a[196:199]
zt a[154:155],v203 offset:20480
zl v208,s[24:27],s50 offen offset:0 lds
zm a[196:199],a[128:131],v[68:71],a[196:199]
zt a[156:157],v203 offset:24576
zl v209,s[24:27],s50 offen offset:1024 lds
zm a[200:203],a[128:131],v[72:75],a[200:203]
zm a[200:203],a[132:135],v[88:91],a[200:203]
zt a[158:159],v203 offset:28672
zm a[204:207],a[132:135],v[92:95],a[204:207]
zm a[204:207],a[128:131],v[76:79],a[204:207]
s_waitcnt lgkmcnt(10)
zt a[160:161],v204 offset:16384
zl v210,s[24:27],s50 offen offset:2048 lds
zm a[220:223],a[136:139],v[76:79],a[220:223]
s_waitcnt lgkmcnt(9)
zt a[162:163],v204 offset:20480
zm a[220:223],a[140:143],v[92:95],a[220:223]
zm a[216:219],a[140:143],v[88:91],a[216:219]
zt a[164:165],v204 offset:24576
zm a[216:219],a[136:139],v[72:75],a[216:219]
zt a[166:167],v204 offset:28672
zl v211,s[24:27],s50 offen offset:3072 lds
zm a[212:215],a[136:139],v[68:71],a[212:215]
zm a[212:215],a[140:143],v[84:87],a[212:215]
zt a[168:169],v205 offset:16384
zm a[208:211],a[140:143],v[80:83],a[208:211]
s_add_u32 s72, s90, 0x3000
zt a[170:171],v205 offset:20480
zm a[208:211],a[136:139],v[64:67],a[208:211]
s_waitcnt lgkmcnt(12)
zl a[112:115],v214,s[40:43],s72 offen offset:0 sc1
zm a[224:227],a[144:147],v[64:67],a[224:227]
s_waitcnt lgkmcnt(10)
zt a[172:173],v205 offset:24576
zm a[224:227],a[148:151],v[80:83],a[224:227]
zt a[174:175],v205 offset:28672
zm a[228:231],a[148:151],v[84:87],a[228:231]
zm a[228:231],a[144:147],v[68:71],a[228:231]
zt a[176:177],v206 offset:16384
zl a[116:119],v214,s[40:43],s72 offen offset:1024 sc1
zm a[232:235],a[144:147],v[72:75],a[232:235]
zm a[232:235],a[148:151],v[88:91],a[232:235]
zt a[178:179],v206 offset:20480
zm a[236:239],a[148:151],v[92:95],a[236:239]
zt a[180:181],v206 offset:24576
zm a[236:239],a[144:147],v[76:79],a[236:239]
s_waitcnt lgkmcnt(13)
zl a[120:123],v214,s[40:43],s72 offen offset:2048 sc1
zm a[252:255],a[152:155],v[76:79],a[252:255]
s_waitcnt lgkmcnt(11)
zt a[182:183],v206 offset:28672
zm a[252:255],a[156:159],v[92:95],a[252:255]
zt a[184:185],v207 offset:16384
zm a[248:251],a[156:159],v[88:91],a[248:251]
zm a[248:251],a[152:155],v[72:75],a[248:251]
zt a[186:187],v207 offset:20480
zl a[124:127],v214,s[40:43],s72 offen offset:3072 sc1
zm a[244:247],a[152:155],v[68:71],a[244:247]
zt a[188:189],v207 offset:24576
zm a[244:247],a[156:159],v[84:87],a[244:247]
zm a[240:243],a[156:159],v[80:83],a[240:243]
s_add_u32 s72, s91, 0
zt a[190:191],v207 offset:28672
zm a[240:243],a[152:155],v[64:67],a[240:243]
s_waitcnt lgkmcnt(14)
zl v[128:131],v214,s[40:43],s72 offen offset:0 sc1
zm a[0:3],a[160:163],v[64:67],a[0:3]
s_waitcnt lgkmcnt(12)
zm a[0:3],a[164:167],v[80:83],a[0:3]
zm a[4:7],a[164:167],v[84:87],a[4:7]
zl v[132:135],v214,s[40:43],s72 offen offset:1024 sc1
zm a[4:7],a[160:163],v[68:71],a[4:7]
zm a[8:11],a[160:163],v[72:75],a[8:11]
zm a[8:11],a[164:167],v[88:91],a[8:11]
zm a[12:15],a[164:167],v[92:95],a[12:15]
zl v[136:139],v214,s[40:43],s72 offen offset:2048 sc1
zm a[12:15],a[160:163],v[76:79],a[12:15]
s_waitcnt lgkmcnt(10)
zm a[28:31],a[168:171],v[76:79],a[28:31]
s_waitcnt lgkmcnt(8)
zm a[28:31],a[172:175],v[92:95],a[28:31]
zm a[24:27],a[172:175],v[88:91],a[24:27]
zl v[140:143],v214,s[40:43],s72 offen offset:3072 sc1
zm a[24:27],a[168:171],v[72:75],a[24:27]
s_waitcnt vmcnt(57) lgkmcnt(0)
zm a[20:23],a[168:171],v[68:71],a[20:23]
s_barrier
zm a[20:23],a[172:175],v[84:87],a[20:23]
s_add_u32 s72, s91, 0x1000
zm a[16:19],a[172:175],v[80:83],a[16:19]
zt a[128:129],v200 offset:32768
zl v[144:147],v214,s[40:43],s72 offen offset:0 sc1
zm a[16:19],a[168:171],v[64:67],a[16:19]
zt a[130:131],v200 offset:36864
zm a[32:35],a[176:179],v[64:67],a[32:35]
zm a[32:35],a[180:183],v[80:83],a[32:35]
zt a[132:133],v200 offset:40960
zm a[36:39],a[180:183],v[84:87],a[36:39]
zt a[134:135],v200 offset:45056
zl v[148:151],v214,s[40:43],s72 offen offset:1024 sc1
zm a[36:39],a[176:179],v[68:71],a[36:39]
zm a[40:43],a[176:179],v[72:75],a[40:43]
zt a[136:137],v200 offset:49152
zm a[40:43],a[180:183],v[88:91],a[40:43]
zt a[138:139],v200 offset:53248
zm a[44:47],a[180:183],v[92:95],a[44:47]
zl v[152:155],v214,s[40:43],s72 offen offset:2048 sc1
zm a[44:47],a[176:179],v[76:79],a[44:47]
zt a[140:141],v200 offset:57344
zm a[60:63],a[184:187],v[76:79],a[60:63]
zt a[142:143],v200 offset:61440
zm a[60:63],a[188:191],v[92:95],a[60:63]
zm a[56:59],a[188:191],v[88:91],a[56:59]
zt a[144:145],v201 offset:32768
zl v[156:159],v214,s[40:43],s72 offen offset:3072 sc1
zm a[56:59],a[184:187],v[72:75],a[56:59]
zm a[52:55],a[184:187],v[68:71],a[52:55]
zt a[146:147],v201 offset:36864
zm a[52:55],a[188:191],v[84:87],a[52:55]
zt a[148:149],v201 offset:40960
zm a[48:51],a[188:191],v[80:83],a[48:51]
zm a[48:51],a[184:187],v[64:67],a[48:51]
s_waitcnt lgkmcnt(9)
s_waitcnt vmcnt(8)
zt a[150:151],v201 offset:45056
zm a[192:195],a[128:131],a[64:67],a[192:195]
s_waitcnt lgkmcnt(8)
zt a[152:153],v201 offset:49152
buffer_load_dwordx2 v[226:227], v215, s[92:95], 0 offen offset:1024 sc1
zm a[192:195],a[132:135],a[80:83],a[192:195]
s_waitcnt lgkmcnt(7)
zm a[192:195],a[136:139],a[96:99],a[192:195]
s_waitcnt lgkmcnt(5)
s_add_u32 s50, s86, 0x20000
zt a[154:155],v201 offset:53248
zm a[192:195],a[140:143],a[112:115],a[192:195]
s_add_u32 m0, s45, 0
zt a[156:157],v201 offset:57344
zm a[196:199],a[140:143],a[116:119],a[196:199]
zl v208,s[24:27],s50 offen offset:0 lds
zm a[196:199],a[136:139],a[100:103],a[196:199]
zt a[158:159],v201 offset:61440
zm a[196:199],a[132:135],a[84:87],a[196:199]
zm a[196:199],a[128:131],a[68:71],a[196:199]
zt a[160:161],v202 offset:32768
zm a[200:203],a[128:131],a[72:75],a[200:203]
zt a[162:163],v202 offset:36864
zm a[200:203],a[132:135],a[88:91],a[200:203]
zl v209,s[24:27],s50 offen offset:1024 lds
zm a[200:203],a[136:139],a[104:107],a[200:203]
zt a[164:165],v202 offset:40960
zm a[200:203],a[140:143],a[120:123],a[200:203]
zt a[166:167],v202 offset:45056
zm a[204:207],a[140:143],a[124:127],a[204:207]
zm a[204:207],a[136:139],a[108:111],a[204:207]
zt a[168:169],v202 offset:49152
zm a[204:207],a[132:135],a[92:95],a[204:207]
zt a[170:171],v202 offset:53248
zl v210,s[24:27],s50 offen offset:2048 lds
zm a[204:207],a[128:131],a[76:79],a[204:207]
s_waitcnt lgkmcnt(12)
zm a[220:223],a[144:147],a[76:79],a[220:223]
s_waitcnt lgkmcnt(10)
zt a[172:173],v202 offset:57344
zm a[220:223],a[148:151],a[92:95],a[220:223]
s_waitcnt lgkmcnt(9)
zt a[174:175],v202 offset:61440
zm a[220:223],a[152:155],a[108:111],a[220:223]
s_waitcnt lgkmcnt(8)
zm a[220:223],a[156:159],a[124:127],a[220:223]
zt a[176:177],v203 offset:32768
zl v211,s[24:27],s50 offen offset:3072 lds
zm a[216:219],a[156:159],a[120:123],a[216:219]
zm a[216:219],a[152:155],a[104:107],a[216:219]
zt a[178:179],v203 offset:36864
zm a[216:219],a[148:151],a[88:91],a[216:219]
s_add_u32 s72, s91, 0x2000
zt a[180:181],v203 offset:40960
zm a[216:219],a[144:147],a[72:75],a[216:219]
zl v[160:163],v214,s[40:43],s72 offen offset:0 sc1
zm a[212:215],a[144:147],a[68:71],a[212:215]
zt a[182:183],v203 offset:45056
zm a[212:215],a[148:151],a[84:87],a[212:215]
zt a[184:185],v203 offset:49152
zm a[212:215],a[152:155],a[100:103],a[212:215]
zm a[212:215],a[156:159],a[116:119],a[212:215]
zt a[186:187],v203 offset:53248
zm a[208:211],a[156:159],a[112:115],a[208:211]
zt a[188:189],v203 offset:57344
zl v[164:167],v214,s[40:43],s72 offen offset:1024 sc1
zm a[208:211],a[152:155],a[96:99],a[208:211]
zm a[208:211],a[148:151],a[80:83],a[208:211]
zt a[190:191],v203 offset:61440
zm a[208:211],a[144:147],a[64:67],a[208:211]
s_waitcnt lgkmcnt(14)
zm a[224:227],a[160:163],a[64:67],a[224:227]
s_waitcnt lgkmcnt(12)
zm a[224:227],a[164:167],a[80:83],a[224:227]
s_waitcnt lgkmcnt(10)
zl v[168:171],v214,s[40:43],s72 offen offset:2048 sc1
zm a[224:227],a[168:171],a[96:99],a[224:227]
s_waitcnt lgkmcnt(8)
zm a[224:227],a[172:175],a[112:115],a[224:227]
zm a[228:231],a[172:175],a[116:119],a[228:231]
zm a[228:231],a[168:171],a[100:103],a[228:231]
zm a[228:231],a[164:167],a[84:87],a[228:231]
zl v[172:175],v214,s[40:43],s72 offen offset:3072 sc1
zm a[228:231],a[160:163],a[68:71],a[228:231]
zm a[232:235],a[160:163],a[72:75],a[232:235]
zm a[232:235],a[164:167],a[88:91],a[232:235]
s_add_u32 s72, s91, 0x3000
zm a[232:235],a[168:171],a[104:107],a[232:235]
zl v[176:179],v214,s[40:43],s72 offen offset:0 sc1
zm a[232:235],a[172:175],a[120:123],a[232:235]
zm a[236:239],a[172:175],a[124:127],a[236:239]
zm a[236:239],a[168:171],a[108:111],a[236:239]
zm a[236:239],a[164:167],a[92:95],a[236:239]
zt a[128:129],v204 offset:32768
zm a[236:239],a[160:163],a[76:79],a[236:239]
s_waitcnt lgkmcnt(7)
zt a[130:131],v204 offset:36864
zl v[180:183],v214,s[40:43],s72 offen offset:1024 sc1
zm a[252:255],a[176:179],a[76:79],a[252:255]
s_waitcnt lgkmcnt(6)
zm a[252:255],a[180:183],a[92:95],a[252:255]
s_waitcnt lgkmcnt(4)
zt a[132:133],v204 offset:40960
zm a[252:255],a[184:187],a[108:111],a[252:255]
s_waitcnt lgkmcnt(3)
zt a[134:135],v204 offset:45056
zm a[252:255],a[188:191],a[124:127],a[252:255]
zm a[248:251],a[188:191],a[120:123],a[248:251]
zt a[136:137],v204 offset:49152
zl v[184:187],v214,s[40:43],s72 offen offset:2048 sc1
zm a[248:251],a[184:187],a[104:107],a[248:251]
zt a[138:139],v204 offset:53248
zm a[248:251],a[180:183],a[88:91],a[248:251]
zm a[248:251],a[176:179],a[72:75],a[248:251]
zt a[140:141],v204 offset:57344
zm a[244:247],a[176:179],a[68:71],a[244:247]
zt a[142:143],v204 offset:61440
zm a[244:247],a[180:183],a[84:87],a[244:247]
zl v[188:191],v214,s[40:43],s72 offen offset:3072 sc1
zm a[244:247],a[184:187],a[100:103],a[244:247]
zt a[144:145],v205 offset:32768
s_waitcnt vmcnt(12)
L_pc1%=:
v_cmp_ne_u64 vcc, v[226:227], v[216:217]
s_nop 1
s_cbranch_vccnz L_ps1%=
L_pd1%=:
zm a[244:247],a[188:191],a[116:119],a[244:247]
zm a[240:243],a[188:191],a[112:115],a[240:243]
zt a[146:147],v205 offset:36864
zm a[240:243],a[184:187],a[96:99],a[240:243]
zt a[148:149],v205 offset:40960
zm a[240:243],a[180:183],a[80:83],a[240:243]
zm a[240:243],a[176:179],a[64:67],a[240:243]
s_waitcnt lgkmcnt(9)
zt a[150:151],v205 offset:45056
zm a[0:3],a[128:131],a[64:67],a[0:3]
s_waitcnt lgkmcnt(8)
s_add_u32 s50, s86, 0x30000
zt a[152:153],v205 offset:49152
zm a[0:3],a[132:135],a[80:83],a[0:3]
s_waitcnt lgkmcnt(7)
s_add_u32 m0, s45, 0x4000
zm a[0:3],a[136:139],a[96:99],a[0:3]
s_waitcnt lgkmcnt(5)
zt a[154:155],v205 offset:53248
zl v208,s[24:27],s50 offen offset:0 lds
zm a[0:3],a[140:143],a[112:115],a[0:3]
zt a[156:157],v205 offset:57344
zm a[4:7],a[140:143],a[116:119],a[4:7]
zl v209,s[24:27],s50 offen offset:1024 lds
zm a[4:7],a[136:139],a[100:103],a[4:7]
zt a[158:159],v205 offset:61440
zm a[4:7],a[132:135],a[84:87],a[4:7]
zm a[4:7],a[128:131],a[68:71],a[4:7]
zt a[160:161],v206 offset:32768
zm a[8:11],a[128:131],a[72:75],a[8:11]
zt a[162:163],v206 offset:36864
zl v210,s[24:27],s50 offen offset:2048 lds
zm a[8:11],a[132:135],a[88:91],a[8:11]
zm a[8:11],a[136:139],a[104:107],a[8:11]
zt a[164:165],v206 offset:40960
zm a[8:11],a[140:143],a[120:123],a[8:11]
zt a[166:167],v206 offset:45056
zm a[12:15],a[140:143],a[124:127],a[12:15]
zm a[12:15],a[136:139],a[108:111],a[12:15]
zt a[168:169],v206 offset:49152
zl v211,s[24:27],s50 offen offset:3072 lds
zm a[12:15],a[132:135],a[92:95],a[12:15]
zt a[170:171],v206 offset:53248
zm a[12:15],a[128:131],a[76:79],a[12:15]
s_waitcnt lgkmcnt(12)
zm a[28:31],a[144:147],a[76:79],a[28:31]
s_waitcnt lgkmcnt(10)
s_add_u32 s72, s89, 0x4000
zt a[172:173],v206 offset:57344
zm a[28:31],a[148:151],a[92:95],a[28:31]
s_waitcnt lgkmcnt(9)
zt a[174:175],v206 offset:61440
zl v[32:35],v214,s[40:43],s72 offen offset:0 sc1
zm a[28:31],a[152:155],a[108:111],a[28:31]
s_waitcnt lgkmcnt(8)
zm a[28:31],a[156:159],a[124:127],a[28:31]
zt a[176:177],v207 offset:32768
zm a[24:27],a[156:159],a[120:123],a[24:27]
zm a[24:27],a[152:155],a[104:107],a[24:27]
zt a[178:179],v207 offset:36864
zm a[24:27],a[148:151],a[88:91],a[24:27]
zt a[180:181],v207 offset:40960
zl v[36:39],v214,s[40:43],s72 offen offset:1024 sc1
zm a[24:27],a[144:147],a[72:75],a[24:27]
zm a[20:23],a[144:147],a[68:71],a[20:23]
zt a[182:183],v207 offset:45056
zm a[20:23],a[148:151],a[84:87],a[20:23]
zt a[184:185],v207 offset:49152
zm a[20:23],a[152:155],a[100:103],a[20:23]
zl v[40:43],v214,s[40:43],s72 offen offset:2048 sc1
zm a[20:23],a[156:159],a[116:119],a[20:23]
zt a[186:187],v207 offset:53248
zm a[16:19],a[156:159],a[112:115],a[16:19]
zt a[188:189],v207 offset:57344
zm a[16:19],a[152:155],a[96:99],a[16:19]
zm a[16:19],a[148:151],a[80:83],a[16:19]
zt a[190:191],v207 offset:61440
zm a[16:19],a[144:147],a[64:67],a[16:19]
s_waitcnt lgkmcnt(14)
zl v[44:47],v214,s[40:43],s72 offen offset:3072 sc1
zm a[32:35],a[160:163],a[64:67],a[32:35]
s_waitcnt lgkmcnt(12)
zm a[32:35],a[164:167],a[80:83],a[32:35]
s_waitcnt lgkmcnt(10)
zm a[32:35],a[168:171],a[96:99],a[32:35]
s_waitcnt lgkmcnt(8)
s_add_u32 s72, s89, 0x5000
zm a[32:35],a[172:175],a[112:115],a[32:35]
zl v[48:51],v214,s[40:43],s72 offen offset:0 sc1
v_xor_b32 v200, 0x10000, v200
zm a[36:39],a[172:175],a[116:119],a[36:39]
v_xor_b32 v201, 0x10000, v201
zm a[36:39],a[168:171],a[100:103],a[36:39]
v_xor_b32 v202, 0x10000, v202
zm a[36:39],a[164:167],a[84:87],a[36:39]
v_xor_b32 v203, 0x10000, v203
zm a[36:39],a[160:163],a[68:71],a[36:39]
zl v[52:55],v214,s[40:43],s72 offen offset:1024 sc1
v_xor_b32 v204, 0x10000, v204
zm a[40:43],a[160:163],a[72:75],a[40:43]
v_xor_b32 v205, 0x10000, v205
zm a[40:43],a[164:167],a[88:91],a[40:43]
v_xor_b32 v206, 0x10000, v206
zm a[40:43],a[168:171],a[104:107],a[40:43]
v_xor_b32 v207, 0x10000, v207
zm a[40:43],a[172:175],a[120:123],a[40:43]
s_waitcnt vmcnt(63) lgkmcnt(0)
zm a[44:47],a[172:175],a[124:127],a[44:47]
s_barrier
zm a[44:47],a[168:171],a[108:111],a[44:47]
zl v[56:59],v214,s[40:43],s72 offen offset:2048 sc1
zm a[44:47],a[164:167],a[92:95],a[44:47]
zt a[128:129],v200 offset:0
zm a[44:47],a[160:163],a[76:79],a[44:47]
zt a[130:131],v200 offset:4096
zm a[60:63],a[176:179],a[76:79],a[60:63]
zl v[60:63],v214,s[40:43],s72 offen offset:3072 sc1
zm a[60:63],a[180:183],a[92:95],a[60:63]
zt a[132:133],v200 offset:8192
zm a[60:63],a[184:187],a[108:111],a[60:63]
zt a[134:135],v200 offset:12288
zm a[60:63],a[188:191],a[124:127],a[60:63]
zm a[56:59],a[188:191],a[120:123],a[56:59]
s_add_u32 s72, s89, 0x6000
zt a[136:137],v200 offset:16384
zm a[56:59],a[184:187],a[104:107],a[56:59]
zt a[138:139],v200 offset:20480
zl v[96:99],v214,s[40:43],s72 offen offset:0 sc1
zm a[56:59],a[180:183],a[88:91],a[56:59]
zm a[56:59],a[176:179],a[72:75],a[56:59]
zt a[140:141],v200 offset:24576
zm a[52:55],a[176:179],a[68:71],a[52:55]
zt a[142:143],v200 offset:28672
zm a[52:55],a[180:183],a[84:87],a[52:55]
zl v[100:103],v214,s[40:43],s72 offen offset:1024 sc1
zm a[52:55],a[184:187],a[100:103],a[52:55]
zt a[144:145],v201 offset:0
zm a[52:55],a[188:191],a[116:119],a[52:55]
zm a[48:51],a[188:191],a[112:115],a[48:51]
zt a[146:147],v201 offset:4096
zm a[48:51],a[184:187],a[96:99],a[48:51]
zt a[148:149],v201 offset:8192
zm a[48:51],a[180:183],a[80:83],a[48:51]
zm a[48:51],a[176:179],a[64:67],a[48:51]
s_waitcnt lgkmcnt(9)
s_waitcnt vmcnt(14)
zt a[150:151],v201 offset:12288
zm a[192:195],a[128:131],v[128:131],a[192:195]
s_waitcnt lgkmcnt(8)
s_add_u32 s50, s87, 0x20000
zt a[152:153],v201 offset:16384
zm a[192:195],a[132:135],v[144:147],a[192:195]
s_waitcnt lgkmcnt(7)
s_add_u32 m0, s45, 0x8000
zm a[192:195],a[136:139],v[160:163],a[192:195]
s_waitcnt lgkmcnt(5)
zt a[154:155],v201 offset:20480
zl v208,s[24:27],s50 offen offset:0 lds
zm a[192:195],a[140:143],v[176:179],a[192:195]
zt a[156:157],v201 offset:24576
zm a[196:199],a[140:143],v[180:183],a[196:199]
zl v209,s[24:27],s50 offen offset:1024 lds
zm a[196:199],a[136:139],v[164:167],a[196:199]
zt a[158:159],v201 offset:28672
zm a[196:199],a[132:135],v[148:151],a[196:199]
zm a[196:199],a[128:131],v[132:135],a[196:199]
zt a[160:161],v202 offset:0
zm a[200:203],a[128:131],v[136:139],a[200:203]
zt a[162:163],v202 offset:4096
zl v210,s[24:27],s50 offen offset:2048 lds
zm a[200:203],a[132:135],v[152:155],a[200:203]
zm a[200:203],a[136:139],v[168:171],a[200:203]
zt a[164:165],v202 offset:8192
zm a[200:203],a[140:143],v[184:187],a[200:203]
zt a[166:167],v202 offset:12288
zm a[204:207],a[140:143],v[188:191],a[204:207]
zm a[204:207],a[136:139],v[172:175],a[204:207]
zt a[168:169],v202 offset:16384
zl v211,s[24:27],s50 offen offset:3072 lds
zm a[204:207],a[132:135],v[156:159],a[204:207]
zt a[170:171],v202 offset:20480
zm a[204:207],a[128:131],v[140:143],a[204:207]
s_waitcnt lgkmcnt(12)
zm a[220:223],a[144:147],v[140:143],a[220:223]
s_waitcnt lgkmcnt(10)
zt a[172:173],v202 offset:24576
zm a[220:223],a[148:151],v[156:159],a[220:223]
s_waitcnt lgkmcnt(9)
zt a[174:175],v202 offset:28672
zl v[104:107],v214,s[40:43],s72 offen offset:2048 sc1
zm a[220:223],a[152:155],v[172:175],a[220:223]
s_waitcnt lgkmcnt(8)
zm a[220:223],a[156:159],v[188:191],a[220:223]
zt a[176:177],v203 offset:0
zm a[216:219],a[156:159],v[184:187],a[216:219]
zm a[216:219],a[152:155],v[168:171],a[216:219]
zt a[178:179],v203 offset:4096
zm a[216:219],a[148:151],v[152:155],a[216:219]
zt a[180:181],v203 offset:8192
zl v[108:111],v214,s[40:43],s72 offen offset:3072 sc1
zm a[216:219],a[144:147],v[136:139],a[216:219]
zm a[212:215],a[144:147],v[132:135],a[212:215]
zt a[182:183],v203 offset:12288
zm a[212:215],a[148:151],v[148:151],a[212:215]
s_add_u32 s72, s89, 0x7000
zt a[184:185],v203 offset:16384
zm a[212:215],a[152:155],v[164:167],a[212:215]
zl v[112:115],v214,s[40:43],s72 offen offset:0 sc1
zm a[212:215],a[156:159],v[180:183],a[212:215]
zt a[186:187],v203 offset:20480
zm a[208:211],a[156:159],v[176:179],a[208:211]
zt a[188:189],v203 offset:24576
zm a[208:211],a[152:155],v[160:163],a[208:211]
zm a[208:211],a[148:151],v[144:147],a[208:211]
zt a[190:191],v203 offset:28672
zm a[208:211],a[144:147],v[128:131],a[208:211]
s_waitcnt lgkmcnt(14)
zl v[116:119],v214,s[40:43],s72 offen offset:1024 sc1
zm a[224:227],a[160:163],v[128:131],a[224:227]
s_waitcnt lgkmcnt(12)
zm a[224:227],a[164:167],v[144:147],a[224:227]
s_waitcnt lgkmcnt(10)
zm a[224:227],a[168:171],v[160:163],a[224:227]
s_waitcnt lgkmcnt(8)
zm a[224:227],a[172:175],v[176:179],a[224:227]
zl v[120:123],v214,s[40:43],s72 offen offset:2048 sc1
zm a[228:231],a[172:175],v[180:183],a[228:231]
zm a[228:231],a[168:171],v[164:167],a[228:231]
zm a[228:231],a[164:167],v[148:151],a[228:231]
zm a[228:231],a[160:163],v[132:135],a[228:231]
zl v[124:127],v214,s[40:43],s72 offen offset:3072 sc1
zm a[232:235],a[160:163],v[136:139],a[232:235]
zm a[232:235],a[164:167],v[152:155],a[232:235]
zm a[232:235],a[168:171],v[168:171],a[232:235]
zm a[232:235],a[172:175],v[184:187],a[232:235]
s_add_u32 s72, s90, 0x4000
zm a[236:239],a[172:175],v[188:191],a[236:239]
zl v[0:3],v214,s[40:43],s72 offen offset:0 sc1
zm a[236:239],a[168:171],v[172:175],a[236:239]
zm a[236:239],a[164:167],v[156:159],a[236:239]
zt a[128:129],v204 offset:0
zm a[236:239],a[160:163],v[140:143],a[236:239]
s_waitcnt lgkmcnt(7)
zt a[130:131],v204 offset:4096
zm a[252:255],a[176:179],v[140:143],a[252:255]
s_waitcnt lgkmcnt(6)
zl v[4:7],v214,s[40:43],s72 offen offset:1024 sc1
zm a[252:255],a[180:183],v[156:159],a[252:255]
s_waitcnt lgkmcnt(4)
zt a[132:133],v204 offset:8192
zm a[252:255],a[184:187],v[172:175],a[252:255]
s_waitcnt lgkmcnt(3)
zt a[134:135],v204 offset:12288
zm a[252:255],a[188:191],v[188:191],a[252:255]
zm a[248:251],a[188:191],v[184:187],a[248:251]
zt a[136:137],v204 offset:16384
zm a[248:251],a[184:187],v[168:171],a[248:251]
zt a[138:139],v204 offset:20480
zl v[8:11],v214,s[40:43],s72 offen offset:2048 sc1
zm a[248:251],a[180:183],v[152:155],a[248:251]
zm a[248:251],a[176:179],v[136:139],a[248:251]
zt a[140:141],v204 offset:24576
zm a[244:247],a[176:179],v[132:135],a[244:247]
zt a[142:143],v204 offset:28672
zm a[244:247],a[180:183],v[148:151],a[244:247]
zl v[12:15],v214,s[40:43],s72 offen offset:3072 sc1
zm a[244:247],a[184:187],v[164:167],a[244:247]
zt a[144:145],v205 offset:0
zm a[244:247],a[188:191],v[180:183],a[244:247]
zm a[240:243],a[188:191],v[176:179],a[240:243]
zt a[146:147],v205 offset:4096
zm a[240:243],a[184:187],v[160:163],a[240:243]
zt a[148:149],v205 offset:8192
zm a[240:243],a[180:183],v[144:147],a[240:243]
zm a[240:243],a[176:179],v[128:131],a[240:243]
s_waitcnt lgkmcnt(9)
zt a[150:151],v205 offset:12288
zm a[0:3],a[128:131],v[128:131],a[0:3]
s_waitcnt lgkmcnt(8)
s_add_u32 s50, s87, 0x30000
zt a[152:153],v205 offset:16384
zm a[0:3],a[132:135],v[144:147],a[0:3]
s_waitcnt lgkmcnt(7)
s_add_u32 m0, s45, 0xc000
zm a[0:3],a[136:139],v[160:163],a[0:3]
s_waitcnt lgkmcnt(5)
zt a[154:155],v205 offset:20480
zl v208,s[24:27],s50 offen offset:0 lds
zm a[0:3],a[140:143],v[176:179],a[0:3]
zt a[156:157],v205 offset:24576
zl v209,s[24:27],s50 offen offset:1024 lds
zm a[4:7],a[140:143],v[180:183],a[4:7]
zm a[4:7],a[136:139],v[164:167],a[4:7]
zt a[158:159],v205 offset:28672
zm a[4:7],a[132:135],v[148:151],a[4:7]
zl v210,s[24:27],s50 offen offset:2048 lds
zm a[4:7],a[128:131],v[132:135],a[4:7]
zt a[160:161],v206 offset:0
zm a[8:11],a[128:131],v[136:139],a[8:11]
zt a[162:163],v206 offset:4096
zm a[8:11],a[132:135],v[152:155],a[8:11]
zm a[8:11],a[136:139],v[168:171],a[8:11]
zt a[164:165],v206 offset:8192
zl v211,s[24:27],s50 offen offset:3072 lds
zm a[8:11],a[140:143],v[184:187],a[8:11]
zt a[166:167],v206 offset:12288
zm a[12:15],a[140:143],v[188:191],a[12:15]
s_add_u32 s72, s90, 0x5000
zm a[12:15],a[136:139],v[172:175],a[12:15]
zt a[168:169],v206 offset:16384
zl v[16:19],v214,s[40:43],s72 offen offset:0 sc1
zm a[12:15],a[132:135],v[156:159],a[12:15]
zt a[170:171],v206 offset:20480
zm a[12:15],a[128:131],v[140:143],a[12:15]
s_waitcnt lgkmcnt(12)
zm a[28:31],a[144:147],v[140:143],a[28:31]
s_waitcnt lgkmcnt(10)
zt a[172:173],v206 offset:24576
zm a[28:31],a[148:151],v[156:159],a[28:31]
s_waitcnt lgkmcnt(9)
zt a[174:175],v206 offset:28672
zl v[20:23],v214,s[40:43],s72 offen offset:1024 sc1
zm a[28:31],a[152:155],v[172:175],a[28:31]
s_waitcnt lgkmcnt(8)
zm a[28:31],a[156:159],v[188:191],a[28:31]
zt a[176:177],v207 offset:0
zm a[24:27],a[156:159],v[184:187],a[24:27]
zl v[24:27],v214,s[40:43],s72 offen offset:2048 sc1
zm a[24:27],a[152:155],v[168:171],a[24:27]
zt a[178:179],v207 offset:4096
zm a[24:27],a[148:151],v[152:155],a[24:27]
zt a[180:181],v207 offset:8192
zm a[24:27],a[144:147],v[136:139],a[24:27]
zm a[20:23],a[144:147],v[132:135],a[20:23]
zt a[182:183],v207 offset:12288
zl v[28:31],v214,s[40:43],s72 offen offset:3072 sc1
zm a[20:23],a[148:151],v[148:151],a[20:23]
zt a[184:185],v207 offset:16384
zm a[20:23],a[152:155],v[164:167],a[20:23]
s_add_u32 s72, s90, 0x6000
zm a[20:23],a[156:159],v[180:183],a[20:23]
zt a[186:187],v207 offset:20480
zl v[64:67],v214,s[40:43],s72 offen offset:0 sc1
zm a[16:19],a[156:159],v[176:179],a[16:19]
zt a[188:189],v207 offset:24576
zm a[16:19],a[152:155],v[160:163],a[16:19]
zm a[16:19],a[148:151],v[144:147],a[16:19]
zt a[190:191],v207 offset:28672
zm a[16:19],a[144:147],v[128:131],a[16:19]
s_waitcnt lgkmcnt(14)
zl v[68:71],v214,s[40:43],s72 offen offset:1024 sc1
zm a[32:35],a[160:163],v[128:131],a[32:35]
s_waitcnt lgkmcnt(12)
zm a[32:35],a[164:167],v[144:147],a[32:35]
s_waitcnt lgkmcnt(10)
zm a[32:35],a[168:171],v[160:163],a[32:35]
s_waitcnt lgkmcnt(8)
zl v[72:75],v214,s[40:43],s72 offen offset:2048 sc1
zm a[32:35],a[172:175],v[176:179],a[32:35]
zm a[36:39],a[172:175],v[180:183],a[36:39]
zm a[36:39],a[168:171],v[164:167],a[36:39]
zl v[76:79],v214,s[40:43],s72 offen offset:3072 sc1
zm a[36:39],a[164:167],v[148:151],a[36:39]
zm a[36:39],a[160:163],v[132:135],a[36:39]
zm a[40:43],a[160:163],v[136:139],a[40:43]
s_add_u32 s72, s90, 0x7000
zm a[40:43],a[164:167],v[152:155],a[40:43]
zl v[80:83],v214,s[40:43],s72 offen offset:0 sc1
zm a[40:43],a[168:171],v[168:171],a[40:43]
zm a[40:43],a[172:175],v[184:187],a[40:43]
s_waitcnt vmcnt(63) lgkmcnt(0)
zm a[44:47],a[172:175],v[188:191],a[44:47]
s_barrier
zm a[44:47],a[168:171],v[172:175],a[44:47]
zl v[84:87],v214,s[40:43],s72 offen offset:1024 sc1
zm a[44:47],a[164:167],v[156:159],a[44:47]
zt a[128:129],v200 offset:32768
zm a[44:47],a[160:163],v[140:143],a[44:47]
zt a[130:131],v200 offset:36864
zm a[60:63],a[176:179],v[140:143],a[60:63]
zl v[88:91],v214,s[40:43],s72 offen offset:2048 sc1
zm a[60:63],a[180:183],v[156:159],a[60:63]
zt a[132:133],v200 offset:40960
zm a[60:63],a[184:187],v[172:175],a[60:63]
zt a[134:135],v200 offset:45056
zm a[60:63],a[188:191],v[188:191],a[60:63]
zl v[92:95],v214,s[40:43],s72 offen offset:3072 sc1
zm a[56:59],a[188:191],v[184:187],a[56:59]
zt a[136:137],v200 offset:49152
zm a[56:59],a[184:187],v[168:171],a[56:59]
zt a[138:139],v200 offset:53248
zm a[56:59],a[180:183],v[152:155],a[56:59]
s_add_u32 s72, s91, 0x4000
zm a[56:59],a[176:179],v[136:139],a[56:59]
zt a[140:141],v200 offset:57344
zl a[64:67],v214,s[40:43],s72 offen offset:0 sc1
zm a[52:55],a[176:179],v[132:135],a[52:55]
zt a[142:143],v200 offset:61440
zm a[52:55],a[180:183],v[148:151],a[52:55]
zm a[52:55],a[184:187],v[164:167],a[52:55]
zt a[144:145],v201 offset:32768
zl a[68:71],v214,s[40:43],s72 offen offset:1024 sc1
zm a[52:55],a[188:191],v[180:183],a[52:55]
zm a[48:51],a[188:191],v[176:179],a[48:51]
zt a[146:147],v201 offset:36864
zm a[48:51],a[184:187],v[160:163],a[48:51]
zt a[148:149],v201 offset:40960
zm a[48:51],a[180:183],v[144:147],a[48:51]
zm a[48:51],a[176:179],v[128:131],a[48:51]
s_waitcnt lgkmcnt(9)
s_waitcnt vmcnt(22)
zt a[150:151],v201 offset:45056
zm a[192:195],a[128:131],v[32:35],a[192:195]
s_waitcnt lgkmcnt(8)
zt a[152:153],v201 offset:49152
zl a[72:75],v214,s[40:43],s72 offen offset:2048 sc1
zm a[192:195],a[132:135],v[48:51],a[192:195]
s_waitcnt lgkmcnt(7)
zm a[192:195],a[136:139],v[96:99],a[192:195]
s_waitcnt lgkmcnt(5)
zt a[154:155],v201 offset:53248
zm a[192:195],a[140:143],v[112:115],a[192:195]
zt a[156:157],v201 offset:57344
zm a[196:199],a[140:143],v[116:119],a[196:199]
zl a[76:79],v214,s[40:43],s72 offen offset:3072 sc1
zm a[196:199],a[136:139],v[100:103],a[196:199]
zt a[158:159],v201 offset:61440
zm a[196:199],a[132:135],v[52:55],a[196:199]
zm a[196:199],a[128:131],v[36:39],a[196:199]
s_add_u32 s72, s91, 0x5000
zt a[160:161],v202 offset:32768
zm a[200:203],a[128:131],v[40:43],a[200:203]
zt a[162:163],v202 offset:36864
zl a[80:83],v214,s[40:43],s72 offen offset:0 sc1
zm a[200:203],a[132:135],v[56:59],a[200:203]
zm a[200:203],a[136:139],v[104:107],a[200:203]
zt a[164:165],v202 offset:40960
zm a[200:203],a[140:143],v[120:123],a[200:203]
zt a[166:167],v202 offset:45056
zm a[204:207],a[140:143],v[124:127],a[204:207]
zm a[204:207],a[136:139],v[108:111],a[204:207]
zt a[168:169],v202 offset:49152
zl a[84:87],v214,s[40:43],s72 offen offset:1024 sc1
zm a[204:207],a[132:135],v[60:63],a[204:207]
zt a[170:171],v202 offset:53248
zm a[204:207],a[128:131],v[44:47],a[204:207]
s_waitcnt lgkmcnt(12)
zm a[220:223],a[144:147],v[44:47],a[220:223]
s_waitcnt lgkmcnt(10)
zt a[172:173],v202 offset:57344
zm a[220:223],a[148:151],v[60:63],a[220:223]
s_waitcnt lgkmcnt(9)
zt a[174:175],v202 offset:61440
zl a[88:91],v214,s[40:43],s72 offen offset:2048 sc1
zm a[220:223],a[152:155],v[108:111],a[220:223]
s_waitcnt lgkmcnt(8)
zm a[220:223],a[156:159],v[124:127],a[220:223]
zt a[176:177],v203 offset:32768
zm a[216:219],a[156:159],v[120:123],a[216:219]
zm a[216:219],a[152:155],v[104:107],a[216:219]
zt a[178:179],v203 offset:36864
zm a[216:219],a[148:151],v[56:59],a[216:219]
zt a[180:181],v203 offset:40960
zl a[92:95],v214,s[40:43],s72 offen offset:3072 sc1
zm a[216:219],a[144:147],v[40:43],a[216:219]
zm a[212:215],a[144:147],v[36:39],a[212:215]
zt a[182:183],v203 offset:45056
zm a[212:215],a[148:151],v[52:55],a[212:215]
s_add_u32 s72, s91, 0x6000
zt a[184:185],v203 offset:49152
zm a[212:215],a[152:155],v[100:103],a[212:215]
zl a[96:99],v214,s[40:43],s72 offen offset:0 sc1
zm a[212:215],a[156:159],v[116:119],a[212:215]
zt a[186:187],v203 offset:53248
zm a[208:211],a[156:159],v[112:115],a[208:211]
zt a[188:189],v203 offset:57344
zm a[208:211],a[152:155],v[96:99],a[208:211]
zm a[208:211],a[148:151],v[48:51],a[208:211]
zt a[190:191],v203 offset:61440
zm a[208:211],a[144:147],v[32:35],a[208:211]
s_waitcnt lgkmcnt(14)
zl a[100:103],v214,s[40:43],s72 offen offset:1024 sc1
zm a[224:227],a[160:163],v[32:35],a[224:227]
s_waitcnt lgkmcnt(12)
zm a[224:227],a[164:167],v[48:51],a[224:227]
s_waitcnt lgkmcnt(10)
zm a[224:227],a[168:171],v[96:99],a[224:227]
s_waitcnt lgkmcnt(8)
zm a[224:227],a[172:175],v[112:115],a[224:227]
zl a[104:107],v214,s[40:43],s72 offen offset:2048 sc1
zm a[228:231],a[172:175],v[116:119],a[228:231]
zm a[228:231],a[168:171],v[100:103],a[228:231]
zm a[228:231],a[164:167],v[52:55],a[228:231]
zm a[228:231],a[160:163],v[36:39],a[228:231]
zl a[108:111],v214,s[40:43],s72 offen offset:3072 sc1
zm a[232:235],a[160:163],v[40:43],a[232:235]
zm a[232:235],a[164:167],v[56:59],a[232:235]
zm a[232:235],a[168:171],v[104:107],a[232:235]
zm a[232:235],a[172:175],v[120:123],a[232:235]
s_add_u32 s72, s91, 0x7000
zm a[236:239],a[172:175],v[124:127],a[236:239]
zl a[112:115],v214,s[40:43],s72 offen offset:0 sc1
)"
R"(zm a[236:239],a[168:171],v[108:111],a[236:239]
zm a[236:239],a[164:167],v[60:63],a[236:239]
zt a[128:129],v204 offset:32768
zm a[236:239],a[160:163],v[44:47],a[236:239]
s_waitcnt lgkmcnt(7)
zt a[130:131],v204 offset:36864
zm a[252:255],a[176:179],v[44:47],a[252:255]
s_waitcnt lgkmcnt(6)
zl a[116:119],v214,s[40:43],s72 offen offset:1024 sc1
zm a[252:255],a[180:183],v[60:63],a[252:255]
s_waitcnt lgkmcnt(4)
zt a[132:133],v204 offset:40960
zm a[252:255],a[184:187],v[108:111],a[252:255]
s_waitcnt lgkmcnt(3)
zt a[134:135],v204 offset:45056
zm a[252:255],a[188:191],v[124:127],a[252:255]
zm a[248:251],a[188:191],v[120:123],a[248:251]
zt a[136:137],v204 offset:49152
zm a[248:251],a[184:187],v[104:107],a[248:251]
zt a[138:139],v204 offset:53248
zl a[120:123],v214,s[40:43],s72 offen offset:2048 sc1
zm a[248:251],a[180:183],v[56:59],a[248:251]
zm a[248:251],a[176:179],v[40:43],a[248:251]
zt a[140:141],v204 offset:57344
zm a[244:247],a[176:179],v[36:39],a[244:247]
zt a[142:143],v204 offset:61440
zm a[244:247],a[180:183],v[52:55],a[244:247]
zl a[124:127],v214,s[40:43],s72 offen offset:3072 sc1
zm a[244:247],a[184:187],v[100:103],a[244:247]
zt a[144:145],v205 offset:32768
zm a[244:247],a[188:191],v[116:119],a[244:247]
zm a[240:243],a[188:191],v[112:115],a[240:243]
zt a[146:147],v205 offset:36864
zm a[240:243],a[184:187],v[96:99],a[240:243]
zt a[148:149],v205 offset:40960
zm a[240:243],a[180:183],v[48:51],a[240:243]
zm a[240:243],a[176:179],v[32:35],a[240:243]
s_waitcnt lgkmcnt(9)
zt a[150:151],v205 offset:45056
zm a[0:3],a[128:131],v[32:35],a[0:3]
s_waitcnt lgkmcnt(8)
zt a[152:153],v205 offset:49152
zm a[0:3],a[132:135],v[48:51],a[0:3]
s_waitcnt lgkmcnt(7)
zm a[0:3],a[136:139],v[96:99],a[0:3]
s_waitcnt lgkmcnt(5)
zt a[154:155],v205 offset:53248
zm a[0:3],a[140:143],v[112:115],a[0:3]
zt a[156:157],v205 offset:57344
zm a[4:7],a[140:143],v[116:119],a[4:7]
zm a[4:7],a[136:139],v[100:103],a[4:7]
zt a[158:159],v205 offset:61440
zm a[4:7],a[132:135],v[52:55],a[4:7]
v_mov_b32 v196, v206
zm a[4:7],a[128:131],v[36:39],a[4:7]
v_mov_b32 v197, v207
zm a[8:11],a[128:131],v[40:43],a[8:11]
v_xor_b32 v200, 0x10000, v200
zm a[8:11],a[132:135],v[56:59],a[8:11]
v_xor_b32 v201, 0x10000, v201
zm a[8:11],a[136:139],v[104:107],a[8:11]
v_xor_b32 v202, 0x10000, v202
zm a[8:11],a[140:143],v[120:123],a[8:11]
v_xor_b32 v203, 0x10000, v203
zm a[12:15],a[140:143],v[124:127],a[12:15]
v_xor_b32 v204, 0x10000, v204
zm a[12:15],a[136:139],v[108:111],a[12:15]
v_xor_b32 v205, 0x10000, v205
zm a[12:15],a[132:135],v[60:63],a[12:15]
v_xor_b32 v206, 0x10000, v206
zm a[12:15],a[128:131],v[44:47],a[12:15]
s_waitcnt lgkmcnt(6)
v_xor_b32 v207, 0x10000, v207
zm a[28:31],a[144:147],v[44:47],a[28:31]
s_waitcnt vmcnt(28) lgkmcnt(0)
zm a[28:31],a[148:151],v[60:63],a[28:31]
s_barrier
zm a[28:31],a[152:155],v[108:111],a[28:31]
zt a[160:161],v200 offset:0
zt a[162:163],v200 offset:4096
zm a[28:31],a[156:159],v[124:127],a[28:31]
zt a[164:165],v200 offset:8192
zm a[24:27],a[156:159],v[120:123],a[24:27]
zt a[166:167],v200 offset:12288
zm a[24:27],a[152:155],v[104:107],a[24:27]
zt a[168:169],v200 offset:16384
zm a[24:27],a[148:151],v[56:59],a[24:27]
zt a[170:171],v200 offset:20480
zt a[172:173],v200 offset:24576
zm a[24:27],a[144:147],v[40:43],a[24:27]
zt a[174:175],v200 offset:28672
zm a[20:23],a[144:147],v[36:39],a[20:23]
zt a[176:177],v200 offset:32768
zm a[20:23],a[148:151],v[52:55],a[20:23]
zt a[178:179],v200 offset:36864
zm a[20:23],a[152:155],v[100:103],a[20:23]
zt a[180:181],v200 offset:40960
zt a[182:183],v200 offset:45056
zm a[20:23],a[156:159],v[116:119],a[20:23]
zt a[184:185],v200 offset:49152
zm a[16:19],a[156:159],v[112:115],a[16:19]
zt a[186:187],v200 offset:53248
zm a[16:19],a[152:155],v[96:99],a[16:19]
zt a[188:189],v200 offset:57344
zm a[16:19],a[148:151],v[48:51],a[16:19]
zt a[190:191],v200 offset:61440
zm a[16:19],a[144:147],v[32:35],a[16:19]
s_waitcnt lgkmcnt(14)
s_waitcnt vmcnt(16)
zm a[192:195],a[160:163],v[0:3],a[192:195]
s_waitcnt lgkmcnt(12)
zt a[128:129],v201 offset:0
zm a[192:195],a[164:167],v[16:19],a[192:195]
s_waitcnt lgkmcnt(11)
zt a[130:131],v201 offset:4096
zm a[192:195],a[168:171],v[64:67],a[192:195]
s_waitcnt lgkmcnt(10)
s_waitcnt vmcnt(0)
zm a[192:195],a[172:175],v[80:83],a[192:195]
s_waitcnt lgkmcnt(8)
zt a[132:133],v201 offset:8192
zm a[192:195],a[176:179],a[64:67],a[192:195]
s_waitcnt lgkmcnt(7)
zt a[134:135],v201 offset:12288
v_lshrrev_b32 v236, 4, v214
zm a[192:195],a[180:183],a[80:83],a[192:195]
s_waitcnt lgkmcnt(6)
v_min_u32 v236, 2, v236
zm a[192:195],a[184:187],a[96:99],a[192:195]
s_waitcnt lgkmcnt(4)
zt a[136:137],v201 offset:16384
zm a[192:195],a[188:191],a[112:115],a[192:195]
s_add_u32 s74, s77, 0x1000
zt a[138:139],v201 offset:20480
zm a[196:199],a[188:191],a[116:119],a[196:199]
v_lshlrev_b32 v236, 10, v236
zm a[196:199],a[184:187],a[100:103],a[196:199]
zt a[140:141],v201 offset:24576
zm a[196:199],a[180:183],a[84:87],a[196:199]
zt a[142:143],v201 offset:28672
v_add_u32 v215, s74, v236
zm a[196:199],a[176:179],a[68:71],a[196:199]
s_mov_b32 s51, 7
zm a[196:199],a[172:175],v[84:87],a[196:199]
zt a[144:145],v201 offset:32768
zm a[196:199],a[168:171],v[68:71],a[196:199]
zt a[146:147],v201 offset:36864
v_mov_b32 v242, 0xffff0000
zm a[196:199],a[164:167],v[20:23],a[196:199]
v_bfe_u32 v237, v214, 4, 4
zm a[196:199],a[160:163],v[4:7],a[196:199]
zt a[148:149],v201 offset:40960
zm a[200:203],a[160:163],v[8:11],a[200:203]
zt a[150:151],v201 offset:45056
v_lshlrev_b32 v237, 11, v237
zm a[200:203],a[164:167],v[24:27],a[200:203]
s_lshl_b32 s74, s64, 17
zm a[200:203],a[168:171],v[72:75],a[200:203]
zt a[152:153],v201 offset:49152
zm a[200:203],a[172:175],v[88:91],a[200:203]
zt a[154:155],v201 offset:53248
v_add_u32 v237, s74, v237
zm a[200:203],a[176:179],a[72:75],a[200:203]
v_bfe_u32 v238, v214, 8, 1
zm a[200:203],a[180:183],a[88:91],a[200:203]
zt a[156:157],v201 offset:57344
zm a[200:203],a[184:187],a[104:107],a[200:203]
zt a[158:159],v201 offset:61440
v_lshl_add_u32 v237, v238, 5, v237
zm a[200:203],a[188:191],a[120:123],a[200:203]
v_lshrrev_b32 v238, 9, v214
zm a[204:207],a[188:191],a[124:127],a[204:207]
zm a[204:207],a[184:187],a[108:111],a[204:207]
v_lshl_add_u32 v237, v238, 4, v237
zm a[204:207],a[180:183],a[92:95],a[204:207]
v_add_u32 v238, 0x8000, v237
zm a[204:207],a[176:179],a[76:79],a[204:207]
zm a[204:207],a[172:175],v[92:95],a[204:207]
v_add_u32 v239, 0x10000, v237
zm a[204:207],a[168:171],v[76:79],a[204:207]
v_add_u32 v240, 0x18000, v237
zm a[204:207],a[164:167],v[28:31],a[204:207]
zm a[204:207],a[160:163],v[12:15],a[204:207]
s_waitcnt lgkmcnt(14)
zm a[208:211],a[128:131],v[0:3],a[208:211]
s_waitcnt lgkmcnt(12)
s_add_u32 s54, s75, 1
zt a[160:161],v202 offset:0
zm a[208:211],a[132:135],v[16:19],a[208:211]
s_waitcnt lgkmcnt(11)
s_and_b32 s54, s54, 3
zt a[162:163],v202 offset:4096
zm a[208:211],a[136:139],v[64:67],a[208:211]
s_waitcnt lgkmcnt(10)
s_lshl_b32 s54, s54, 2
zr v128,a192
zr v129,a193
zm a[208:211],a[140:143],v[80:83],a[208:211]
s_waitcnt lgkmcnt(8)
s_add_u32 s54, s54, s75
zt a[164:165],v202 offset:8192
zr v130,a194
zm a[208:211],a[144:147],a[64:67],a[208:211]
s_waitcnt lgkmcnt(7)
s_lshl_b32 s72, s54, 15
zt a[166:167],v202 offset:12288
zr v131,a195
zm a[208:211],a[148:151],a[80:83],a[208:211]
s_waitcnt lgkmcnt(6)
zr v132,a196
zr v133,a197
zm a[208:211],a[152:155],a[96:99],a[208:211]
s_waitcnt lgkmcnt(4)
s_add_u32 s74, s75, 3
zt a[168:169],v202 offset:16384
zr v134,a198
zm a[208:211],a[156:159],a[112:115],a[208:211]
s_and_b32 s74, s74, 3
zt a[170:171],v202 offset:20480
zr v135,a199
zm a[212:215],a[156:159],a[116:119],a[212:215]
s_lshl_b32 s84, s75, 2
zc v144,v128,v129
zc v145,v130,v131
zm a[212:215],a[152:155],a[100:103],a[212:215]
s_add_u32 s84, s84, s74
zt a[172:173],v202 offset:24576
zc v146,v132,v133
zm a[212:215],a[148:151],a[84:87],a[212:215]
s_lshl_b32 s84, s84, 15
zt a[174:175],v202 offset:28672
zc v147,v134,v135
zm a[212:215],a[144:147],a[68:71],a[212:215]
s_add_u32 s88, s77, 0x100
buffer_store_dwordx4 v[144:147], v214, s[96:99], s72 offen offset:0 sc1
zr v136,a200
zm a[212:215],a[140:143],v[84:87],a[212:215]
s_and_b32 s88, s88, 0x3ff
zt a[176:177],v202 offset:32768
zr v137,a201
zm a[212:215],a[136:139],v[68:71],a[212:215]
s_add_u32 s88, s88, 0x1000
zt a[178:179],v202 offset:36864
zr v138,a202
zm a[212:215],a[132:135],v[20:23],a[212:215]
s_add_u32 s74, s75, 2
zr v139,a203
zr v140,a204
zm a[212:215],a[128:131],v[4:7],a[212:215]
s_and_b32 s74, s74, 3
zt a[180:181],v202 offset:40960
zr v141,a205
zm a[216:219],a[128:131],v[8:11],a[216:219]
s_lshl_b32 s85, s75, 2
zt a[182:183],v202 offset:45056
zr v142,a206
zm a[216:219],a[132:135],v[24:27],a[216:219]
s_add_u32 s85, s85, s74
zr v143,a207
zc v148,v136,v137
zm a[216:219],a[136:139],v[72:75],a[216:219]
s_lshl_b32 s85, s85, 15
zt a[184:185],v202 offset:49152
zc v149,v138,v139
zm a[216:219],a[140:143],v[88:91],a[216:219]
s_add_u32 s89, s77, 0x200
zt a[186:187],v202 offset:53248
zc v150,v140,v141
zm a[216:219],a[144:147],a[72:75],a[216:219]
s_and_b32 s89, s89, 0x3ff
zc v151,v142,v143
zm a[216:219],a[148:151],a[88:91],a[216:219]
s_add_u32 s89, s89, 0x1400
zt a[188:189],v202 offset:57344
buffer_store_dwordx4 v[148:151], v214, s[96:99], s72 offen offset:1024 sc1
zm a[216:219],a[152:155],a[104:107],a[216:219]
s_add_u32 s74, s75, 1
zt a[190:191],v202 offset:61440
zm a[216:219],a[156:159],a[120:123],a[216:219]
s_and_b32 s74, s74, 3
zm a[220:223],a[156:159],a[124:127],a[220:223]
s_lshl_b32 s86, s75, 2
zm a[220:223],a[152:155],a[108:111],a[220:223]
s_add_u32 s86, s86, s74
zm a[220:223],a[148:151],a[92:95],a[220:223]
s_lshl_b32 s86, s86, 15
zm a[220:223],a[144:147],a[76:79],a[220:223]
s_add_u32 s90, s77, 0x300
zm a[220:223],a[140:143],v[92:95],a[220:223]
s_and_b32 s90, s90, 0x3ff
zm a[220:223],a[136:139],v[76:79],a[220:223]
s_add_u32 s90, s90, 0x1800
zm a[220:223],a[132:135],v[28:31],a[220:223]
zm a[220:223],a[128:131],v[12:15],a[220:223]
s_waitcnt lgkmcnt(14)
zm a[224:227],a[160:163],v[0:3],a[224:227]
s_waitcnt lgkmcnt(12)
zt a[128:129],v203 offset:0
zm a[224:227],a[164:167],v[16:19],a[224:227]
s_waitcnt lgkmcnt(11)
zt a[130:131],v203 offset:4096
zm a[224:227],a[168:171],v[64:67],a[224:227]
s_waitcnt lgkmcnt(10)
zr v128,a208
zr v129,a209
zm a[224:227],a[172:175],v[80:83],a[224:227]
s_waitcnt lgkmcnt(8)
zt a[132:133],v203 offset:8192
zr v130,a210
zm a[224:227],a[176:179],a[64:67],a[224:227]
s_waitcnt lgkmcnt(7)
zt a[134:135],v203 offset:12288
zr v131,a211
zm a[224:227],a[180:183],a[80:83],a[224:227]
s_waitcnt lgkmcnt(6)
zr v132,a212
zr v133,a213
zm a[224:227],a[184:187],a[96:99],a[224:227]
s_waitcnt lgkmcnt(4)
zt a[136:137],v203 offset:16384
zr v134,a214
zm a[224:227],a[188:191],a[112:115],a[224:227]
zt a[138:139],v203 offset:20480
zr v135,a215
zm a[228:231],a[188:191],a[116:119],a[228:231]
zc v152,v128,v129
zc v153,v130,v131
zm a[228:231],a[184:187],a[100:103],a[228:231]
zt a[140:141],v203 offset:24576
zc v154,v132,v133
zm a[228:231],a[180:183],a[84:87],a[228:231]
zt a[142:143],v203 offset:28672
zc v155,v134,v135
zm a[228:231],a[176:179],a[68:71],a[228:231]
buffer_store_dwordx4 v[152:155], v214, s[96:99], s72 offen offset:2048 sc1
zr v136,a216
zm a[228:231],a[172:175],v[84:87],a[228:231]
zt a[144:145],v203 offset:32768
zr v137,a217
zm a[228:231],a[168:171],v[68:71],a[228:231]
zt a[146:147],v203 offset:36864
zr v138,a218
zm a[228:231],a[164:167],v[20:23],a[228:231]
zr v139,a219
zr v140,a220
zm a[228:231],a[160:163],v[4:7],a[228:231]
zt a[148:149],v203 offset:40960
zr v141,a221
zm a[232:235],a[160:163],v[8:11],a[232:235]
zt a[150:151],v203 offset:45056
zr v142,a222
zm a[232:235],a[164:167],v[24:27],a[232:235]
zr v143,a223
zc v156,v136,v137
zm a[232:235],a[168:171],v[72:75],a[232:235]
zt a[152:153],v203 offset:49152
zc v157,v138,v139
zm a[232:235],a[172:175],v[88:91],a[232:235]
zt a[154:155],v203 offset:53248
zc v158,v140,v141
zm a[232:235],a[176:179],a[72:75],a[232:235]
zc v159,v142,v143
zm a[232:235],a[180:183],a[88:91],a[232:235]
zt a[156:157],v203 offset:57344
buffer_store_dwordx4 v[156:159], v214, s[96:99], s72 offen offset:3072 sc1
zm a[232:235],a[184:187],a[104:107],a[232:235]
zt a[158:159],v203 offset:61440
zm a[232:235],a[188:191],a[120:123],a[232:235]
zm a[236:239],a[188:191],a[124:127],a[236:239]
zm a[236:239],a[184:187],a[108:111],a[236:239]
zm a[236:239],a[180:183],a[92:95],a[236:239]
zm a[236:239],a[176:179],a[76:79],a[236:239]
zm a[236:239],a[172:175],v[92:95],a[236:239]
zm a[236:239],a[168:171],v[76:79],a[236:239]
zm a[236:239],a[164:167],v[28:31],a[236:239]
zm a[236:239],a[160:163],v[12:15],a[236:239]
s_waitcnt lgkmcnt(14)
zm a[240:243],a[128:131],v[0:3],a[240:243]
s_waitcnt lgkmcnt(12)
s_add_u32 s54, s75, 2
zt a[160:161],v204 offset:0
zm a[240:243],a[132:135],v[16:19],a[240:243]
s_waitcnt lgkmcnt(11)
s_and_b32 s54, s54, 3
zt a[162:163],v204 offset:4096
zm a[240:243],a[136:139],v[64:67],a[240:243]
s_waitcnt lgkmcnt(10)
s_lshl_b32 s54, s54, 2
zr v128,a224
zr v129,a225
zm a[240:243],a[140:143],v[80:83],a[240:243]
s_waitcnt lgkmcnt(8)
s_add_u32 s54, s54, s75
zt a[164:165],v204 offset:8192
zr v130,a226
zm a[240:243],a[144:147],a[64:67],a[240:243]
s_waitcnt lgkmcnt(7)
s_lshl_b32 s72, s54, 15
zt a[166:167],v204 offset:12288
zr v131,a227
zm a[240:243],a[148:151],a[80:83],a[240:243]
s_waitcnt lgkmcnt(6)
zr v132,a228
zr v133,a229
zm a[240:243],a[152:155],a[96:99],a[240:243]
s_waitcnt lgkmcnt(4)
zt a[168:169],v204 offset:16384
zr v134,a230
zm a[240:243],a[156:159],a[112:115],a[240:243]
zt a[170:171],v204 offset:20480
zr v135,a231
zm a[244:247],a[156:159],a[116:119],a[244:247]
zc v144,v128,v129
zc v145,v130,v131
zm a[244:247],a[152:155],a[100:103],a[244:247]
zt a[172:173],v204 offset:24576
zc v146,v132,v133
zm a[244:247],a[148:151],a[84:87],a[244:247]
zt a[174:175],v204 offset:28672
zc v147,v134,v135
zm a[244:247],a[144:147],a[68:71],a[244:247]
buffer_store_dwordx4 v[144:147], v214, s[96:99], s72 offen offset:0 sc1
zr v136,a232
zm a[244:247],a[140:143],v[84:87],a[244:247]
zt a[176:177],v204 offset:32768
zr v137,a233
zm a[244:247],a[136:139],v[68:71],a[244:247]
zt a[178:179],v204 offset:36864
zr v138,a234
zm a[244:247],a[132:135],v[20:23],a[244:247]
zr v139,a235
zr v140,a236
zm a[244:247],a[128:131],v[4:7],a[244:247]
zt a[180:181],v204 offset:40960
zr v141,a237
zm a[248:251],a[128:131],v[8:11],a[248:251]
zt a[182:183],v204 offset:45056
zr v142,a238
zm a[248:251],a[132:135],v[24:27],a[248:251]
zr v143,a239
zc v148,v136,v137
zm a[248:251],a[136:139],v[72:75],a[248:251]
zt a[184:185],v204 offset:49152
zc v149,v138,v139
zm a[248:251],a[140:143],v[88:91],a[248:251]
zt a[186:187],v204 offset:53248
zc v150,v140,v141
zm a[248:251],a[144:147],a[72:75],a[248:251]
zc v151,v142,v143
zm a[248:251],a[148:151],a[88:91],a[248:251]
zt a[188:189],v204 offset:57344
buffer_store_dwordx4 v[148:151], v214, s[96:99], s72 offen offset:1024 sc1
zm a[248:251],a[152:155],a[104:107],a[248:251]
zt a[190:191],v204 offset:61440
zm a[248:251],a[156:159],a[120:123],a[248:251]
zm a[252:255],a[156:159],a[124:127],a[252:255]
zm a[252:255],a[152:155],a[108:111],a[252:255]
zm a[252:255],a[148:151],a[92:95],a[252:255]
zm a[252:255],a[144:147],a[76:79],a[252:255]
zm a[252:255],a[140:143],v[92:95],a[252:255]
zm a[252:255],a[136:139],v[76:79],a[252:255]
zm a[252:255],a[132:135],v[28:31],a[252:255]
zm a[252:255],a[128:131],v[12:15],a[252:255]
s_waitcnt lgkmcnt(14)
zm a[0:3],a[160:163],v[0:3],a[0:3]
s_waitcnt lgkmcnt(12)
zt a[128:129],v205 offset:0
zm a[0:3],a[164:167],v[16:19],a[0:3]
s_waitcnt lgkmcnt(11)
zt a[130:131],v205 offset:4096
zm a[0:3],a[168:171],v[64:67],a[0:3]
s_waitcnt lgkmcnt(10)
zr v128,a240
zr v129,a241
zm a[0:3],a[172:175],v[80:83],a[0:3]
s_waitcnt lgkmcnt(8)
zt a[132:133],v205 offset:8192
zr v130,a242
zm a[0:3],a[176:179],a[64:67],a[0:3]
s_waitcnt lgkmcnt(7)
zt a[134:135],v205 offset:12288
zr v131,a243
zm a[0:3],a[180:183],a[80:83],a[0:3]
s_waitcnt lgkmcnt(6)
zr v132,a244
zr v133,a245
zm a[0:3],a[184:187],a[96:99],a[0:3]
s_waitcnt lgkmcnt(4)
zt a[136:137],v205 offset:16384
zr v134,a246
zm a[0:3],a[188:191],a[112:115],a[0:3]
zt a[138:139],v205 offset:20480
zr v135,a247
zm a[4:7],a[188:191],a[116:119],a[4:7]
zc v152,v128,v129
zc v153,v130,v131
zm a[4:7],a[184:187],a[100:103],a[4:7]
zt a[140:141],v205 offset:24576
zc v154,v132,v133
zm a[4:7],a[180:183],a[84:87],a[4:7]
zt a[142:143],v205 offset:28672
zc v155,v134,v135
zm a[4:7],a[176:179],a[68:71],a[4:7]
buffer_store_dwordx4 v[152:155], v214, s[96:99], s72 offen offset:2048 sc1
zr v136,a248
zm a[4:7],a[172:175],v[84:87],a[4:7]
zt a[144:145],v205 offset:32768
zr v137,a249
zm a[4:7],a[168:171],v[68:71],a[4:7]
zt a[146:147],v205 offset:36864
zr v138,a250
zm a[4:7],a[164:167],v[20:23],a[4:7]
zr v139,a251
zr v140,a252
zm a[4:7],a[160:163],v[4:7],a[4:7]
zt a[148:149],v205 offset:40960
zr v141,a253
zm a[8:11],a[160:163],v[8:11],a[8:11]
zt a[150:151],v205 offset:45056
zr v142,a254
zm a[8:11],a[164:167],v[24:27],a[8:11]
zr v143,a255
zc v156,v136,v137
zm a[8:11],a[168:171],v[72:75],a[8:11]
zt a[152:153],v205 offset:49152
zc v157,v138,v139
zm a[8:11],a[172:175],v[88:91],a[8:11]
zt a[154:155],v205 offset:53248
zc v158,v140,v141
zm a[8:11],a[176:179],a[72:75],a[8:11]
zc v159,v142,v143
zm a[8:11],a[180:183],a[88:91],a[8:11]
zt a[156:157],v205 offset:57344
buffer_store_dwordx4 v[156:159], v214, s[96:99], s72 offen offset:3072 sc1
zm a[8:11],a[184:187],a[104:107],a[8:11]
zt a[158:159],v205 offset:61440
zm a[8:11],a[188:191],a[120:123],a[8:11]
s_waitcnt vmcnt(4)
s_mov_b64 exec, 1
buffer_store_dwordx2 v[216:217], off, s[92:95], s88 sc1
s_mov_b64 exec, -1
zm a[12:15],a[188:191],a[124:127],a[12:15]
zm a[12:15],a[184:187],a[108:111],a[12:15]
zm a[12:15],a[180:183],a[92:95],a[12:15]
zm a[12:15],a[176:179],a[76:79],a[12:15]
zm a[12:15],a[172:175],v[92:95],a[12:15]
zm a[12:15],a[168:171],v[76:79],a[12:15]
zm a[12:15],a[164:167],v[28:31],a[12:15]
zm a[12:15],a[160:163],v[12:15],a[12:15]
s_waitcnt lgkmcnt(14)
zm a[16:19],a[128:131],v[0:3],a[16:19]
s_waitcnt lgkmcnt(12)
s_add_u32 s54, s75, 3
zt a[160:161],v206 offset:0
zm a[16:19],a[132:135],v[16:19],a[16:19]
s_waitcnt lgkmcnt(11)
s_and_b32 s54, s54, 3
zt a[162:163],v206 offset:4096
zm a[16:19],a[136:139],v[64:67],a[16:19]
s_waitcnt lgkmcnt(10)
s_lshl_b32 s54, s54, 2
zr v128,a0
zr v129,a1
zm a[16:19],a[140:143],v[80:83],a[16:19]
s_waitcnt lgkmcnt(8)
s_add_u32 s54, s54, s75
zt a[164:165],v206 offset:8192
zr v130,a2
zm a[16:19],a[144:147],a[64:67],a[16:19]
s_waitcnt lgkmcnt(7)
s_lshl_b32 s72, s54, 15
zt a[166:167],v206 offset:12288
zr v131,a3
zm a[16:19],a[148:151],a[80:83],a[16:19]
s_waitcnt lgkmcnt(6)
zr v132,a4
zr v133,a5
zm a[16:19],a[152:155],a[96:99],a[16:19]
s_waitcnt lgkmcnt(4)
zt a[168:169],v206 offset:16384
zr v134,a6
zm a[16:19],a[156:159],a[112:115],a[16:19]
zt a[170:171],v206 offset:20480
zr v135,a7
zm a[20:23],a[156:159],a[116:119],a[20:23]
zc v144,v128,v129
zc v145,v130,v131
zm a[20:23],a[152:155],a[100:103],a[20:23]
zt a[172:173],v206 offset:24576
zc v146,v132,v133
zm a[20:23],a[148:151],a[84:87],a[20:23]
zt a[174:175],v206 offset:28672
zc v147,v134,v135
zm a[20:23],a[144:147],a[68:71],a[20:23]
buffer_store_dwordx4 v[144:147], v214, s[96:99], s72 offen offset:0 sc1
zr v136,a8
zm a[20:23],a[140:143],v[84:87],a[20:23]
zt a[176:177],v206 offset:32768
zr v137,a9
zm a[20:23],a[136:139],v[68:71],a[20:23]
zt a[178:179],v206 offset:36864
zr v138,a10
zm a[20:23],a[132:135],v[20:23],a[20:23]
zr v139,a11
zr v140,a12
zm a[20:23],a[128:131],v[4:7],a[20:23]
zt a[180:181],v206 offset:40960
zr v141,a13
zm a[24:27],a[128:131],v[8:11],a[24:27]
zt a[182:183],v206 offset:45056
zr v142,a14
zm a[24:27],a[132:135],v[24:27],a[24:27]
zr v143,a15
zc v148,v136,v137
zm a[24:27],a[136:139],v[72:75],a[24:27]
zt a[184:185],v206 offset:49152
zc v149,v138,v139
zm a[24:27],a[140:143],v[88:91],a[24:27]
zt a[186:187],v206 offset:53248
zc v150,v140,v141
zm a[24:27],a[144:147],a[72:75],a[24:27]
zc v151,v142,v143
zm a[24:27],a[148:151],a[88:91],a[24:27]
zt a[188:189],v206 offset:57344
buffer_store_dwordx4 v[148:151], v214, s[96:99], s72 offen offset:1024 sc1
zm a[24:27],a[152:155],a[104:107],a[24:27]
zt a[190:191],v206 offset:61440
zm a[24:27],a[156:159],a[120:123],a[24:27]
zm a[28:31],a[156:159],a[124:127],a[28:31]
zr v128,a16
zr v129,a17
zm a[28:31],a[152:155],a[108:111],a[28:31]
zr v130,a18
zr v131,a19
zm a[28:31],a[148:151],a[92:95],a[28:31]
zr v132,a20
zr v133,a21
zm a[28:31],a[144:147],a[76:79],a[28:31]
zr v134,a22
zr v135,a23
zm a[28:31],a[140:143],v[92:95],a[28:31]
zc v152,v128,v129
zc v153,v130,v131
zm a[28:31],a[136:139],v[76:79],a[28:31]
zc v154,v132,v133
zc v155,v134,v135
zm a[28:31],a[132:135],v[28:31],a[28:31]
buffer_store_dwordx4 v[152:155], v214, s[96:99], s72 offen offset:2048 sc1
zm a[28:31],a[128:131],v[12:15],a[28:31]
s_waitcnt lgkmcnt(14)
zm a[32:35],a[160:163],v[0:3],a[32:35]
s_waitcnt lgkmcnt(12)
zm a[32:35],a[164:167],v[16:19],a[32:35]
s_waitcnt lgkmcnt(10)
zm a[32:35],a[168:171],v[64:67],a[32:35]
s_waitcnt lgkmcnt(8)
zr v136,a24
zr v137,a25
zm a[32:35],a[172:175],v[80:83],a[32:35]
s_waitcnt lgkmcnt(6)
zr v138,a26
zr v139,a27
zm a[32:35],a[176:179],a[64:67],a[32:35]
s_waitcnt lgkmcnt(4)
zr v140,a28
zr v141,a29
zm a[32:35],a[180:183],a[80:83],a[32:35]
s_waitcnt lgkmcnt(2)
zr v142,a30
zr v143,a31
zm a[32:35],a[184:187],a[96:99],a[32:35]
s_waitcnt lgkmcnt(0)
zc v156,v136,v137
zc v157,v138,v139
zm a[32:35],a[188:191],a[112:115],a[32:35]
zc v158,v140,v141
zc v159,v142,v143
zm a[36:39],a[188:191],a[116:119],a[36:39]
buffer_store_dwordx4 v[156:159], v214, s[96:99], s72 offen offset:3072 sc1
zm a[36:39],a[184:187],a[100:103],a[36:39]
zm a[36:39],a[180:183],a[84:87],a[36:39]
zt a[128:129],v207 offset:0
zm a[36:39],a[176:179],a[68:71],a[36:39]
zt a[130:131],v207 offset:4096
zm a[36:39],a[172:175],v[84:87],a[36:39]
zt a[132:133],v207 offset:8192
buffer_load_dwordx2 v[244:245], v215, s[92:95], 0 offen offset:0 sc1
zm a[36:39],a[168:171],v[68:71],a[36:39]
zt a[134:135],v207 offset:12288
zm a[36:39],a[164:167],v[20:23],a[36:39]
zt a[136:137],v207 offset:16384
zm a[36:39],a[160:163],v[4:7],a[36:39]
zt a[138:139],v207 offset:20480
zm a[40:43],a[160:163],v[8:11],a[40:43]
zt a[140:141],v207 offset:24576
zm a[40:43],a[164:167],v[24:27],a[40:43]
zt a[142:143],v207 offset:28672
zm a[40:43],a[168:171],v[72:75],a[40:43]
zt a[144:145],v207 offset:32768
zm a[40:43],a[172:175],v[88:91],a[40:43]
zt a[146:147],v207 offset:36864
zm a[40:43],a[176:179],a[72:75],a[40:43]
zt a[148:149],v207 offset:40960
zm a[40:43],a[180:183],a[88:91],a[40:43]
zt a[150:151],v207 offset:45056
zm a[40:43],a[184:187],a[104:107],a[40:43]
zt a[152:153],v207 offset:49152
zm a[40:43],a[188:191],a[120:123],a[40:43]
zt a[154:155],v207 offset:53248
s_waitcnt vmcnt(6)
s_mov_b64 exec, 1
buffer_store_dwordx2 v[216:217], off, s[92:95], s89 sc1
s_mov_b64 exec, -1
zm a[44:47],a[188:191],a[124:127],a[44:47]
zt a[156:157],v207 offset:57344
zm a[44:47],a[184:187],a[108:111],a[44:47]
zt a[158:159],v207 offset:61440
zm a[44:47],a[180:183],a[92:95],a[44:47]
zm a[44:47],a[176:179],a[76:79],a[44:47]
zm a[44:47],a[172:175],v[92:95],a[44:47]
zm a[44:47],a[168:171],v[76:79],a[44:47]
zm a[44:47],a[164:167],v[28:31],a[44:47]
zm a[44:47],a[160:163],v[12:15],a[44:47]
s_waitcnt lgkmcnt(14)
zm a[48:51],a[128:131],v[0:3],a[48:51]
s_waitcnt lgkmcnt(12)
zt a[160:161],v196 offset:32768
zm a[48:51],a[132:135],v[16:19],a[48:51]
s_waitcnt lgkmcnt(11)
zt a[162:163],v196 offset:36864
zm a[48:51],a[136:139],v[64:67],a[48:51]
s_waitcnt lgkmcnt(10)
zm a[48:51],a[140:143],v[80:83],a[48:51]
s_waitcnt lgkmcnt(8)
zt a[164:165],v196 offset:40960
zm a[48:51],a[144:147],a[64:67],a[48:51]
s_waitcnt lgkmcnt(7)
zt a[166:167],v196 offset:45056
zm a[48:51],a[148:151],a[80:83],a[48:51]
s_waitcnt lgkmcnt(6)
zm a[48:51],a[152:155],a[96:99],a[48:51]
s_waitcnt lgkmcnt(4)
zt a[168:169],v196 offset:49152
zm a[48:51],a[156:159],a[112:115],a[48:51]
zt a[170:171],v196 offset:53248
zm a[52:55],a[156:159],a[116:119],a[52:55]
zm a[52:55],a[152:155],a[100:103],a[52:55]
zt a[172:173],v196 offset:57344
zm a[52:55],a[148:151],a[84:87],a[52:55]
zt a[174:175],v196 offset:61440
zm a[52:55],a[144:147],a[68:71],a[52:55]
zm a[52:55],a[140:143],v[84:87],a[52:55]
zt a[176:177],v197 offset:32768
zm a[52:55],a[136:139],v[68:71],a[52:55]
zt a[178:179],v197 offset:36864
zm a[52:55],a[132:135],v[20:23],a[52:55]
zm a[52:55],a[128:131],v[4:7],a[52:55]
zt a[180:181],v197 offset:40960
zm a[56:59],a[128:131],v[8:11],a[56:59]
zt a[182:183],v197 offset:45056
zm a[56:59],a[132:135],v[24:27],a[56:59]
zm a[56:59],a[136:139],v[72:75],a[56:59]
zt a[184:185],v197 offset:49152
zm a[56:59],a[140:143],v[88:91],a[56:59]
zt a[186:187],v197 offset:53248
zm a[56:59],a[144:147],a[72:75],a[56:59]
zm a[56:59],a[148:151],a[88:91],a[56:59]
zt a[188:189],v197 offset:57344
zm a[56:59],a[152:155],a[104:107],a[56:59]
zt a[190:191],v197 offset:61440
zm a[56:59],a[156:159],a[120:123],a[56:59]
zm a[60:63],a[156:159],a[124:127],a[60:63]
zm a[60:63],a[152:155],a[108:111],a[60:63]
zm a[60:63],a[148:151],a[92:95],a[60:63]
zm a[60:63],a[144:147],a[76:79],a[60:63]
zm a[60:63],a[140:143],v[92:95],a[60:63]
zm a[60:63],a[136:139],v[76:79],a[60:63]
zm a[60:63],a[132:135],v[28:31],a[60:63]
zm a[60:63],a[128:131],v[12:15],a[60:63]
s_waitcnt lgkmcnt(14)
zm a[32:35],a[160:163],v[32:35],a[32:35]
s_waitcnt lgkmcnt(12)
zm a[32:35],a[164:167],v[48:51],a[32:35]
s_waitcnt lgkmcnt(10)
zm a[32:35],a[168:171],v[96:99],a[32:35]
s_waitcnt lgkmcnt(8)
zm a[32:35],a[172:175],v[112:115],a[32:35]
zm a[36:39],a[172:175],v[116:119],a[36:39]
zm a[36:39],a[168:171],v[100:103],a[36:39]
zm a[36:39],a[164:167],v[52:55],a[36:39]
zm a[36:39],a[160:163],v[36:39],a[36:39]
zm a[40:43],a[160:163],v[40:43],a[40:43]
zm a[40:43],a[164:167],v[56:59],a[40:43]
zm a[40:43],a[168:171],v[104:107],a[40:43]
s_waitcnt vmcnt(1)
s_mov_b64 exec, 1
buffer_store_dwordx2 v[216:217], off, s[92:95], s90 sc1
s_mov_b64 exec, -1
buffer_load_dwordx2 v[246:247], v215, s[92:95], 0 offen offset:0 sc1
v_cmp_eq_u64 vcc, v[244:245], v[216:217]
s_nop 0
s_and_b32 s71, vcc_lo, s51
s_xor_b32 s51, s51, s71
s_bitcmp1_b32 s71, 0
s_cbranch_scc0 L_ka0%=
zl v[160:163],v214,s[96:99],s84 offen offset:0 sc1
zl v[164:167],v214,s[96:99],s84 offen offset:1024 sc1
zl v[168:171],v214,s[96:99],s84 offen offset:2048 sc1
zl v[172:175],v214,s[96:99],s84 offen offset:3072 sc1
L_ka0%=:
s_bitcmp1_b32 s71, 1
s_cbranch_scc0 L_ka1%=
zl v[176:179],v214,s[96:99],s85 offen offset:0 sc1
zl v[180:183],v214,s[96:99],s85 offen offset:1024 sc1
zl v[184:187],v214,s[96:99],s85 offen offset:2048 sc1
zl v[188:191],v214,s[96:99],s85 offen offset:3072 sc1
L_ka1%=:
s_bitcmp1_b32 s71, 2
s_cbranch_scc0 L_ka2%=
zl v[128:131],v214,s[96:99],s86 offen offset:0 sc1
zl v[132:135],v214,s[96:99],s86 offen offset:1024 sc1
zl v[136:139],v214,s[96:99],s86 offen offset:2048 sc1
zl v[140:143],v214,s[96:99],s86 offen offset:3072 sc1
L_ka2%=:
zm a[40:43],a[172:175],v[120:123],a[40:43]
zm a[44:47],a[172:175],v[124:127],a[44:47]
zm a[44:47],a[168:171],v[108:111],a[44:47]
zm a[44:47],a[164:167],v[60:63],a[44:47]
zm a[44:47],a[160:163],v[44:47],a[44:47]
s_waitcnt lgkmcnt(6)
zm a[48:51],a[176:179],v[32:35],a[48:51]
s_waitcnt lgkmcnt(4)
zm a[48:51],a[180:183],v[48:51],a[48:51]
s_waitcnt lgkmcnt(2)
zm a[48:51],a[184:187],v[96:99],a[48:51]
s_waitcnt lgkmcnt(0)
zm a[48:51],a[188:191],v[112:115],a[48:51]
zm a[52:55],a[188:191],v[116:119],a[52:55]
zm a[52:55],a[184:187],v[100:103],a[52:55]
zm a[52:55],a[180:183],v[52:55],a[52:55]
zm a[52:55],a[176:179],v[36:39],a[52:55]
zm a[56:59],a[176:179],v[40:43],a[56:59]
zm a[56:59],a[180:183],v[56:59],a[56:59]
zm a[56:59],a[184:187],v[104:107],a[56:59]
zm a[56:59],a[188:191],v[120:123],a[56:59]
zm a[60:63],a[188:191],v[124:127],a[60:63]
zm a[60:63],a[184:187],v[108:111],a[60:63]
zm a[60:63],a[180:183],v[60:63],a[60:63]
zm a[60:63],a[176:179],v[44:47],a[60:63]
s_nop 7
.p2align 3
zr v96,a32
zr v97,a33
zr v98,a34
zr v99,a35
zr v100,a36
zr v101,a37
zr v102,a38
zr v103,a39
zr v104,a40
zr v105,a41
zr v106,a42
zr v107,a43
zr v108,a44
zr v109,a45
zr v110,a46
zr v111,a47
zr v112,a48
zr v113,a49
zr v114,a50
zr v115,a51
zr v116,a52
zr v117,a53
zr v118,a54
zr v119,a55
zr v120,a56
zr v121,a57
zr v122,a58
zr v123,a59
zr v124,a60
zr v125,a61
zr v126,a62
zr v127,a63
s_mov_b32 s54, s51
L_e1%=:
s_waitcnt vmcnt(0)
v_cmp_eq_u64 vcc, v[246:247], v[216:217]
s_nop 0
s_and_b32 s71, vcc_lo, s51
s_xor_b32 s51, s51, s71
s_bitcmp1_b32 s71, 0
s_cbranch_scc0 L_kb0%=
zl v[160:163],v214,s[96:99],s84 offen offset:0 sc1
zl v[164:167],v214,s[96:99],s84 offen offset:1024 sc1
zl v[168:171],v214,s[96:99],s84 offen offset:2048 sc1
zl v[172:175],v214,s[96:99],s84 offen offset:3072 sc1
L_kb0%=:
s_bitcmp1_b32 s71, 1
s_cbranch_scc0 L_kb1%=
zl v[176:179],v214,s[96:99],s85 offen offset:0 sc1
zl v[180:183],v214,s[96:99],s85 offen offset:1024 sc1
zl v[184:187],v214,s[96:99],s85 offen offset:2048 sc1
zl v[188:191],v214,s[96:99],s85 offen offset:3072 sc1
L_kb1%=:
s_bitcmp1_b32 s71, 2
s_cbranch_scc0 L_kb2%=
zl v[128:131],v214,s[96:99],s86 offen offset:0 sc1
zl v[132:135],v214,s[96:99],s86 offen offset:1024 sc1
zl v[136:139],v214,s[96:99],s86 offen offset:2048 sc1
zl v[140:143],v214,s[96:99],s86 offen offset:3072 sc1
L_kb2%=:
s_and_b32 s74, s51, 3
s_cbranch_scc1 L_es%=
L_e2%=:
s_bitcmp1_b32 s54, 0
s_cbranch_scc0 L_e3%=
s_waitcnt vmcnt(0)
L_e3%=:
.p2align 3
buffer_load_dwordx2 v[244:245], v215, s[92:95], 0 offen offset:0 sc1
v_lshlrev_b32 v0, 16, v160
v_and_b32 v1, v242, v160
v_pk_add_f32 v[96:97], v[96:97], v[0:1]
v_lshlrev_b32 v2, 16, v161
v_and_b32 v3, v242, v161
v_pk_add_f32 v[98:99], v[98:99], v[2:3]
v_lshlrev_b32 v4, 16, v162
v_and_b32 v5, v242, v162
v_pk_add_f32 v[100:101], v[100:101], v[4:5]
v_lshlrev_b32 v6, 16, v163
v_and_b32 v7, v242, v163
v_pk_add_f32 v[102:103], v[102:103], v[6:7]
buffer_load_dwordx2 v[246:247], v215, s[92:95], 0 offen offset:0 sc1
v_lshlrev_b32 v0, 16, v164
v_and_b32 v1, v242, v164
v_pk_add_f32 v[104:105], v[104:105], v[0:1]
v_lshlrev_b32 v2, 16, v165
v_and_b32 v3, v242, v165
v_pk_add_f32 v[106:107], v[106:107], v[2:3]
v_lshlrev_b32 v4, 16, v166
v_and_b32 v5, v242, v166
v_pk_add_f32 v[108:109], v[108:109], v[4:5]
v_lshlrev_b32 v6, 16, v167
v_and_b32 v7, v242, v167
v_pk_add_f32 v[110:111], v[110:111], v[6:7]
buffer_load_dwordx2 v[248:249], v215, s[92:95], 0 offen offset:0 sc1
v_lshlrev_b32 v0, 16, v168
v_and_b32 v1, v242, v168
v_pk_add_f32 v[112:113], v[112:113], v[0:1]
v_lshlrev_b32 v2, 16, v169
v_and_b32 v3, v242, v169
v_pk_add_f32 v[114:115], v[114:115], v[2:3]
v_lshlrev_b32 v4, 16, v170
v_and_b32 v5, v242, v170
v_pk_add_f32 v[116:117], v[116:117], v[4:5]
v_lshlrev_b32 v6, 16, v171
v_and_b32 v7, v242, v171
v_pk_add_f32 v[118:119], v[118:119], v[6:7]
buffer_load_dwordx2 v[250:251], v215, s[92:95], 0 offen offset:0 sc1
v_lshlrev_b32 v0, 16, v172
v_and_b32 v1, v242, v172
v_pk_add_f32 v[120:121], v[120:121], v[0:1]
v_lshlrev_b32 v2, 16, v173
v_and_b32 v3, v242, v173
v_pk_add_f32 v[122:123], v[122:123], v[2:3]
v_lshlrev_b32 v4, 16, v174
v_and_b32 v5, v242, v174
v_pk_add_f32 v[124:125], v[124:125], v[4:5]
v_lshlrev_b32 v6, 16, v175
v_and_b32 v7, v242, v175
v_pk_add_f32 v[126:127], v[126:127], v[6:7]
s_bitcmp1_b32 s51, 2
s_cbranch_scc0 L_e4%=
L_pq%=:
s_waitcnt vmcnt(3)
v_cmp_ne_u64 vcc, v[244:245], v[216:217]
s_nop 1
s_cbranch_vccz L_pqd%=
buffer_load_dwordx2 v[244:245], v215, s[92:95], 0 offen offset:0 sc1
s_sleep 2
s_waitcnt vmcnt(3)
v_cmp_ne_u64 vcc, v[246:247], v[216:217]
s_nop 1
s_cbranch_vccz L_pqd%=
buffer_load_dwordx2 v[246:247], v215, s[92:95], 0 offen offset:0 sc1
s_sleep 2
s_waitcnt vmcnt(3)
v_cmp_ne_u64 vcc, v[248:249], v[216:217]
s_nop 1
s_cbranch_vccz L_pqd%=
buffer_load_dwordx2 v[248:249], v215, s[92:95], 0 offen offset:0 sc1
s_sleep 2
s_waitcnt vmcnt(3)
v_cmp_ne_u64 vcc, v[250:251], v[216:217]
s_nop 1
s_cbranch_vccz L_pqd%=
buffer_load_dwordx2 v[250:251], v215, s[92:95], 0 offen offset:0 sc1
s_sleep 2
s_add_u32 s63, s63, 1
s_cmp_lt_u32 s63, 0x100000
)"
R"(s_cbranch_scc1 L_pq%=
L_pqd%=:
zl v[128:131],v214,s[96:99],s86 offen offset:0 sc1
zl v[132:135],v214,s[96:99],s86 offen offset:1024 sc1
zl v[136:139],v214,s[96:99],s86 offen offset:2048 sc1
zl v[140:143],v214,s[96:99],s86 offen offset:3072 sc1
s_branch L_e5%=
L_e4%=:
s_waitcnt vmcnt(4)
L_e5%=:
.p2align 3
v_lshlrev_b32 v0, 16, v176
v_and_b32 v1, v242, v176
v_pk_add_f32 v[96:97], v[96:97], v[0:1]
v_lshlrev_b32 v2, 16, v177
v_and_b32 v3, v242, v177
v_pk_add_f32 v[98:99], v[98:99], v[2:3]
v_lshlrev_b32 v4, 16, v178
v_and_b32 v5, v242, v178
v_pk_add_f32 v[100:101], v[100:101], v[4:5]
v_lshlrev_b32 v6, 16, v179
v_and_b32 v7, v242, v179
v_pk_add_f32 v[102:103], v[102:103], v[6:7]
v_lshlrev_b32 v0, 16, v180
v_and_b32 v1, v242, v180
v_pk_add_f32 v[104:105], v[104:105], v[0:1]
v_lshlrev_b32 v2, 16, v181
v_and_b32 v3, v242, v181
v_pk_add_f32 v[106:107], v[106:107], v[2:3]
v_lshlrev_b32 v4, 16, v182
v_and_b32 v5, v242, v182
v_pk_add_f32 v[108:109], v[108:109], v[4:5]
v_lshlrev_b32 v6, 16, v183
v_and_b32 v7, v242, v183
v_pk_add_f32 v[110:111], v[110:111], v[6:7]
v_lshlrev_b32 v0, 16, v184
v_and_b32 v1, v242, v184
v_pk_add_f32 v[112:113], v[112:113], v[0:1]
v_lshlrev_b32 v2, 16, v185
v_and_b32 v3, v242, v185
v_pk_add_f32 v[114:115], v[114:115], v[2:3]
v_lshlrev_b32 v4, 16, v186
v_and_b32 v5, v242, v186
v_pk_add_f32 v[116:117], v[116:117], v[4:5]
v_lshlrev_b32 v6, 16, v187
v_and_b32 v7, v242, v187
v_pk_add_f32 v[118:119], v[118:119], v[6:7]
v_lshlrev_b32 v0, 16, v188
v_and_b32 v1, v242, v188
v_pk_add_f32 v[120:121], v[120:121], v[0:1]
v_lshlrev_b32 v2, 16, v189
v_and_b32 v3, v242, v189
v_pk_add_f32 v[122:123], v[122:123], v[2:3]
v_lshlrev_b32 v4, 16, v190
v_and_b32 v5, v242, v190
v_pk_add_f32 v[124:125], v[124:125], v[4:5]
v_lshlrev_b32 v6, 16, v191
v_and_b32 v7, v242, v191
v_pk_add_f32 v[126:127], v[126:127], v[6:7]
s_bitcmp1_b32 s51, 2
s_cbranch_scc0 L_e6%=
s_waitcnt vmcnt(0)
L_e6%=:
s_cmp_eq_u32 s65, 0
s_cselect_b32 s94, s94, 0
s_mov_b64 exec, 1
buffer_store_dwordx2 v[216:217], off, s[92:95], 0 offset:3072 sc1
s_mov_b64 exec, -1
.p2align 3
v_lshlrev_b32 v0, 16, v128
v_and_b32 v1, v242, v128
v_pk_add_f32 v[96:97], v[96:97], v[0:1]
v_lshlrev_b32 v2, 16, v129
v_and_b32 v3, v242, v129
v_pk_add_f32 v[98:99], v[98:99], v[2:3]
v_lshlrev_b32 v4, 16, v130
v_and_b32 v5, v242, v130
v_pk_add_f32 v[100:101], v[100:101], v[4:5]
v_lshlrev_b32 v6, 16, v131
v_and_b32 v7, v242, v131
v_pk_add_f32 v[102:103], v[102:103], v[6:7]
v_lshlrev_b32 v0, 16, v132
v_and_b32 v1, v242, v132
v_pk_add_f32 v[104:105], v[104:105], v[0:1]
v_lshlrev_b32 v2, 16, v133
v_and_b32 v3, v242, v133
v_pk_add_f32 v[106:107], v[106:107], v[2:3]
v_lshlrev_b32 v4, 16, v134
v_and_b32 v5, v242, v134
v_pk_add_f32 v[108:109], v[108:109], v[4:5]
v_lshlrev_b32 v6, 16, v135
v_and_b32 v7, v242, v135
v_pk_add_f32 v[110:111], v[110:111], v[6:7]
v_lshlrev_b32 v0, 16, v136
v_and_b32 v1, v242, v136
v_pk_add_f32 v[112:113], v[112:113], v[0:1]
v_lshlrev_b32 v2, 16, v137
v_and_b32 v3, v242, v137
v_pk_add_f32 v[114:115], v[114:115], v[2:3]
v_lshlrev_b32 v4, 16, v138
v_and_b32 v5, v242, v138
v_pk_add_f32 v[116:117], v[116:117], v[4:5]
v_lshlrev_b32 v6, 16, v139
v_and_b32 v7, v242, v139
v_pk_add_f32 v[118:119], v[118:119], v[6:7]
v_lshlrev_b32 v0, 16, v140
v_and_b32 v1, v242, v140
v_pk_add_f32 v[120:121], v[120:121], v[0:1]
v_lshlrev_b32 v2, 16, v141
v_and_b32 v3, v242, v141
v_pk_add_f32 v[122:123], v[122:123], v[2:3]
v_lshlrev_b32 v4, 16, v142
v_and_b32 v5, v242, v142
v_pk_add_f32 v[124:125], v[124:125], v[4:5]
v_lshlrev_b32 v6, 16, v143
v_and_b32 v7, v242, v143
v_pk_add_f32 v[126:127], v[126:127], v[6:7]
zc v16,v96,v97
zc v17,v98,v99
zc v18,v112,v113
zc v19,v114,v115
zc v20,v100,v101
zc v21,v102,v103
zc v22,v116,v117
zc v23,v118,v119
zc v24,v104,v105
zc v25,v106,v107
zc v26,v120,v121
zc v27,v122,v123
zc v28,v108,v109
zc v29,v110,v111
zc v30,v124,v125
zc v31,v126,v127
v_permlane16_swap_b32 v16, v18
v_permlane16_swap_b32 v17, v19
buffer_store_dwordx4 v[16:19], v237, s[28:31], 0 offen offset:0 sc1
v_permlane16_swap_b32 v20, v22
v_permlane16_swap_b32 v21, v23
buffer_store_dwordx4 v[20:23], v238, s[28:31], 0 offen offset:0 sc1
v_permlane16_swap_b32 v24, v26
v_permlane16_swap_b32 v25, v27
buffer_store_dwordx4 v[24:27], v239, s[28:31], 0 offen offset:0 sc1
v_permlane16_swap_b32 v28, v30
v_permlane16_swap_b32 v29, v31
buffer_store_dwordx4 v[28:31], v240, s[28:31], 0 offen offset:0 sc1
s_endpgm
L_es%=:
s_sleep 1
buffer_load_dwordx2 v[246:247], v215, s[92:95], 0 offen offset:0 sc1
s_add_u32 s63, s63, 1
s_cmp_lt_u32 s63, 0x100000
s_cbranch_scc1 L_e1%=
s_waitcnt vmcnt(0)
s_branch L_e2%=
L_ps1%=:
s_sleep 1
buffer_load_dwordx2 v[226:227], v215, s[92:95], 0 offen offset:1024 sc1
s_add_u32 s63, s63, 1
s_waitcnt vmcnt(0)
s_cmp_lt_u32 s63, 0x100000
s_cbranch_scc1 L_pc1%=
s_branch L_pd1%=
L_ps0%=:
s_sleep 1
buffer_load_dwordx2 v[226:227], v215, s[92:95], 0 offen offset:0 sc1
s_add_u32 s63, s63, 1
s_waitcnt vmcnt(0)
s_cmp_lt_u32 s63, 0x100000
s_cbranch_scc1 L_pc0%=
s_branch L_pd0%=
L_w3%=:
s_waitcnt vmcnt(10)
s_barrier
zk a[128:131],v226 offset:0
zk a[132:135],v227 offset:0
zk a[136:139],v226 offset:2048
zk a[140:143],v227 offset:2048
zk a[144:147],v226 offset:4096
zk a[148:151],v227 offset:4096
zt v[128:129],v232 offset:0
zt v[130:131],v232 offset:1024
zt v[132:133],v232 offset:8192
zt v[134:135],v232 offset:9216
zt v[136:137],v232 offset:8
zt v[138:139],v232 offset:1032
zt v[140:141],v232 offset:8200
zt v[142:143],v232 offset:9224
zt v[144:145],v233 offset:0
s_memtime s[56:57]
s_memrealtime s[66:67]
s_waitcnt lgkmcnt(0)
s_mov_b32 s61, s66
s_mov_b32 s79, s56
s_sub_u32 s60, s66, 16
zm v[0:3],v[128:131],a[128:131],0
s_mov_b32 s49, 0x80
zt v[146:147],v233 offset:1024
zm v[0:3],v[132:135],a[132:135],v[0:3]
s_add_u32 m0, s46, 0x8000
zm v[4:7],v[132:135],a[140:143],0
zt v[148:149],v233 offset:8192
zl v218,s[24:27],s49 offen offset:0 lds
zm v[4:7],v[128:131],a[136:139],v[4:7]
zt v[150:151],v233 offset:9216
zl v219,s[24:27],s49 offen offset:1024 lds
zm v[8:11],v[128:131],a[144:147],0
zm v[8:11],v[132:135],a[148:151],v[8:11]
zt v[152:153],v233 offset:8
zt v[154:155],v233 offset:1032
zl v220,s[24:27],s49 offen offset:2048 lds
zt v[156:157],v233 offset:8200
zm v[24:27],v[140:143],a[148:151],0
zt v[158:159],v233 offset:9224
zm v[24:27],v[136:139],a[144:147],v[24:27]
zl v221,s[24:27],s49 offen offset:3072 lds
zm v[20:23],v[136:139],a[136:139],0
zt v[160:161],v234 offset:0
zm v[20:23],v[140:143],a[140:143],v[20:23]
zt v[162:163],v234 offset:1024
zm v[16:19],v[140:143],a[132:135],0
s_add_u32 m0, s46, 0x9000
zm v[16:19],v[136:139],a[128:131],v[16:19]
s_waitcnt lgkmcnt(8)
zt v[164:165],v234 offset:8192
zl v222,s[24:27],s49 offen offset:0 lds
zm v[32:35],v[144:147],a[128:131],0
s_waitcnt lgkmcnt(7)
zt v[166:167],v234 offset:9216
zm v[32:35],v[148:151],a[132:135],v[32:35]
zm v[36:39],v[148:151],a[140:143],0
zt v[168:169],v234 offset:8
zm v[36:39],v[144:147],a[136:139],v[36:39]
zl v223,s[24:27],s49 offen offset:1024 lds
zm v[40:43],v[144:147],a[144:147],0
zt v[170:171],v234 offset:1032
zm v[40:43],v[148:151],a[148:151],v[40:43]
s_waitcnt lgkmcnt(6)
zt v[172:173],v234 offset:8200
zt v[174:175],v234 offset:9224
zt v[176:177],v235 offset:0
zm v[56:59],v[156:159],a[148:151],0
zt v[178:179],v235 offset:1024
zm v[56:59],v[152:155],a[144:147],v[56:59]
zm v[52:55],v[152:155],a[136:139],0
s_mov_b32 s50, 0x80100
zt v[180:181],v235 offset:8192
zm v[52:55],v[156:159],a[140:143],v[52:55]
s_add_u32 m0, s45, 0xc000
zt v[182:183],v235 offset:9216
zm v[48:51],v[156:159],a[132:135],0
zl v228,s[28:31],s50 offen offset:0 lds
zm v[48:51],v[152:155],a[128:131],v[48:51]
s_waitcnt lgkmcnt(10)
zt v[184:185],v235 offset:8
zm v[64:67],v[160:163],a[128:131],0
s_waitcnt lgkmcnt(9)
zm v[64:67],v[164:167],a[132:135],v[64:67]
zt v[186:187],v235 offset:1032
zm v[68:71],v[164:167],a[140:143],0
zt v[188:189],v235 offset:8200
zl v229,s[28:31],s50 offen offset:1024 lds
zm v[68:71],v[160:163],a[136:139],v[68:71]
zm v[72:75],v[160:163],a[144:147],0
zt v[190:191],v235 offset:9224
zm v[72:75],v[164:167],a[148:151],v[72:75]
s_waitcnt lgkmcnt(8)
zl v230,s[28:31],s50 offen offset:2048 lds
zm v[88:91],v[172:175],a[148:151],0
zl v231,s[28:31],s50 offen offset:3072 lds
zm v[88:91],v[168:171],a[144:147],v[88:91]
zm v[84:87],v[168:171],a[136:139],0
zm v[84:87],v[172:175],a[140:143],v[84:87]
zm v[80:83],v[172:175],a[132:135],0
zm v[80:83],v[168:171],a[128:131],v[80:83]
s_waitcnt vmcnt(15) lgkmcnt(0)
zm v[96:99],v[176:179],a[128:131],0
s_cselect_b32 s71, 1, 0
s_add_u32 s60, s60, 0x46
L_pz1%=:
s_memrealtime s[66:67]
s_waitcnt lgkmcnt(0)
s_sub_i32 s62, s60, s66
s_cmp_gt_i32 s62, 0
s_cbranch_scc1 L_pz1%=
s_add_u32 s62, s62, 6
s_min_i32 s62, s62, 0
s_sub_u32 s60, s60, s62
s_cmp_lg_u32 s71, 0
s_barrier
zm v[96:99],v[180:183],a[132:135],v[96:99]
zm v[100:103],v[180:183],a[140:143],0
zt v[128:129],v232 offset:16384
zm v[100:103],v[176:179],a[136:139],v[100:103]
zt v[130:131],v232 offset:17408
zm v[104:107],v[176:179],a[144:147],0
zm v[104:107],v[180:183],a[148:151],v[104:107]
zt v[132:133],v232 offset:24576
zt v[134:135],v232 offset:25600
zt v[136:137],v232 offset:16392
s_cmp_ge_u32 s68, 0x300
zm v[120:123],v[188:191],a[148:151],0
s_cselect_b32 s49, -6, 0
zt v[138:139],v232 offset:17416
zm v[120:123],v[184:187],a[144:147],v[120:123]
s_lshl_b32 s49, s49, 7
zt v[140:141],v232 offset:24584
zm v[116:119],v[184:187],a[136:139],0
s_add_u32 s49, s49, s68
zm v[116:119],v[188:191],a[140:143],v[116:119]
s_add_u32 s49, s49, 0x100
zt v[142:143],v232 offset:25608
zm v[112:115],v[188:191],a[132:135],0
s_add_u32 m0, s46, 0x20000
zm v[112:115],v[184:187],a[128:131],v[112:115]
s_waitcnt lgkmcnt(6)
zt v[144:145],v233 offset:16384
zl v218,s[24:27],s49 offen offset:0 lds
zm a[0:3],v[128:131],a[128:131],0
s_waitcnt lgkmcnt(5)
zt v[146:147],v233 offset:17408
zl v219,s[24:27],s49 offen offset:1024 lds
zm a[0:3],v[132:135],a[132:135],a[0:3]
zm a[4:7],v[132:135],a[140:143],0
zt v[148:149],v233 offset:24576
zm a[4:7],v[128:131],a[136:139],a[4:7]
zt v[150:151],v233 offset:25600
zl v220,s[24:27],s49 offen offset:2048 lds
zm a[8:11],v[128:131],a[144:147],0
zm a[8:11],v[132:135],a[148:151],a[8:11]
s_waitcnt lgkmcnt(4)
zt v[152:153],v233 offset:16392
zt v[154:155],v233 offset:17416
zt v[156:157],v233 offset:24584
zl v221,s[24:27],s49 offen offset:3072 lds
zm a[24:27],v[140:143],a[148:151],0
zt v[158:159],v233 offset:25608
zm a[24:27],v[136:139],a[144:147],a[24:27]
zm a[20:23],v[136:139],a[136:139],0
zt v[160:161],v234 offset:16384
zm a[20:23],v[140:143],a[140:143],a[20:23]
s_add_u32 m0, s46, 0x21000
zt v[162:163],v234 offset:17408
zm a[16:19],v[140:143],a[132:135],0
zl v222,s[24:27],s49 offen offset:0 lds
zm a[16:19],v[136:139],a[128:131],a[16:19]
s_waitcnt lgkmcnt(8)
zt v[164:165],v234 offset:24576
zm a[32:35],v[144:147],a[128:131],0
s_waitcnt lgkmcnt(7)
zt v[166:167],v234 offset:25600
zm a[32:35],v[148:151],a[132:135],a[32:35]
zm a[36:39],v[148:151],a[140:143],0
zt v[168:169],v234 offset:16392
zm a[36:39],v[144:147],a[136:139],a[36:39]
zl v223,s[24:27],s49 offen offset:1024 lds
zm a[40:43],v[144:147],a[144:147],0
zt v[170:171],v234 offset:17416
zm a[40:43],v[148:151],a[148:151],a[40:43]
s_waitcnt lgkmcnt(6)
zt v[172:173],v234 offset:24584
zt v[174:175],v234 offset:25608
zt v[176:177],v235 offset:16384
zm a[56:59],v[156:159],a[148:151],0
zt v[178:179],v235 offset:17408
zm a[56:59],v[152:155],a[144:147],a[56:59]
zm a[52:55],v[152:155],a[136:139],0
zt v[180:181],v235 offset:24576
zm a[52:55],v[156:159],a[140:143],a[52:55]
zt v[182:183],v235 offset:25600
zm a[48:51],v[156:159],a[132:135],0
s_cmp_ge_u32 s69, 0x300000
zm a[48:51],v[152:155],a[128:131],a[48:51]
s_waitcnt lgkmcnt(10)
s_cselect_b32 s50, -6, 0
zt v[184:185],v235 offset:16392
zm a[64:67],v[160:163],a[128:131],0
s_waitcnt lgkmcnt(9)
s_lshl_b32 s50, s50, 19
zm a[64:67],v[164:167],a[132:135],a[64:67]
s_add_u32 s50, s50, s69
zt v[186:187],v235 offset:17416
zm a[68:71],v[164:167],a[140:143],0
s_add_u32 s50, s50, 0x100000
zt v[188:189],v235 offset:24584
zm a[68:71],v[160:163],a[136:139],a[68:71]
s_add_u32 m0, s45, 0
zm a[72:75],v[160:163],a[144:147],0
zt v[190:191],v235 offset:25608
zl v228,s[28:31],s50 offen offset:0 lds
zm a[72:75],v[164:167],a[148:151],a[72:75]
s_waitcnt lgkmcnt(8)
zl v229,s[28:31],s50 offen offset:1024 lds
zm a[88:91],v[172:175],a[148:151],0
zm a[88:91],v[168:171],a[144:147],a[88:91]
zm a[84:87],v[168:171],a[136:139],0
zm a[84:87],v[172:175],a[140:143],a[84:87]
zl v230,s[28:31],s50 offen offset:2048 lds
zm a[80:83],v[172:175],a[132:135],0
zm a[80:83],v[168:171],a[128:131],a[80:83]
s_waitcnt vmcnt(13) lgkmcnt(0)
zm a[96:99],v[176:179],a[128:131],0
s_cselect_b32 s71, 1, 0
s_add_u32 s60, s60, 0x46
L_pz2%=:
s_memrealtime s[66:67]
s_waitcnt lgkmcnt(0)
s_sub_i32 s62, s60, s66
s_cmp_gt_i32 s62, 0
s_cbranch_scc1 L_pz2%=
s_add_u32 s62, s62, 6
s_min_i32 s62, s62, 0
s_sub_u32 s60, s60, s62
s_cmp_lg_u32 s71, 0
s_barrier
zm a[96:99],v[180:183],a[132:135],a[96:99]
zk a[160:163],v250 offset:32768
zm a[100:103],v[180:183],a[140:143],0
zk a[164:167],v251 offset:32768
zt v[128:129],v232 offset:32768
zm a[100:103],v[176:179],a[136:139],a[100:103]
zk a[168:171],v250 offset:34816
zt v[130:131],v232 offset:33792
zm a[104:107],v[176:179],a[144:147],0
zk a[172:175],v251 offset:34816
zl v231,s[28:31],s50 offen offset:3072 lds
zm a[104:107],v[180:183],a[148:151],a[104:107]
zk a[176:179],v250 offset:36864
zt v[132:133],v232 offset:40960
zk a[180:183],v251 offset:36864
zt v[134:135],v232 offset:41984
zt v[136:137],v232 offset:32776
zm a[120:123],v[188:191],a[148:151],0
zt v[138:139],v232 offset:33800
zm a[120:123],v[184:187],a[144:147],a[120:123]
zt v[140:141],v232 offset:40968
zm a[116:119],v[184:187],a[136:139],0
s_cmp_ge_u32 s69, 0x300000
zm a[116:119],v[188:191],a[140:143],a[116:119]
s_cselect_b32 s50, -6, 0
zt v[142:143],v232 offset:41992
zm a[112:115],v[188:191],a[132:135],0
s_lshl_b32 s50, s50, 19
zm a[112:115],v[184:187],a[128:131],a[112:115]
s_waitcnt lgkmcnt(9)
s_add_u32 s50, s50, s69
zt v[144:145],v233 offset:32768
zm v[0:3],v[128:131],a[160:163],v[0:3]
s_waitcnt lgkmcnt(5)
s_add_u32 s50, s50, 0x100100
zt v[146:147],v233 offset:33792
zm v[0:3],v[132:135],a[164:167],v[0:3]
s_add_u32 m0, s45, 0x4000
zm v[4:7],v[132:135],a[172:175],v[4:7]
zt v[148:149],v233 offset:40960
zl v228,s[28:31],s50 offen offset:0 lds
zm v[4:7],v[128:131],a[168:171],v[4:7]
zt v[150:151],v233 offset:41984
zm v[8:11],v[128:131],a[176:179],v[8:11]
zm v[8:11],v[132:135],a[180:183],v[8:11]
s_waitcnt lgkmcnt(4)
zt v[152:153],v233 offset:32776
zt v[154:155],v233 offset:33800
zt v[156:157],v233 offset:40968
zl v229,s[28:31],s50 offen offset:1024 lds
zm v[24:27],v[140:143],a[180:183],v[24:27]
zt v[158:159],v233 offset:41992
zm v[24:27],v[136:139],a[176:179],v[24:27]
zm v[20:23],v[136:139],a[168:171],v[20:23]
zt v[160:161],v234 offset:32768
zm v[20:23],v[140:143],a[172:175],v[20:23]
zt v[162:163],v234 offset:33792
zm v[16:19],v[140:143],a[164:167],v[16:19]
zm v[16:19],v[136:139],a[160:163],v[16:19]
s_waitcnt lgkmcnt(8)
zt v[164:165],v234 offset:40960
zm v[32:35],v[144:147],a[160:163],v[32:35]
s_waitcnt lgkmcnt(7)
zt v[166:167],v234 offset:41984
zm v[32:35],v[148:151],a[164:167],v[32:35]
zm v[36:39],v[148:151],a[172:175],v[36:39]
zt v[168:169],v234 offset:32776
zm v[36:39],v[144:147],a[168:171],v[36:39]
zm v[40:43],v[144:147],a[176:179],v[40:43]
zt v[170:171],v234 offset:33800
zm v[40:43],v[148:151],a[180:183],v[40:43]
s_waitcnt lgkmcnt(6)
zt v[172:173],v234 offset:40968
zl v230,s[28:31],s50 offen offset:2048 lds
zt v[174:175],v234 offset:41992
zt v[176:177],v235 offset:32768
zm v[56:59],v[156:159],a[180:183],v[56:59]
zt v[178:179],v235 offset:33792
zm v[56:59],v[152:155],a[176:179],v[56:59]
zm v[52:55],v[152:155],a[168:171],v[52:55]
zt v[180:181],v235 offset:40960
zm v[52:55],v[156:159],a[172:175],v[52:55]
zt v[182:183],v235 offset:41984
zm v[48:51],v[156:159],a[164:167],v[48:51]
zm v[48:51],v[152:155],a[160:163],v[48:51]
s_waitcnt lgkmcnt(10)
zt v[184:185],v235 offset:32776
zm v[64:67],v[160:163],a[160:163],v[64:67]
s_waitcnt lgkmcnt(9)
zm v[64:67],v[164:167],a[164:167],v[64:67]
zt v[186:187],v235 offset:33800
zl v231,s[28:31],s50 offen offset:3072 lds
zm v[68:71],v[164:167],a[172:175],v[68:71]
zt v[188:189],v235 offset:40968
zm v[68:71],v[160:163],a[168:171],v[68:71]
zm v[72:75],v[160:163],a[176:179],v[72:75]
zt v[190:191],v235 offset:41992
zm v[72:75],v[164:167],a[180:183],v[72:75]
s_waitcnt lgkmcnt(8)
zm v[88:91],v[172:175],a[180:183],v[88:91]
zm v[88:91],v[168:171],a[176:179],v[88:91]
zm v[84:87],v[168:171],a[168:171],v[84:87]
zm v[84:87],v[172:175],a[172:175],v[84:87]
zm v[80:83],v[172:175],a[164:167],v[80:83]
zm v[80:83],v[168:171],a[160:163],v[80:83]
s_waitcnt vmcnt(8) lgkmcnt(0)
zm v[96:99],v[176:179],a[160:163],v[96:99]
s_cselect_b32 s71, 1, 0
s_add_u32 s60, s60, 0x46
L_pz3%=:
s_memrealtime s[66:67]
s_waitcnt lgkmcnt(0)
s_sub_i32 s62, s60, s66
s_cmp_gt_i32 s62, 0
s_cbranch_scc1 L_pz3%=
s_add_u32 s62, s62, 6
s_min_i32 s62, s62, 0
s_sub_u32 s60, s60, s62
s_cmp_lg_u32 s71, 0
s_barrier
zm v[96:99],v[180:183],a[164:167],v[96:99]
zk a[128:131],v226 offset:0
zm v[100:103],v[180:183],a[172:175],v[100:103]
zk a[132:135],v227 offset:0
zt v[128:129],v232 offset:49152
zm v[100:103],v[176:179],a[168:171],v[100:103]
zk a[136:139],v226 offset:2048
zt v[130:131],v232 offset:50176
zm v[104:107],v[176:179],a[176:179],v[104:107]
zk a[140:143],v227 offset:2048
zm v[104:107],v[180:183],a[180:183],v[104:107]
zk a[144:147],v226 offset:4096
zt v[132:133],v232 offset:57344
zk a[148:151],v227 offset:4096
zt v[134:135],v232 offset:58368
zt v[136:137],v232 offset:49160
s_cmp_ge_u32 s68, 0x280
zm v[120:123],v[188:191],a[180:183],v[120:123]
s_cselect_b32 s49, -6, 0
zt v[138:139],v232 offset:50184
zm v[120:123],v[184:187],a[176:179],v[120:123]
s_lshl_b32 s49, s49, 7
zt v[140:141],v232 offset:57352
zm v[116:119],v[184:187],a[168:171],v[116:119]
s_add_u32 s49, s49, s68
zm v[116:119],v[188:191],a[172:175],v[116:119]
s_add_u32 s49, s49, 0x180
zt v[142:143],v232 offset:58376
zm v[112:115],v[188:191],a[164:167],v[112:115]
s_waitcnt lgkmcnt(3)
s_add_u32 m0, s46, 0x20000
zm v[112:115],v[184:187],a[160:163],v[112:115]
zt v[144:145],v233 offset:49152
zl v218,s[24:27],s49 offen offset:0 lds
zm a[0:3],v[128:131],a[160:163],a[0:3]
zt v[146:147],v233 offset:50176
zl v219,s[24:27],s49 offen offset:1024 lds
zm a[0:3],v[132:135],a[164:167],a[0:3]
zm a[4:7],v[132:135],a[172:175],a[4:7]
zt v[148:149],v233 offset:57344
zm a[4:7],v[128:131],a[168:171],a[4:7]
zt v[150:151],v233 offset:58368
zl v220,s[24:27],s49 offen offset:2048 lds
zm a[8:11],v[128:131],a[176:179],a[8:11]
zm a[8:11],v[132:135],a[180:183],a[8:11]
s_waitcnt lgkmcnt(4)
zt v[152:153],v233 offset:49160
zt v[154:155],v233 offset:50184
zt v[156:157],v233 offset:57352
zl v221,s[24:27],s49 offen offset:3072 lds
zm a[24:27],v[140:143],a[180:183],a[24:27]
zt v[158:159],v233 offset:58376
zm a[24:27],v[136:139],a[176:179],a[24:27]
zm a[20:23],v[136:139],a[168:171],a[20:23]
zt v[160:161],v234 offset:49152
zm a[20:23],v[140:143],a[172:175],a[20:23]
s_add_u32 m0, s46, 0x21000
zt v[162:163],v234 offset:50176
zm a[16:19],v[140:143],a[164:167],a[16:19]
zl v222,s[24:27],s49 offen offset:0 lds
zm a[16:19],v[136:139],a[160:163],a[16:19]
s_waitcnt lgkmcnt(8)
zt v[164:165],v234 offset:57344
zm a[32:35],v[144:147],a[160:163],a[32:35]
s_waitcnt lgkmcnt(7)
zt v[166:167],v234 offset:58368
zm a[32:35],v[148:151],a[164:167],a[32:35]
zm a[36:39],v[148:151],a[172:175],a[36:39]
zt v[168:169],v234 offset:49160
zm a[36:39],v[144:147],a[168:171],a[36:39]
zl v223,s[24:27],s49 offen offset:1024 lds
zm a[40:43],v[144:147],a[176:179],a[40:43]
zt v[170:171],v234 offset:50184
zm a[40:43],v[148:151],a[180:183],a[40:43]
s_waitcnt lgkmcnt(6)
zt v[172:173],v234 offset:57352
zt v[174:175],v234 offset:58376
zt v[176:177],v235 offset:49152
zm a[56:59],v[156:159],a[180:183],a[56:59]
zt v[178:179],v235 offset:50176
zm a[56:59],v[152:155],a[176:179],a[56:59]
zm a[52:55],v[152:155],a[168:171],a[52:55]
zt v[180:181],v235 offset:57344
zm a[52:55],v[156:159],a[172:175],a[52:55]
zt v[182:183],v235 offset:58368
zm a[48:51],v[156:159],a[164:167],a[48:51]
s_cmp_ge_u32 s69, 0x280000
zm a[48:51],v[152:155],a[160:163],a[48:51]
s_waitcnt lgkmcnt(10)
s_cselect_b32 s50, -6, 0
zt v[184:185],v235 offset:49160
zm a[64:67],v[160:163],a[160:163],a[64:67]
s_waitcnt lgkmcnt(9)
s_lshl_b32 s50, s50, 19
zm a[64:67],v[164:167],a[164:167],a[64:67]
s_add_u32 s50, s50, s69
zt v[186:187],v235 offset:50184
zm a[68:71],v[164:167],a[172:175],a[68:71]
s_add_u32 s50, s50, 0x180000
zt v[188:189],v235 offset:57352
zm a[68:71],v[160:163],a[168:171],a[68:71]
s_add_u32 m0, s45, 0x8000
zm a[72:75],v[160:163],a[176:179],a[72:75]
zt v[190:191],v235 offset:58376
zl v228,s[28:31],s50 offen offset:0 lds
zm a[72:75],v[164:167],a[180:183],a[72:75]
s_waitcnt lgkmcnt(8)
zl v229,s[28:31],s50 offen offset:1024 lds
zm a[88:91],v[172:175],a[180:183],a[88:91]
zm a[88:91],v[168:171],a[176:179],a[88:91]
zm a[84:87],v[168:171],a[168:171],a[84:87]
zm a[84:87],v[172:175],a[172:175],a[84:87]
zl v230,s[28:31],s50 offen offset:2048 lds
zm a[80:83],v[172:175],a[164:167],a[80:83]
zm a[80:83],v[168:171],a[160:163],a[80:83]
s_waitcnt vmcnt(13) lgkmcnt(0)
zm a[96:99],v[176:179],a[160:163],a[96:99]
s_cselect_b32 s71, 1, 0
s_add_u32 s60, s60, 0x46
L_pz4%=:
s_memrealtime s[66:67]
s_waitcnt lgkmcnt(0)
s_sub_i32 s62, s60, s66
s_cmp_gt_i32 s62, 0
s_cbranch_scc1 L_pz4%=
s_add_u32 s62, s62, 6
s_min_i32 s62, s62, 0
s_sub_u32 s60, s60, s62
s_cmp_lg_u32 s71, 0
s_barrier
zm a[96:99],v[180:183],a[164:167],a[96:99]
zm a[100:103],v[180:183],a[172:175],a[100:103]
zt v[128:129],v232 offset:0
zl v231,s[28:31],s50 offen offset:3072 lds
zm a[100:103],v[176:179],a[168:171],a[100:103]
zt v[130:131],v232 offset:1024
zm a[104:107],v[176:179],a[176:179],a[104:107]
zm a[104:107],v[180:183],a[180:183],a[104:107]
zt v[132:133],v232 offset:8192
zt v[134:135],v232 offset:9216
zt v[136:137],v232 offset:8
zm a[120:123],v[188:191],a[180:183],a[120:123]
zt v[138:139],v232 offset:1032
zm a[120:123],v[184:187],a[176:179],a[120:123]
zt v[140:141],v232 offset:8200
zm a[116:119],v[184:187],a[168:171],a[116:119]
s_cmp_ge_u32 s69, 0x280000
zm a[116:119],v[188:191],a[172:175],a[116:119]
s_cselect_b32 s50, -6, 0
zt v[142:143],v232 offset:9224
zm a[112:115],v[188:191],a[164:167],a[112:115]
s_lshl_b32 s50, s50, 19
zm a[112:115],v[184:187],a[160:163],a[112:115]
s_waitcnt lgkmcnt(6)
s_add_u32 s50, s50, s69
zt v[144:145],v233 offset:0
zm v[0:3],v[128:131],a[128:131],v[0:3]
s_waitcnt lgkmcnt(5)
s_add_u32 s50, s50, 0x180100
zt v[146:147],v233 offset:1024
zm v[0:3],v[132:135],a[132:135],v[0:3]
s_add_u32 m0, s45, 0xc000
zm v[4:7],v[132:135],a[140:143],v[4:7]
zt v[148:149],v233 offset:8192
zl v228,s[28:31],s50 offen offset:0 lds
zm v[4:7],v[128:131],a[136:139],v[4:7]
zt v[150:151],v233 offset:9216
zl v229,s[28:31],s50 offen offset:1024 lds
zm v[8:11],v[128:131],a[144:147],v[8:11]
zm v[8:11],v[132:135],a[148:151],v[8:11]
s_waitcnt lgkmcnt(4)
zt v[152:153],v233 offset:8
zt v[154:155],v233 offset:1032
zt v[156:157],v233 offset:8200
zl v230,s[28:31],s50 offen offset:2048 lds
zm v[24:27],v[140:143],a[148:151],v[24:27]
zt v[158:159],v233 offset:9224
zm v[24:27],v[136:139],a[144:147],v[24:27]
zm v[20:23],v[136:139],a[136:139],v[20:23]
zt v[160:161],v234 offset:0
zm v[20:23],v[140:143],a[140:143],v[20:23]
zt v[162:163],v234 offset:1024
zm v[16:19],v[140:143],a[132:135],v[16:19]
zl v231,s[28:31],s50 offen offset:3072 lds
zm v[16:19],v[136:139],a[128:131],v[16:19]
s_waitcnt lgkmcnt(8)
zt v[164:165],v234 offset:8192
zm v[32:35],v[144:147],a[128:131],v[32:35]
s_waitcnt lgkmcnt(7)
zt v[166:167],v234 offset:9216
zm v[32:35],v[148:151],a[132:135],v[32:35]
zm v[36:39],v[148:151],a[140:143],v[36:39]
zt v[168:169],v234 offset:8
zm v[36:39],v[144:147],a[136:139],v[36:39]
zm v[40:43],v[144:147],a[144:147],v[40:43]
zt v[170:171],v234 offset:1032
zm v[40:43],v[148:151],a[148:151],v[40:43]
s_waitcnt lgkmcnt(6)
zt v[172:173],v234 offset:8200
buffer_load_dword v254, v214, s[40:43], s70 offen offset:0
zt v[174:175],v234 offset:9224
zt v[176:177],v235 offset:0
zm v[56:59],v[156:159],a[148:151],v[56:59]
zt v[178:179],v235 offset:1024
zm v[56:59],v[152:155],a[144:147],v[56:59]
buffer_load_dword v254, v214, s[40:43], s70 offen offset:1024
zm v[52:55],v[152:155],a[136:139],v[52:55]
zt v[180:181],v235 offset:8192
zm v[52:55],v[156:159],a[140:143],v[52:55]
zt v[182:183],v235 offset:9216
zm v[48:51],v[156:159],a[132:135],v[48:51]
zm v[48:51],v[152:155],a[128:131],v[48:51]
s_waitcnt lgkmcnt(10)
zt v[184:185],v235 offset:8
zm v[64:67],v[160:163],a[128:131],v[64:67]
s_waitcnt lgkmcnt(9)
zm v[64:67],v[164:167],a[132:135],v[64:67]
zt v[186:187],v235 offset:1032
buffer_load_dword v254, v214, s[40:43], s70 offen offset:2048
zm v[68:71],v[164:167],a[140:143],v[68:71]
zt v[188:189],v235 offset:8200
zm v[68:71],v[160:163],a[136:139],v[68:71]
zm v[72:75],v[160:163],a[144:147],v[72:75]
zt v[190:191],v235 offset:9224
zm v[72:75],v[164:167],a[148:151],v[72:75]
s_waitcnt lgkmcnt(8)
buffer_load_dword v254, v214, s[40:43], s70 offen offset:3072
s_add_u32 s70, s70, 0x1000
zm v[88:91],v[172:175],a[148:151],v[88:91]
zm v[88:91],v[168:171],a[144:147],v[88:91]
zm v[84:87],v[168:171],a[136:139],v[84:87]
zm v[84:87],v[172:175],a[140:143],v[84:87]
zm v[80:83],v[172:175],a[132:135],v[80:83]
zm v[80:83],v[168:171],a[128:131],v[80:83]
s_waitcnt vmcnt(12) lgkmcnt(0)
zm v[96:99],v[176:179],a[128:131],v[96:99]
s_cselect_b32 s71, 1, 0
s_add_u32 s60, s60, 0x46
L_pz5%=:
s_memrealtime s[66:67]
s_waitcnt lgkmcnt(0)
s_sub_i32 s62, s60, s66
s_cmp_gt_i32 s62, 0
s_cbranch_scc1 L_pz5%=
s_add_u32 s62, s62, 6
s_min_i32 s62, s62, 0
s_sub_u32 s60, s60, s62
s_cmp_lg_u32 s71, 0
s_barrier
zm v[96:99],v[180:183],a[132:135],v[96:99]
zk a[160:163],v226 offset:0
zm v[100:103],v[180:183],a[140:143],v[100:103]
zk a[164:167],v227 offset:0
zt v[128:129],v232 offset:16384
zm v[100:103],v[176:179],a[136:139],v[100:103]
zk a[168:171],v226 offset:2048
zt v[130:131],v232 offset:17408
zm v[104:107],v[176:179],a[144:147],v[104:107]
zk a[172:175],v227 offset:2048
zm v[104:107],v[180:183],a[148:151],v[104:107]
zk a[176:179],v226 offset:4096
zt v[132:133],v232 offset:24576
zk a[180:183],v227 offset:4096
zt v[134:135],v232 offset:25600
zt v[136:137],v232 offset:16392
s_cmp_ge_u32 s68, 0x200
zm v[120:123],v[188:191],a[148:151],v[120:123]
s_cselect_b32 s49, -6, 0
zt v[138:139],v232 offset:17416
zm v[120:123],v[184:187],a[144:147],v[120:123]
s_lshl_b32 s49, s49, 7
zt v[140:141],v232 offset:24584
zm v[116:119],v[184:187],a[136:139],v[116:119]
s_add_u32 s49, s49, s68
zm v[116:119],v[188:191],a[140:143],v[116:119]
s_add_u32 s49, s49, 0x200
zt v[142:143],v232 offset:25608
zm v[112:115],v[188:191],a[132:135],v[112:115]
s_waitcnt lgkmcnt(3)
s_add_u32 m0, s46, 0x20000
zm v[112:115],v[184:187],a[128:131],v[112:115]
zt v[144:145],v233 offset:16384
zl v218,s[24:27],s49 offen offset:0 lds
zm a[0:3],v[128:131],a[128:131],a[0:3]
zt v[146:147],v233 offset:17408
zl v219,s[24:27],s49 offen offset:1024 lds
zm a[0:3],v[132:135],a[132:135],a[0:3]
zm a[4:7],v[132:135],a[140:143],a[4:7]
zt v[148:149],v233 offset:24576
zm a[4:7],v[128:131],a[136:139],a[4:7]
zt v[150:151],v233 offset:25600
zl v220,s[24:27],s49 offen offset:2048 lds
zm a[8:11],v[128:131],a[144:147],a[8:11]
zm a[8:11],v[132:135],a[148:151],a[8:11]
s_waitcnt lgkmcnt(4)
zt v[152:153],v233 offset:16392
zt v[154:155],v233 offset:17416
zt v[156:157],v233 offset:24584
zl v221,s[24:27],s49 offen offset:3072 lds
zm a[24:27],v[140:143],a[148:151],a[24:27]
zt v[158:159],v233 offset:25608
zm a[24:27],v[136:139],a[144:147],a[24:27]
zm a[20:23],v[136:139],a[136:139],a[20:23]
zt v[160:161],v234 offset:16384
zm a[20:23],v[140:143],a[140:143],a[20:23]
s_add_u32 m0, s46, 0x21000
zt v[162:163],v234 offset:17408
zm a[16:19],v[140:143],a[132:135],a[16:19]
zl v222,s[24:27],s49 offen offset:0 lds
zm a[16:19],v[136:139],a[128:131],a[16:19]
s_waitcnt lgkmcnt(8)
zt v[164:165],v234 offset:24576
zm a[32:35],v[144:147],a[128:131],a[32:35]
s_waitcnt lgkmcnt(7)
zt v[166:167],v234 offset:25600
zm a[32:35],v[148:151],a[132:135],a[32:35]
zm a[36:39],v[148:151],a[140:143],a[36:39]
zt v[168:169],v234 offset:16392
zm a[36:39],v[144:147],a[136:139],a[36:39]
zl v223,s[24:27],s49 offen offset:1024 lds
zm a[40:43],v[144:147],a[144:147],a[40:43]
zt v[170:171],v234 offset:17416
zm a[40:43],v[148:151],a[148:151],a[40:43]
s_waitcnt lgkmcnt(6)
zt v[172:173],v234 offset:24584
zt v[174:175],v234 offset:25608
zt v[176:177],v235 offset:16384
zm a[56:59],v[156:159],a[148:151],a[56:59]
zt v[178:179],v235 offset:17408
zm a[56:59],v[152:155],a[144:147],a[56:59]
zm a[52:55],v[152:155],a[136:139],a[52:55]
zt v[180:181],v235 offset:24576
zm a[52:55],v[156:159],a[140:143],a[52:55]
zt v[182:183],v235 offset:25600
zm a[48:51],v[156:159],a[132:135],a[48:51]
s_cmp_ge_u32 s69, 0x200000
zm a[48:51],v[152:155],a[128:131],a[48:51]
s_waitcnt lgkmcnt(10)
s_cselect_b32 s50, -6, 0
zt v[184:185],v235 offset:16392
zm a[64:67],v[160:163],a[128:131],a[64:67]
s_waitcnt lgkmcnt(9)
s_lshl_b32 s50, s50, 19
zm a[64:67],v[164:167],a[132:135],a[64:67]
s_add_u32 s50, s50, s69
zt v[186:187],v235 offset:17416
zm a[68:71],v[164:167],a[140:143],a[68:71]
s_add_u32 s50, s50, 0x200000
zt v[188:189],v235 offset:24584
zm a[68:71],v[160:163],a[136:139],a[68:71]
s_add_u32 m0, s45, 0
zm a[72:75],v[160:163],a[144:147],a[72:75]
zt v[190:191],v235 offset:25608
zl v228,s[28:31],s50 offen offset:0 lds
zm a[72:75],v[164:167],a[148:151],a[72:75]
s_waitcnt lgkmcnt(8)
zl v229,s[28:31],s50 offen offset:1024 lds
zm a[88:91],v[172:175],a[148:151],a[88:91]
zm a[88:91],v[168:171],a[144:147],a[88:91]
zm a[84:87],v[168:171],a[136:139],a[84:87]
zm a[84:87],v[172:175],a[140:143],a[84:87]
zl v230,s[28:31],s50 offen offset:2048 lds
zm a[80:83],v[172:175],a[132:135],a[80:83]
zm a[80:83],v[168:171],a[128:131],a[80:83]
s_waitcnt vmcnt(17) lgkmcnt(0)
zm a[96:99],v[176:179],a[128:131],a[96:99]
s_cselect_b32 s71, 1, 0
s_add_u32 s60, s60, 0x46
L_pz6%=:
s_memrealtime s[66:67]
s_waitcnt lgkmcnt(0)
s_sub_i32 s62, s60, s66
s_cmp_gt_i32 s62, 0
s_cbranch_scc1 L_pz6%=
s_add_u32 s62, s62, 6
s_min_i32 s62, s62, 0
s_sub_u32 s60, s60, s62
s_cmp_lg_u32 s71, 0
s_barrier
zm a[96:99],v[180:183],a[132:135],a[96:99]
zm a[100:103],v[180:183],a[140:143],a[100:103]
zt v[128:129],v232 offset:32768
zl v231,s[28:31],s50 offen offset:3072 lds
zm a[100:103],v[176:179],a[136:139],a[100:103]
zt v[130:131],v232 offset:33792
zm a[104:107],v[176:179],a[144:147],a[104:107]
zm a[104:107],v[180:183],a[148:151],a[104:107]
zt v[132:133],v232 offset:40960
zt v[134:135],v232 offset:41984
zt v[136:137],v232 offset:32776
zm a[120:123],v[188:191],a[148:151],a[120:123]
zt v[138:139],v232 offset:33800
zm a[120:123],v[184:187],a[144:147],a[120:123]
zt v[140:141],v232 offset:40968
zm a[116:119],v[184:187],a[136:139],a[116:119]
s_cmp_ge_u32 s69, 0x200000
zm a[116:119],v[188:191],a[140:143],a[116:119]
s_cselect_b32 s50, -6, 0
zt v[142:143],v232 offset:41992
zm a[112:115],v[188:191],a[132:135],a[112:115]
s_lshl_b32 s50, s50, 19
zm a[112:115],v[184:187],a[128:131],a[112:115]
s_waitcnt lgkmcnt(6)
s_add_u32 s50, s50, s69
zt v[144:145],v233 offset:32768
zm v[0:3],v[128:131],a[160:163],v[0:3]
s_waitcnt lgkmcnt(5)
s_add_u32 s50, s50, 0x200100
zt v[146:147],v233 offset:33792
zm v[0:3],v[132:135],a[164:167],v[0:3]
s_add_u32 m0, s45, 0x4000
zm v[4:7],v[132:135],a[172:175],v[4:7]
zt v[148:149],v233 offset:40960
zl v228,s[28:31],s50 offen offset:0 lds
zm v[4:7],v[128:131],a[168:171],v[4:7]
zt v[150:151],v233 offset:41984
zl v229,s[28:31],s50 offen offset:1024 lds
zm v[8:11],v[128:131],a[176:179],v[8:11]
zm v[8:11],v[132:135],a[180:183],v[8:11]
s_waitcnt lgkmcnt(4)
zt v[152:153],v233 offset:32776
zt v[154:155],v233 offset:33800
zt v[156:157],v233 offset:40968
zl v230,s[28:31],s50 offen offset:2048 lds
zm v[24:27],v[140:143],a[180:183],v[24:27]
zt v[158:159],v233 offset:41992
zm v[24:27],v[136:139],a[176:179],v[24:27]
zm v[20:23],v[136:139],a[168:171],v[20:23]
zt v[160:161],v234 offset:32768
zm v[20:23],v[140:143],a[172:175],v[20:23]
zt v[162:163],v234 offset:33792
zm v[16:19],v[140:143],a[164:167],v[16:19]
zl v231,s[28:31],s50 offen offset:3072 lds
zm v[16:19],v[136:139],a[160:163],v[16:19]
s_waitcnt lgkmcnt(8)
zt v[164:165],v234 offset:40960
zm v[32:35],v[144:147],a[160:163],v[32:35]
s_waitcnt lgkmcnt(7)
zt v[166:167],v234 offset:41984
zm v[32:35],v[148:151],a[164:167],v[32:35]
zm v[36:39],v[148:151],a[172:175],v[36:39]
zt v[168:169],v234 offset:32776
zm v[36:39],v[144:147],a[168:171],v[36:39]
zm v[40:43],v[144:147],a[176:179],v[40:43]
s_and_b32 m0, s47, 0xffff
zt v[170:171],v234 offset:33800
zm v[40:43],v[148:151],a[180:183],v[40:43]
s_waitcnt lgkmcnt(6)
zt v[172:173],v234 offset:40968
zl v212,s[96:99],s47 offen offset:0 lds
zt v[174:175],v234 offset:41992
s_add_u32 s47, s47, 0x1000
zt v[176:177],v235 offset:32768
zm v[56:59],v[156:159],a[180:183],v[56:59]
s_and_b32 m0, s47, 0xffff
zt v[178:179],v235 offset:33792
zm v[56:59],v[152:155],a[176:179],v[56:59]
zl v212,s[96:99],s47 offen offset:0 lds
zm v[52:55],v[152:155],a[168:171],v[52:55]
zt v[180:181],v235 offset:40960
zm v[52:55],v[156:159],a[172:175],v[52:55]
zt v[182:183],v235 offset:41984
zm v[48:51],v[156:159],a[164:167],v[48:51]
zm v[48:51],v[152:155],a[160:163],v[48:51]
s_waitcnt lgkmcnt(10)
s_add_u32 s47, s47, 0x1000
)"
R"(zt v[184:185],v235 offset:32776
zm v[64:67],v[160:163],a[160:163],v[64:67]
s_waitcnt lgkmcnt(9)
s_and_b32 m0, s47, 0xffff
zm v[64:67],v[164:167],a[164:167],v[64:67]
zt v[186:187],v235 offset:33800
zl v212,s[96:99],s47 offen offset:0 nt lds
zm v[68:71],v[164:167],a[172:175],v[68:71]
zt v[188:189],v235 offset:40968
zm v[68:71],v[160:163],a[168:171],v[68:71]
zm v[72:75],v[160:163],a[176:179],v[72:75]
zt v[190:191],v235 offset:41992
zm v[72:75],v[164:167],a[180:183],v[72:75]
s_waitcnt lgkmcnt(8)
s_add_u32 s47, s47, 0x1000
s_and_b32 m0, s47, 0xffff
s_nop 0
zl v212,s[96:99],s47 offen offset:0 nt lds
s_add_u32 s47, s47, 0x1000
zm v[88:91],v[172:175],a[180:183],v[88:91]
zm v[88:91],v[168:171],a[176:179],v[88:91]
zm v[84:87],v[168:171],a[168:171],v[84:87]
zm v[84:87],v[172:175],a[172:175],v[84:87]
zm v[80:83],v[172:175],a[164:167],v[80:83]
zm v[80:83],v[168:171],a[160:163],v[80:83]
s_waitcnt vmcnt(12) lgkmcnt(0)
zm v[96:99],v[176:179],a[160:163],v[96:99]
s_cselect_b32 s71, 1, 0
s_add_u32 s60, s60, 0x46
L_pz7%=:
s_memrealtime s[66:67]
s_waitcnt lgkmcnt(0)
s_sub_i32 s62, s60, s66
s_cmp_gt_i32 s62, 0
s_cbranch_scc1 L_pz7%=
s_add_u32 s62, s62, 6
s_min_i32 s62, s62, 0
s_sub_u32 s60, s60, s62
s_cmp_lg_u32 s71, 0
s_barrier
zm v[96:99],v[180:183],a[164:167],v[96:99]
zk a[128:131],v226 offset:0
zm v[100:103],v[180:183],a[172:175],v[100:103]
zk a[132:135],v227 offset:0
zt v[128:129],v232 offset:49152
zm v[100:103],v[176:179],a[168:171],v[100:103]
zk a[136:139],v226 offset:2048
zt v[130:131],v232 offset:50176
zm v[104:107],v[176:179],a[176:179],v[104:107]
zk a[140:143],v227 offset:2048
zm v[104:107],v[180:183],a[180:183],v[104:107]
zk a[144:147],v226 offset:4096
zt v[132:133],v232 offset:57344
zk a[148:151],v227 offset:4096
zt v[134:135],v232 offset:58368
zt v[136:137],v232 offset:49160
s_cmp_ge_u32 s68, 0x180
zm v[120:123],v[188:191],a[180:183],v[120:123]
s_cselect_b32 s49, -6, 0
zt v[138:139],v232 offset:50184
zm v[120:123],v[184:187],a[176:179],v[120:123]
s_lshl_b32 s49, s49, 7
zt v[140:141],v232 offset:57352
zm v[116:119],v[184:187],a[168:171],v[116:119]
s_add_u32 s49, s49, s68
zm v[116:119],v[188:191],a[172:175],v[116:119]
s_add_u32 s49, s49, 0x280
zt v[142:143],v232 offset:58376
zm v[112:115],v[188:191],a[164:167],v[112:115]
s_waitcnt lgkmcnt(3)
s_add_u32 m0, s46, 0x20000
zm v[112:115],v[184:187],a[160:163],v[112:115]
zt v[144:145],v233 offset:49152
zl v218,s[24:27],s49 offen offset:0 lds
zm a[0:3],v[128:131],a[160:163],a[0:3]
zt v[146:147],v233 offset:50176
zl v219,s[24:27],s49 offen offset:1024 lds
zm a[0:3],v[132:135],a[164:167],a[0:3]
zm a[4:7],v[132:135],a[172:175],a[4:7]
zt v[148:149],v233 offset:57344
zm a[4:7],v[128:131],a[168:171],a[4:7]
zt v[150:151],v233 offset:58368
zl v220,s[24:27],s49 offen offset:2048 lds
zm a[8:11],v[128:131],a[176:179],a[8:11]
zm a[8:11],v[132:135],a[180:183],a[8:11]
s_waitcnt lgkmcnt(4)
zt v[152:153],v233 offset:49160
zt v[154:155],v233 offset:50184
zt v[156:157],v233 offset:57352
zl v221,s[24:27],s49 offen offset:3072 lds
zm a[24:27],v[140:143],a[180:183],a[24:27]
zt v[158:159],v233 offset:58376
zm a[24:27],v[136:139],a[176:179],a[24:27]
zm a[20:23],v[136:139],a[168:171],a[20:23]
zt v[160:161],v234 offset:49152
zm a[20:23],v[140:143],a[172:175],a[20:23]
s_add_u32 m0, s46, 0x21000
zt v[162:163],v234 offset:50176
zm a[16:19],v[140:143],a[164:167],a[16:19]
zl v222,s[24:27],s49 offen offset:0 lds
zm a[16:19],v[136:139],a[160:163],a[16:19]
s_waitcnt lgkmcnt(8)
zt v[164:165],v234 offset:57344
zm a[32:35],v[144:147],a[160:163],a[32:35]
s_waitcnt lgkmcnt(7)
zt v[166:167],v234 offset:58368
zm a[32:35],v[148:151],a[164:167],a[32:35]
zm a[36:39],v[148:151],a[172:175],a[36:39]
zt v[168:169],v234 offset:49160
zm a[36:39],v[144:147],a[168:171],a[36:39]
zl v223,s[24:27],s49 offen offset:1024 lds
zm a[40:43],v[144:147],a[176:179],a[40:43]
zt v[170:171],v234 offset:50184
zm a[40:43],v[148:151],a[180:183],a[40:43]
s_waitcnt lgkmcnt(6)
zt v[172:173],v234 offset:57352
zt v[174:175],v234 offset:58376
zt v[176:177],v235 offset:49152
zm a[56:59],v[156:159],a[180:183],a[56:59]
zt v[178:179],v235 offset:50176
zm a[56:59],v[152:155],a[176:179],a[56:59]
zm a[52:55],v[152:155],a[168:171],a[52:55]
zt v[180:181],v235 offset:57344
zm a[52:55],v[156:159],a[172:175],a[52:55]
zt v[182:183],v235 offset:58368
zm a[48:51],v[156:159],a[164:167],a[48:51]
s_cmp_ge_u32 s69, 0x180000
zm a[48:51],v[152:155],a[160:163],a[48:51]
s_waitcnt lgkmcnt(10)
s_cselect_b32 s50, -6, 0
zt v[184:185],v235 offset:49160
zm a[64:67],v[160:163],a[160:163],a[64:67]
s_waitcnt lgkmcnt(9)
s_lshl_b32 s50, s50, 19
zm a[64:67],v[164:167],a[164:167],a[64:67]
s_add_u32 s50, s50, s69
zt v[186:187],v235 offset:50184
zm a[68:71],v[164:167],a[172:175],a[68:71]
s_add_u32 s50, s50, 0x280000
zt v[188:189],v235 offset:57352
zm a[68:71],v[160:163],a[168:171],a[68:71]
s_add_u32 m0, s45, 0x8000
zm a[72:75],v[160:163],a[176:179],a[72:75]
zt v[190:191],v235 offset:58376
zl v228,s[28:31],s50 offen offset:0 lds
zm a[72:75],v[164:167],a[180:183],a[72:75]
s_waitcnt lgkmcnt(8)
zl v229,s[28:31],s50 offen offset:1024 lds
zm a[88:91],v[172:175],a[180:183],a[88:91]
zm a[88:91],v[168:171],a[176:179],a[88:91]
zm a[84:87],v[168:171],a[168:171],a[84:87]
zm a[84:87],v[172:175],a[172:175],a[84:87]
zl v230,s[28:31],s50 offen offset:2048 lds
zm a[80:83],v[172:175],a[164:167],a[80:83]
zm a[80:83],v[168:171],a[160:163],a[80:83]
s_waitcnt vmcnt(17) lgkmcnt(0)
zm a[96:99],v[176:179],a[160:163],a[96:99]
s_cselect_b32 s71, 1, 0
s_add_u32 s60, s60, 0x46
L_pz8%=:
s_memrealtime s[66:67]
s_waitcnt lgkmcnt(0)
s_sub_i32 s62, s60, s66
s_cmp_gt_i32 s62, 0
s_cbranch_scc1 L_pz8%=
s_add_u32 s62, s62, 6
s_min_i32 s62, s62, 0
s_sub_u32 s60, s60, s62
s_cmp_lg_u32 s71, 0
s_barrier
zm a[96:99],v[180:183],a[164:167],a[96:99]
zm a[100:103],v[180:183],a[172:175],a[100:103]
zt v[128:129],v232 offset:0
zl v231,s[28:31],s50 offen offset:3072 lds
zm a[100:103],v[176:179],a[168:171],a[100:103]
zt v[130:131],v232 offset:1024
zm a[104:107],v[176:179],a[176:179],a[104:107]
zm a[104:107],v[180:183],a[180:183],a[104:107]
zt v[132:133],v232 offset:8192
zt v[134:135],v232 offset:9216
zt v[136:137],v232 offset:8
zm a[120:123],v[188:191],a[180:183],a[120:123]
zt v[138:139],v232 offset:1032
zm a[120:123],v[184:187],a[176:179],a[120:123]
zt v[140:141],v232 offset:8200
zm a[116:119],v[184:187],a[168:171],a[116:119]
s_cmp_ge_u32 s69, 0x180000
zm a[116:119],v[188:191],a[172:175],a[116:119]
s_cselect_b32 s50, -6, 0
zt v[142:143],v232 offset:9224
zm a[112:115],v[188:191],a[164:167],a[112:115]
s_lshl_b32 s50, s50, 19
zm a[112:115],v[184:187],a[160:163],a[112:115]
s_waitcnt lgkmcnt(6)
s_add_u32 s50, s50, s69
zt v[144:145],v233 offset:0
zm v[0:3],v[128:131],a[128:131],v[0:3]
s_waitcnt lgkmcnt(5)
s_add_u32 s50, s50, 0x280100
zt v[146:147],v233 offset:1024
zm v[0:3],v[132:135],a[132:135],v[0:3]
s_add_u32 m0, s45, 0xc000
zm v[4:7],v[132:135],a[140:143],v[4:7]
zt v[148:149],v233 offset:8192
zl v228,s[28:31],s50 offen offset:0 lds
zm v[4:7],v[128:131],a[136:139],v[4:7]
zt v[150:151],v233 offset:9216
zl v229,s[28:31],s50 offen offset:1024 lds
zm v[8:11],v[128:131],a[144:147],v[8:11]
zm v[8:11],v[132:135],a[148:151],v[8:11]
s_waitcnt lgkmcnt(4)
zt v[152:153],v233 offset:8
zt v[154:155],v233 offset:1032
zt v[156:157],v233 offset:8200
zl v230,s[28:31],s50 offen offset:2048 lds
zm v[24:27],v[140:143],a[148:151],v[24:27]
zt v[158:159],v233 offset:9224
zm v[24:27],v[136:139],a[144:147],v[24:27]
zm v[20:23],v[136:139],a[136:139],v[20:23]
zt v[160:161],v234 offset:0
zm v[20:23],v[140:143],a[140:143],v[20:23]
zt v[162:163],v234 offset:1024
zm v[16:19],v[140:143],a[132:135],v[16:19]
zl v231,s[28:31],s50 offen offset:3072 lds
zm v[16:19],v[136:139],a[128:131],v[16:19]
s_waitcnt lgkmcnt(8)
zt v[164:165],v234 offset:8192
zm v[32:35],v[144:147],a[128:131],v[32:35]
s_waitcnt lgkmcnt(7)
zt v[166:167],v234 offset:9216
zm v[32:35],v[148:151],a[132:135],v[32:35]
zm v[36:39],v[148:151],a[140:143],v[36:39]
zt v[168:169],v234 offset:8
zm v[36:39],v[144:147],a[136:139],v[36:39]
zm v[40:43],v[144:147],a[144:147],v[40:43]
s_and_b32 m0, s47, 0xffff
zt v[170:171],v234 offset:1032
zm v[40:43],v[148:151],a[148:151],v[40:43]
s_waitcnt lgkmcnt(6)
zt v[172:173],v234 offset:8200
zl v212,s[96:99],s47 offen offset:0 lds
zt v[174:175],v234 offset:9224
s_add_u32 s47, s47, 0x1000
zt v[176:177],v235 offset:0
zm v[56:59],v[156:159],a[148:151],v[56:59]
s_and_b32 m0, s47, 0xffff
zt v[178:179],v235 offset:1024
zm v[56:59],v[152:155],a[144:147],v[56:59]
zl v212,s[96:99],s47 offen offset:0 lds
zm v[52:55],v[152:155],a[136:139],v[52:55]
zt v[180:181],v235 offset:8192
zm v[52:55],v[156:159],a[140:143],v[52:55]
zt v[182:183],v235 offset:9216
zm v[48:51],v[156:159],a[132:135],v[48:51]
zm v[48:51],v[152:155],a[128:131],v[48:51]
s_waitcnt lgkmcnt(10)
s_add_u32 s47, s47, 0x1000
zt v[184:185],v235 offset:8
zm v[64:67],v[160:163],a[128:131],v[64:67]
s_waitcnt lgkmcnt(9)
s_and_b32 m0, s47, 0xffff
zm v[64:67],v[164:167],a[132:135],v[64:67]
zt v[186:187],v235 offset:1032
zl v212,s[96:99],s47 offen offset:0 nt lds
zm v[68:71],v[164:167],a[140:143],v[68:71]
zt v[188:189],v235 offset:8200
zm v[68:71],v[160:163],a[136:139],v[68:71]
zm v[72:75],v[160:163],a[144:147],v[72:75]
zt v[190:191],v235 offset:9224
zm v[72:75],v[164:167],a[148:151],v[72:75]
s_waitcnt lgkmcnt(8)
s_add_u32 s47, s47, 0x1000
s_and_b32 m0, s47, 0xffff
s_nop 0
zl v212,s[96:99],s47 offen offset:0 nt lds
s_add_u32 s47, s47, 0x1000
zm v[88:91],v[172:175],a[148:151],v[88:91]
zm v[88:91],v[168:171],a[144:147],v[88:91]
zm v[84:87],v[168:171],a[136:139],v[84:87]
zm v[84:87],v[172:175],a[140:143],v[84:87]
zm v[80:83],v[172:175],a[132:135],v[80:83]
zm v[80:83],v[168:171],a[128:131],v[80:83]
s_waitcnt vmcnt(12) lgkmcnt(0)
zm v[96:99],v[176:179],a[128:131],v[96:99]
s_cselect_b32 s71, 1, 0
s_add_u32 s60, s60, 0x46
L_pz9%=:
s_memrealtime s[66:67]
s_waitcnt lgkmcnt(0)
s_sub_i32 s62, s60, s66
s_cmp_gt_i32 s62, 0
s_cbranch_scc1 L_pz9%=
s_add_u32 s62, s62, 6
s_min_i32 s62, s62, 0
s_sub_u32 s60, s60, s62
s_cmp_lg_u32 s71, 0
s_barrier
zm v[96:99],v[180:183],a[132:135],v[96:99]
zk a[160:163],v226 offset:0
zm v[100:103],v[180:183],a[140:143],v[100:103]
zk a[164:167],v227 offset:0
zt v[128:129],v232 offset:16384
zm v[100:103],v[176:179],a[136:139],v[100:103]
zk a[168:171],v226 offset:2048
zt v[130:131],v232 offset:17408
zm v[104:107],v[176:179],a[144:147],v[104:107]
zk a[172:175],v227 offset:2048
zm v[104:107],v[180:183],a[148:151],v[104:107]
zk a[176:179],v226 offset:4096
zt v[132:133],v232 offset:24576
zk a[180:183],v227 offset:4096
zt v[134:135],v232 offset:25600
zt v[136:137],v232 offset:16392
s_cmp_ge_u32 s68, 0x100
zm v[120:123],v[188:191],a[148:151],v[120:123]
s_cselect_b32 s49, -6, 0
zt v[138:139],v232 offset:17416
zm v[120:123],v[184:187],a[144:147],v[120:123]
s_lshl_b32 s49, s49, 7
zt v[140:141],v232 offset:24584
zm v[116:119],v[184:187],a[136:139],v[116:119]
s_add_u32 s49, s49, s68
zm v[116:119],v[188:191],a[140:143],v[116:119]
s_add_u32 s49, s49, 0x300
zt v[142:143],v232 offset:25608
zm v[112:115],v[188:191],a[132:135],v[112:115]
s_waitcnt lgkmcnt(3)
s_add_u32 m0, s46, 0x20000
zm v[112:115],v[184:187],a[128:131],v[112:115]
zt v[144:145],v233 offset:16384
zl v218,s[24:27],s49 offen offset:0 lds
zm a[0:3],v[128:131],a[128:131],a[0:3]
zt v[146:147],v233 offset:17408
zl v219,s[24:27],s49 offen offset:1024 lds
zm a[0:3],v[132:135],a[132:135],a[0:3]
zm a[4:7],v[132:135],a[140:143],a[4:7]
zt v[148:149],v233 offset:24576
zm a[4:7],v[128:131],a[136:139],a[4:7]
zt v[150:151],v233 offset:25600
zl v220,s[24:27],s49 offen offset:2048 lds
zm a[8:11],v[128:131],a[144:147],a[8:11]
zm a[8:11],v[132:135],a[148:151],a[8:11]
s_waitcnt lgkmcnt(4)
zt v[152:153],v233 offset:16392
zt v[154:155],v233 offset:17416
zt v[156:157],v233 offset:24584
zl v221,s[24:27],s49 offen offset:3072 lds
zm a[24:27],v[140:143],a[148:151],a[24:27]
zt v[158:159],v233 offset:25608
zm a[24:27],v[136:139],a[144:147],a[24:27]
zm a[20:23],v[136:139],a[136:139],a[20:23]
zt v[160:161],v234 offset:16384
zm a[20:23],v[140:143],a[140:143],a[20:23]
s_add_u32 m0, s46, 0x21000
zt v[162:163],v234 offset:17408
zm a[16:19],v[140:143],a[132:135],a[16:19]
zl v222,s[24:27],s49 offen offset:0 lds
zm a[16:19],v[136:139],a[128:131],a[16:19]
s_waitcnt lgkmcnt(8)
zt v[164:165],v234 offset:24576
zm a[32:35],v[144:147],a[128:131],a[32:35]
s_waitcnt lgkmcnt(7)
zt v[166:167],v234 offset:25600
zm a[32:35],v[148:151],a[132:135],a[32:35]
zm a[36:39],v[148:151],a[140:143],a[36:39]
zt v[168:169],v234 offset:16392
zm a[36:39],v[144:147],a[136:139],a[36:39]
zl v223,s[24:27],s49 offen offset:1024 lds
zm a[40:43],v[144:147],a[144:147],a[40:43]
zt v[170:171],v234 offset:17416
zm a[40:43],v[148:151],a[148:151],a[40:43]
s_waitcnt lgkmcnt(6)
zt v[172:173],v234 offset:24584
zt v[174:175],v234 offset:25608
zt v[176:177],v235 offset:16384
zm a[56:59],v[156:159],a[148:151],a[56:59]
zt v[178:179],v235 offset:17408
zm a[56:59],v[152:155],a[144:147],a[56:59]
zm a[52:55],v[152:155],a[136:139],a[52:55]
zt v[180:181],v235 offset:24576
zm a[52:55],v[156:159],a[140:143],a[52:55]
zt v[182:183],v235 offset:25600
zm a[48:51],v[156:159],a[132:135],a[48:51]
s_cmp_ge_u32 s69, 0x100000
zm a[48:51],v[152:155],a[128:131],a[48:51]
s_waitcnt lgkmcnt(10)
s_cselect_b32 s50, -6, 0
zt v[184:185],v235 offset:16392
zm a[64:67],v[160:163],a[128:131],a[64:67]
s_waitcnt lgkmcnt(9)
s_lshl_b32 s50, s50, 19
zm a[64:67],v[164:167],a[132:135],a[64:67]
s_add_u32 s50, s50, s69
zt v[186:187],v235 offset:17416
zm a[68:71],v[164:167],a[140:143],a[68:71]
s_add_u32 s50, s50, 0x300000
zt v[188:189],v235 offset:24584
zm a[68:71],v[160:163],a[136:139],a[68:71]
s_add_u32 m0, s45, 0
zm a[72:75],v[160:163],a[144:147],a[72:75]
zt v[190:191],v235 offset:25608
zl v228,s[28:31],s50 offen offset:0 lds
zm a[72:75],v[164:167],a[148:151],a[72:75]
s_waitcnt lgkmcnt(8)
zl v229,s[28:31],s50 offen offset:1024 lds
zm a[88:91],v[172:175],a[148:151],a[88:91]
zm a[88:91],v[168:171],a[144:147],a[88:91]
zm a[84:87],v[168:171],a[136:139],a[84:87]
zm a[84:87],v[172:175],a[140:143],a[84:87]
zl v230,s[28:31],s50 offen offset:2048 lds
zm a[80:83],v[172:175],a[132:135],a[80:83]
zm a[80:83],v[168:171],a[128:131],a[80:83]
s_waitcnt vmcnt(17) lgkmcnt(0)
zm a[96:99],v[176:179],a[128:131],a[96:99]
s_cselect_b32 s71, 1, 0
s_add_u32 s60, s60, 0x46
L_pz10%=:
s_memrealtime s[66:67]
s_waitcnt lgkmcnt(0)
s_sub_i32 s62, s60, s66
s_cmp_gt_i32 s62, 0
s_cbranch_scc1 L_pz10%=
s_add_u32 s62, s62, 6
s_min_i32 s62, s62, 0
s_sub_u32 s60, s60, s62
s_cmp_lg_u32 s71, 0
s_barrier
zm a[96:99],v[180:183],a[132:135],a[96:99]
zm a[100:103],v[180:183],a[140:143],a[100:103]
zt v[128:129],v232 offset:32768
zl v231,s[28:31],s50 offen offset:3072 lds
zm a[100:103],v[176:179],a[136:139],a[100:103]
zt v[130:131],v232 offset:33792
zm a[104:107],v[176:179],a[144:147],a[104:107]
zm a[104:107],v[180:183],a[148:151],a[104:107]
zt v[132:133],v232 offset:40960
zt v[134:135],v232 offset:41984
zt v[136:137],v232 offset:32776
zm a[120:123],v[188:191],a[148:151],a[120:123]
zt v[138:139],v232 offset:33800
zm a[120:123],v[184:187],a[144:147],a[120:123]
zt v[140:141],v232 offset:40968
zm a[116:119],v[184:187],a[136:139],a[116:119]
s_cmp_ge_u32 s69, 0x80000
zm a[116:119],v[188:191],a[140:143],a[116:119]
s_cselect_b32 s50, -6, 0
zt v[142:143],v232 offset:41992
zm a[112:115],v[188:191],a[132:135],a[112:115]
s_lshl_b32 s50, s50, 19
zm a[112:115],v[184:187],a[128:131],a[112:115]
s_waitcnt lgkmcnt(6)
s_add_u32 s50, s50, s69
zt v[144:145],v233 offset:32768
zm v[0:3],v[128:131],a[160:163],v[0:3]
s_waitcnt lgkmcnt(5)
s_add_u32 s50, s50, 0x380000
zt v[146:147],v233 offset:33792
zm v[0:3],v[132:135],a[164:167],v[0:3]
s_add_u32 m0, s45, 0x4000
zm v[4:7],v[132:135],a[172:175],v[4:7]
zt v[148:149],v233 offset:40960
zl v228,s[28:31],s50 offen offset:0 lds
zm v[4:7],v[128:131],a[168:171],v[4:7]
zt v[150:151],v233 offset:41984
zl v229,s[28:31],s50 offen offset:1024 lds
zm v[8:11],v[128:131],a[176:179],v[8:11]
zm v[8:11],v[132:135],a[180:183],v[8:11]
s_waitcnt lgkmcnt(4)
zt v[152:153],v233 offset:32776
zt v[154:155],v233 offset:33800
zt v[156:157],v233 offset:40968
zl v230,s[28:31],s50 offen offset:2048 lds
zm v[24:27],v[140:143],a[180:183],v[24:27]
zt v[158:159],v233 offset:41992
zm v[24:27],v[136:139],a[176:179],v[24:27]
zm v[20:23],v[136:139],a[168:171],v[20:23]
zt v[160:161],v234 offset:32768
zm v[20:23],v[140:143],a[172:175],v[20:23]
zt v[162:163],v234 offset:33792
zm v[16:19],v[140:143],a[164:167],v[16:19]
zl v231,s[28:31],s50 offen offset:3072 lds
zm v[16:19],v[136:139],a[160:163],v[16:19]
s_waitcnt lgkmcnt(8)
zt v[164:165],v234 offset:40960
zm v[32:35],v[144:147],a[160:163],v[32:35]
s_waitcnt lgkmcnt(7)
zt v[166:167],v234 offset:41984
zm v[32:35],v[148:151],a[164:167],v[32:35]
zm v[36:39],v[148:151],a[172:175],v[36:39]
zt v[168:169],v234 offset:32776
zm v[36:39],v[144:147],a[168:171],v[36:39]
zm v[40:43],v[144:147],a[176:179],v[40:43]
s_and_b32 m0, s47, 0xffff
zt v[170:171],v234 offset:33800
zm v[40:43],v[148:151],a[180:183],v[40:43]
s_waitcnt lgkmcnt(6)
zt v[172:173],v234 offset:40968
zl v212,s[96:99],s47 offen offset:0 lds
zt v[174:175],v234 offset:41992
s_add_u32 s47, s47, 0x1000
zt v[176:177],v235 offset:32768
zm v[56:59],v[156:159],a[180:183],v[56:59]
s_and_b32 m0, s47, 0xffff
zt v[178:179],v235 offset:33792
zm v[56:59],v[152:155],a[176:179],v[56:59]
zl v212,s[96:99],s47 offen offset:0 lds
zm v[52:55],v[152:155],a[168:171],v[52:55]
zt v[180:181],v235 offset:40960
zm v[52:55],v[156:159],a[172:175],v[52:55]
zt v[182:183],v235 offset:41984
zm v[48:51],v[156:159],a[164:167],v[48:51]
zm v[48:51],v[152:155],a[160:163],v[48:51]
s_waitcnt lgkmcnt(10)
s_add_u32 s47, s47, 0x1000
zt v[184:185],v235 offset:32776
zm v[64:67],v[160:163],a[160:163],v[64:67]
s_waitcnt lgkmcnt(9)
s_and_b32 m0, s47, 0xffff
zm v[64:67],v[164:167],a[164:167],v[64:67]
zt v[186:187],v235 offset:33800
zl v212,s[96:99],s47 offen offset:0 nt lds
zm v[68:71],v[164:167],a[172:175],v[68:71]
zt v[188:189],v235 offset:40968
zm v[68:71],v[160:163],a[168:171],v[68:71]
zm v[72:75],v[160:163],a[176:179],v[72:75]
zt v[190:191],v235 offset:41992
zm v[72:75],v[164:167],a[180:183],v[72:75]
s_waitcnt lgkmcnt(8)
s_add_u32 s47, s47, 0x1000
s_and_b32 m0, s47, 0xffff
s_nop 0
zl v212,s[96:99],s47 offen offset:0 nt lds
s_add_u32 s47, s47, 0x1000
zm v[88:91],v[172:175],a[180:183],v[88:91]
zm v[88:91],v[168:171],a[176:179],v[88:91]
zm v[84:87],v[168:171],a[168:171],v[84:87]
zm v[84:87],v[172:175],a[172:175],v[84:87]
zm v[80:83],v[172:175],a[164:167],v[80:83]
zm v[80:83],v[168:171],a[160:163],v[80:83]
s_waitcnt vmcnt(12) lgkmcnt(0)
zm v[96:99],v[176:179],a[160:163],v[96:99]
s_cselect_b32 s71, 1, 0
s_add_u32 s60, s60, 0x46
L_pz11%=:
s_memrealtime s[66:67]
s_waitcnt lgkmcnt(0)
s_sub_i32 s62, s60, s66
s_cmp_gt_i32 s62, 0
s_cbranch_scc1 L_pz11%=
s_add_u32 s62, s62, 6
s_min_i32 s62, s62, 0
s_sub_u32 s60, s60, s62
s_cmp_lg_u32 s71, 0
s_barrier
zm v[96:99],v[180:183],a[164:167],v[96:99]
zk a[128:131],v226 offset:0
zm v[100:103],v[180:183],a[172:175],v[100:103]
zk a[132:135],v227 offset:0
zt v[128:129],v232 offset:49152
zm v[100:103],v[176:179],a[168:171],v[100:103]
zk a[136:139],v226 offset:2048
zt v[130:131],v232 offset:50176
zm v[104:107],v[176:179],a[176:179],v[104:107]
zk a[140:143],v227 offset:2048
zm v[104:107],v[180:183],a[180:183],v[104:107]
zk a[144:147],v226 offset:4096
zt v[132:133],v232 offset:57344
zk a[148:151],v227 offset:4096
zt v[134:135],v232 offset:58368
zt v[136:137],v232 offset:49160
s_cmp_ge_u32 s68, 0x80
zm v[120:123],v[188:191],a[180:183],v[120:123]
s_cselect_b32 s49, -6, 0
zt v[138:139],v232 offset:50184
zm v[120:123],v[184:187],a[176:179],v[120:123]
s_lshl_b32 s49, s49, 7
zt v[140:141],v232 offset:57352
zm v[116:119],v[184:187],a[168:171],v[116:119]
s_add_u32 s49, s49, s68
zm v[116:119],v[188:191],a[172:175],v[116:119]
s_add_u32 s49, s49, 0x380
zt v[142:143],v232 offset:58376
zm v[112:115],v[188:191],a[164:167],v[112:115]
s_waitcnt lgkmcnt(3)
s_add_u32 m0, s46, 0x20000
zm v[112:115],v[184:187],a[160:163],v[112:115]
zt v[144:145],v233 offset:49152
zl v218,s[24:27],s49 offen offset:0 lds
zm a[0:3],v[128:131],a[160:163],a[0:3]
zt v[146:147],v233 offset:50176
zl v219,s[24:27],s49 offen offset:1024 lds
zm a[0:3],v[132:135],a[164:167],a[0:3]
zm a[4:7],v[132:135],a[172:175],a[4:7]
zt v[148:149],v233 offset:57344
zm a[4:7],v[128:131],a[168:171],a[4:7]
zt v[150:151],v233 offset:58368
zl v220,s[24:27],s49 offen offset:2048 lds
zm a[8:11],v[128:131],a[176:179],a[8:11]
zm a[8:11],v[132:135],a[180:183],a[8:11]
s_waitcnt lgkmcnt(4)
zt v[152:153],v233 offset:49160
zt v[154:155],v233 offset:50184
zt v[156:157],v233 offset:57352
zl v221,s[24:27],s49 offen offset:3072 lds
zm a[24:27],v[140:143],a[180:183],a[24:27]
zt v[158:159],v233 offset:58376
zm a[24:27],v[136:139],a[176:179],a[24:27]
zm a[20:23],v[136:139],a[168:171],a[20:23]
zt v[160:161],v234 offset:49152
zm a[20:23],v[140:143],a[172:175],a[20:23]
s_add_u32 m0, s46, 0x21000
zt v[162:163],v234 offset:50176
zm a[16:19],v[140:143],a[164:167],a[16:19]
zl v222,s[24:27],s49 offen offset:0 lds
zm a[16:19],v[136:139],a[160:163],a[16:19]
s_waitcnt lgkmcnt(8)
zt v[164:165],v234 offset:57344
zm a[32:35],v[144:147],a[160:163],a[32:35]
s_waitcnt lgkmcnt(7)
zt v[166:167],v234 offset:58368
zm a[32:35],v[148:151],a[164:167],a[32:35]
zm a[36:39],v[148:151],a[172:175],a[36:39]
zt v[168:169],v234 offset:49160
zm a[36:39],v[144:147],a[168:171],a[36:39]
zl v223,s[24:27],s49 offen offset:1024 lds
zm a[40:43],v[144:147],a[176:179],a[40:43]
zt v[170:171],v234 offset:50184
zm a[40:43],v[148:151],a[180:183],a[40:43]
s_waitcnt lgkmcnt(6)
zt v[172:173],v234 offset:57352
zt v[174:175],v234 offset:58376
zt v[176:177],v235 offset:49152
zm a[56:59],v[156:159],a[180:183],a[56:59]
zt v[178:179],v235 offset:50176
zm a[56:59],v[152:155],a[176:179],a[56:59]
zm a[52:55],v[152:155],a[168:171],a[52:55]
zt v[180:181],v235 offset:57344
zm a[52:55],v[156:159],a[172:175],a[52:55]
zt v[182:183],v235 offset:58368
zm a[48:51],v[156:159],a[164:167],a[48:51]
s_cmp_ge_u32 s69, 0x100000
zm a[48:51],v[152:155],a[160:163],a[48:51]
s_waitcnt lgkmcnt(10)
s_cselect_b32 s50, -6, 0
zt v[184:185],v235 offset:49160
zm a[64:67],v[160:163],a[160:163],a[64:67]
s_waitcnt lgkmcnt(9)
s_lshl_b32 s50, s50, 19
zm a[64:67],v[164:167],a[164:167],a[64:67]
s_add_u32 s50, s50, s69
zt v[186:187],v235 offset:50184
zm a[68:71],v[164:167],a[172:175],a[68:71]
s_add_u32 s50, s50, 0x300100
zt v[188:189],v235 offset:57352
zm a[68:71],v[160:163],a[168:171],a[68:71]
s_add_u32 m0, s45, 0x8000
zm a[72:75],v[160:163],a[176:179],a[72:75]
zt v[190:191],v235 offset:58376
zl v228,s[28:31],s50 offen offset:0 lds
zm a[72:75],v[164:167],a[180:183],a[72:75]
s_waitcnt lgkmcnt(8)
zl v229,s[28:31],s50 offen offset:1024 lds
zm a[88:91],v[172:175],a[180:183],a[88:91]
zm a[88:91],v[168:171],a[176:179],a[88:91]
zm a[84:87],v[168:171],a[168:171],a[84:87]
zm a[84:87],v[172:175],a[172:175],a[84:87]
zl v230,s[28:31],s50 offen offset:2048 lds
zm a[80:83],v[172:175],a[164:167],a[80:83]
zm a[80:83],v[168:171],a[160:163],a[80:83]
s_waitcnt vmcnt(17) lgkmcnt(0)
zm a[96:99],v[176:179],a[160:163],a[96:99]
s_cselect_b32 s71, 1, 0
s_add_u32 s60, s60, 0x46
L_pz12%=:
s_memrealtime s[66:67]
s_waitcnt lgkmcnt(0)
s_sub_i32 s62, s60, s66
s_cmp_gt_i32 s62, 0
s_cbranch_scc1 L_pz12%=
s_add_u32 s62, s62, 6
s_min_i32 s62, s62, 0
s_sub_u32 s60, s60, s62
s_cmp_lg_u32 s71, 0
s_barrier
zm a[96:99],v[180:183],a[164:167],a[96:99]
zm a[100:103],v[180:183],a[172:175],a[100:103]
zt v[128:129],v232 offset:0
zl v231,s[28:31],s50 offen offset:3072 lds
zm a[100:103],v[176:179],a[168:171],a[100:103]
zt v[130:131],v232 offset:1024
zm a[104:107],v[176:179],a[176:179],a[104:107]
zm a[104:107],v[180:183],a[180:183],a[104:107]
zt v[132:133],v232 offset:8192
zt v[134:135],v232 offset:9216
zt v[136:137],v232 offset:8
zm a[120:123],v[188:191],a[180:183],a[120:123]
zt v[138:139],v232 offset:1032
zm a[120:123],v[184:187],a[176:179],a[120:123]
zt v[140:141],v232 offset:8200
zm a[116:119],v[184:187],a[168:171],a[116:119]
s_cmp_ge_u32 s69, 0x80000
zm a[116:119],v[188:191],a[172:175],a[116:119]
s_cselect_b32 s50, -6, 0
zt v[142:143],v232 offset:9224
zm a[112:115],v[188:191],a[164:167],a[112:115]
s_lshl_b32 s50, s50, 19
zm a[112:115],v[184:187],a[160:163],a[112:115]
s_waitcnt lgkmcnt(6)
s_add_u32 s50, s50, s69
zt v[144:145],v233 offset:0
zm v[0:3],v[128:131],a[128:131],v[0:3]
s_waitcnt lgkmcnt(5)
s_add_u32 s50, s50, 0x380100
zt v[146:147],v233 offset:1024
zm v[0:3],v[132:135],a[132:135],v[0:3]
s_add_u32 m0, s45, 0xc000
zm v[4:7],v[132:135],a[140:143],v[4:7]
zt v[148:149],v233 offset:8192
zl v228,s[28:31],s50 offen offset:0 lds
zm v[4:7],v[128:131],a[136:139],v[4:7]
zt v[150:151],v233 offset:9216
zl v229,s[28:31],s50 offen offset:1024 lds
zm v[8:11],v[128:131],a[144:147],v[8:11]
zm v[8:11],v[132:135],a[148:151],v[8:11]
s_waitcnt lgkmcnt(4)
zt v[152:153],v233 offset:8
zt v[154:155],v233 offset:1032
zl v230,s[28:31],s50 offen offset:2048 lds
zt v[156:157],v233 offset:8200
zm v[24:27],v[140:143],a[148:151],v[24:27]
zt v[158:159],v233 offset:9224
zm v[24:27],v[136:139],a[144:147],v[24:27]
zm v[20:23],v[136:139],a[136:139],v[20:23]
zt v[160:161],v234 offset:0
zl v231,s[28:31],s50 offen offset:3072 lds
zm v[20:23],v[140:143],a[140:143],v[20:23]
zt v[162:163],v234 offset:1024
zm v[16:19],v[140:143],a[132:135],v[16:19]
zm v[16:19],v[136:139],a[128:131],v[16:19]
s_waitcnt lgkmcnt(8)
zt v[164:165],v234 offset:8192
zm v[32:35],v[144:147],a[128:131],v[32:35]
s_waitcnt lgkmcnt(7)
zt v[166:167],v234 offset:9216
zm v[32:35],v[148:151],a[132:135],v[32:35]
s_and_b32 m0, s47, 0xffff
zm v[36:39],v[148:151],a[140:143],v[36:39]
zt v[168:169],v234 offset:8
zl v212,s[96:99],s47 offen offset:0 lds
zm v[36:39],v[144:147],a[136:139],v[36:39]
zm v[40:43],v[144:147],a[144:147],v[40:43]
zt v[170:171],v234 offset:1032
zm v[40:43],v[148:151],a[148:151],v[40:43]
s_waitcnt lgkmcnt(6)
s_add_u32 s47, s47, 0x1000
zt v[172:173],v234 offset:8200
s_and_b32 m0, s47, 0xffff
zt v[174:175],v234 offset:9224
zl v212,s[96:99],s47 offen offset:0 lds
zt v[176:177],v235 offset:0
zm v[56:59],v[156:159],a[148:151],v[56:59]
zt v[178:179],v235 offset:1024
zm v[56:59],v[152:155],a[144:147],v[56:59]
s_add_u32 s47, s47, 0x1000
zm v[52:55],v[152:155],a[136:139],v[52:55]
s_and_b32 m0, s47, 0xffff
zt v[180:181],v235 offset:8192
zm v[52:55],v[156:159],a[140:143],v[52:55]
zt v[182:183],v235 offset:9216
zl v212,s[96:99],s47 offen offset:0 nt lds
zm v[48:51],v[156:159],a[132:135],v[48:51]
zm v[48:51],v[152:155],a[128:131],v[48:51]
s_waitcnt lgkmcnt(10)
zt v[184:185],v235 offset:8
zm v[64:67],v[160:163],a[128:131],v[64:67]
s_waitcnt lgkmcnt(9)
s_add_u32 s47, s47, 0x1000
zm v[64:67],v[164:167],a[132:135],v[64:67]
s_and_b32 m0, s47, 0xffff
zt v[186:187],v235 offset:1032
zm v[68:71],v[164:167],a[140:143],v[68:71]
zt v[188:189],v235 offset:8200
zl v212,s[96:99],s47 offen offset:0 nt lds
zm v[68:71],v[160:163],a[136:139],v[68:71]
zm v[72:75],v[160:163],a[144:147],v[72:75]
zt v[190:191],v235 offset:9224
zm v[72:75],v[164:167],a[148:151],v[72:75]
s_waitcnt lgkmcnt(8)
s_add_u32 s47, s47, 0x1000
global_load_dword v254, v255, s[52:53]
zm v[88:91],v[172:175],a[148:151],v[88:91]
zm v[88:91],v[168:171],a[144:147],v[88:91]
zm v[84:87],v[168:171],a[136:139],v[84:87]
zm v[84:87],v[172:175],a[140:143],v[84:87]
zm v[80:83],v[172:175],a[132:135],v[80:83]
zm v[80:83],v[168:171],a[128:131],v[80:83]
s_waitcnt vmcnt(13) lgkmcnt(0)
zm v[96:99],v[176:179],a[128:131],v[96:99]
s_cselect_b32 s71, 1, 0
s_add_u32 s60, s60, 0x46
L_pz13%=:
s_memrealtime s[66:67]
s_waitcnt lgkmcnt(0)
s_sub_i32 s62, s60, s66
s_cmp_gt_i32 s62, 0
s_cbranch_scc1 L_pz13%=
s_add_u32 s62, s62, 6
s_min_i32 s62, s62, 0
s_sub_u32 s60, s60, s62
s_cmp_lg_u32 s71, 0
s_barrier
zm v[96:99],v[180:183],a[132:135],v[96:99]
zk a[160:163],v226 offset:0
zm v[100:103],v[180:183],a[140:143],v[100:103]
s_and_b32 m0, s48, 0xffff
zk a[164:167],v227 offset:0
zt v[128:129],v232 offset:16384
zm v[100:103],v[176:179],a[136:139],v[100:103]
s_bitset1_b32 m0, 16
zk a[168:171],v226 offset:2048
zt v[130:131],v232 offset:17408
zm v[104:107],v[176:179],a[144:147],v[104:107]
zk a[172:175],v227 offset:2048
zl v212,s[36:39],s48 offen offset:0 nt lds
zm v[104:107],v[180:183],a[148:151],v[104:107]
zk a[176:179],v226 offset:4096
zt v[132:133],v232 offset:24576
zk a[180:183],v227 offset:4096
zt v[134:135],v232 offset:25600
zt v[136:137],v232 offset:16392
zm v[120:123],v[188:191],a[148:151],v[120:123]
zt v[138:139],v232 offset:17416
zm v[120:123],v[184:187],a[144:147],v[120:123]
zt v[140:141],v232 offset:24584
zm v[116:119],v[184:187],a[136:139],v[116:119]
zm v[116:119],v[188:191],a[140:143],v[116:119]
zt v[142:143],v232 offset:25608
zm v[112:115],v[188:191],a[132:135],v[112:115]
s_add_u32 s48, s48, 0x1000
zm v[112:115],v[184:187],a[128:131],v[112:115]
s_waitcnt lgkmcnt(9)
s_and_b32 m0, s48, 0xffff
zt v[144:145],v233 offset:16384
zm v[0:3],v[128:131],a[160:163],v[0:3]
s_waitcnt lgkmcnt(5)
s_bitset1_b32 m0, 16
zt v[146:147],v233 offset:17408
zm v[0:3],v[132:135],a[164:167],v[0:3]
zl v212,s[36:39],s48 offen offset:0 nt lds
zm v[4:7],v[132:135],a[172:175],v[4:7]
zt v[148:149],v233 offset:24576
zm v[4:7],v[128:131],a[168:171],v[4:7]
zt v[150:151],v233 offset:25600
zm v[8:11],v[128:131],a[176:179],v[8:11]
zm v[8:11],v[132:135],a[180:183],v[8:11]
s_waitcnt lgkmcnt(4)
zt v[152:153],v233 offset:16392
zt v[154:155],v233 offset:17416
zt v[156:157],v233 offset:24584
zm v[24:27],v[140:143],a[180:183],v[24:27]
zt v[158:159],v233 offset:25608
zm v[24:27],v[136:139],a[176:179],v[24:27]
zm v[20:23],v[136:139],a[168:171],v[20:23]
zt v[160:161],v234 offset:16384
zm v[20:23],v[140:143],a[172:175],v[20:23]
s_add_u32 s48, s48, 0x1000
zt v[162:163],v234 offset:17408
zm v[16:19],v[140:143],a[164:167],v[16:19]
s_and_b32 m0, s48, 0xffff
zm v[16:19],v[136:139],a[160:163],v[16:19]
s_waitcnt lgkmcnt(8)
s_bitset1_b32 m0, 16
zt v[164:165],v234 offset:24576
zm v[32:35],v[144:147],a[160:163],v[32:35]
s_waitcnt lgkmcnt(7)
zt v[166:167],v234 offset:25600
zl v212,s[36:39],s48 offen offset:0 nt lds
zm v[32:35],v[148:151],a[164:167],v[32:35]
zm v[36:39],v[148:151],a[172:175],v[36:39]
zt v[168:169],v234 offset:16392
zm v[36:39],v[144:147],a[168:171],v[36:39]
zm v[40:43],v[144:147],a[176:179],v[40:43]
zt v[170:171],v234 offset:17416
zm v[40:43],v[148:151],a[180:183],v[40:43]
s_waitcnt lgkmcnt(6)
zt v[172:173],v234 offset:24584
v_mul_f32 v0, s58, v0
v_mul_f32 v1, s58, v1
zt v[174:175],v234 offset:25608
v_mul_f32 v2, s58, v2
v_mul_f32 v3, s58, v3
v_mul_f32 v16, s58, v16
zt v[176:177],v235 offset:16384
v_mul_f32 v17, s58, v17
zm v[56:59],v[156:159],a[180:183],v[56:59]
zt v[178:179],v235 offset:17408
v_mul_f32 v18, s58, v18
zm v[56:59],v[152:155],a[176:179],v[56:59]
v_mul_f32 v19, s58, v19
zc v0,v0,v1
zm v[52:55],v[152:155],a[168:171],v[52:55]
s_add_u32 s48, s48, 0x1000
zt v[180:181],v235 offset:24576
zc v2,v2,v3
zm v[52:55],v[156:159],a[172:175],v[52:55]
s_and_b32 m0, s48, 0xffff
zt v[182:183],v235 offset:25600
zc v16,v16,v17
zm v[48:51],v[156:159],a[164:167],v[48:51]
s_bitset1_b32 m0, 16
zc v18,v18,v19
zw a192,v0
zm v[48:51],v[152:155],a[160:163],v[48:51]
s_waitcnt lgkmcnt(10)
zt v[184:185],v235 offset:16392
zl v212,s[36:39],s48 offen offset:0 nt lds
zm v[64:67],v[160:163],a[160:163],v[64:67]
s_waitcnt lgkmcnt(9)
s_add_u32 s48, s48, 0x1000
zw a193,v2
zw a194,v16
zm v[64:67],v[164:167],a[164:167],v[64:67]
zt v[186:187],v235 offset:17416
zw a195,v18
zm v[68:71],v[164:167],a[172:175],v[68:71]
zt v[188:189],v235 offset:24584
v_mul_f32 v4, s58, v4
zm v[68:71],v[160:163],a[168:171],v[68:71]
v_mul_f32 v5, s58, v5
v_mul_f32 v6, s58, v6
zm v[72:75],v[160:163],a[176:179],v[72:75]
zt v[190:191],v235 offset:25608
v_mul_f32 v7, s58, v7
zm v[72:75],v[164:167],a[180:183],v[72:75]
s_waitcnt lgkmcnt(8)
v_mul_f32 v20, s58, v20
v_mul_f32 v21, s58, v21
v_mul_f32 v22, s58, v22
v_mul_f32 v23, s58, v23
zc v4,v4,v5
zc v6,v6,v7
zc v20,v20,v21
zc v22,v22,v23
zm v[88:91],v[172:175],a[180:183],v[88:91]
zw a208,v4
zm v[88:91],v[168:171],a[176:179],v[88:91]
zw a209,v6
zw a210,v20
zm v[84:87],v[168:171],a[168:171],v[84:87]
zw a211,v22
zm v[84:87],v[172:175],a[172:175],v[84:87]
v_mul_f32 v8, s58, v8
v_mul_f32 v9, s58, v9
zm v[80:83],v[172:175],a[164:167],v[80:83]
v_mul_f32 v10, s58, v10
zm v[80:83],v[168:171],a[160:163],v[80:83]
s_waitcnt vmcnt(13) lgkmcnt(0)
v_mul_f32 v11, s58, v11
v_mul_f32 v24, s58, v24
zm v[96:99],v[176:179],a[160:163],v[96:99]
s_cselect_b32 s71, 1, 0
s_add_u32 s60, s60, 0x46
L_pz14%=:
s_memrealtime s[66:67]
s_waitcnt lgkmcnt(0)
s_sub_i32 s62, s60, s66
s_cmp_gt_i32 s62, 0
s_cbranch_scc1 L_pz14%=
s_add_u32 s62, s62, 6
s_min_i32 s62, s62, 0
s_sub_u32 s60, s60, s62
s_cmp_lg_u32 s71, 0
s_barrier
v_mul_f32 v25, s58, v25
zm v[96:99],v[180:183],a[164:167],v[96:99]
v_mul_f32 v26, s58, v26
v_mul_f32 v27, s58, v27
zm v[100:103],v[180:183],a[172:175],v[100:103]
s_and_b32 m0, s48, 0xffff
zt v[128:129],v232 offset:32768
zc v8,v8,v9
zm v[100:103],v[176:179],a[168:171],v[100:103]
s_bitset1_b32 m0, 16
zt v[130:131],v232 offset:33792
zc v10,v10,v11
zm v[104:107],v[176:179],a[176:179],v[104:107]
zl v212,s[36:39],s48 offen offset:0 nt lds
zc v24,v24,v25
zm v[104:107],v[180:183],a[180:183],v[104:107]
zt v[132:133],v232 offset:40960
zc v26,v26,v27
s_add_u32 s48, s48, 0x1000
zt v[134:135],v232 offset:41984
zw a224,v8
s_and_b32 m0, s48, 0xffff
zw a225,v10
zw a226,v24
s_bitset1_b32 m0, 16
zt v[136:137],v232 offset:32776
zw a227,v26
)"
R"(zl v212,s[36:39],s48 offen offset:0 nt lds
zm v[120:123],v[188:191],a[180:183],v[120:123]
zt v[138:139],v232 offset:33800
zm v[120:123],v[184:187],a[176:179],v[120:123]
zt v[140:141],v232 offset:40968
zm v[116:119],v[184:187],a[168:171],v[116:119]
zm v[116:119],v[188:191],a[172:175],v[116:119]
zt v[142:143],v232 offset:41992
zm v[112:115],v[188:191],a[164:167],v[112:115]
s_add_u32 s48, s48, 0x1000
zm v[112:115],v[184:187],a[160:163],v[112:115]
s_waitcnt lgkmcnt(6)
s_and_b32 m0, s48, 0xffff
zt v[144:145],v233 offset:32768
zm a[0:3],v[128:131],a[128:131],a[0:3]
s_waitcnt lgkmcnt(5)
s_bitset1_b32 m0, 16
zt v[146:147],v233 offset:33792
zm a[0:3],v[132:135],a[132:135],a[0:3]
zl v212,s[36:39],s48 offen offset:0 nt lds
zm a[4:7],v[132:135],a[140:143],a[4:7]
zt v[148:149],v233 offset:40960
zm a[4:7],v[128:131],a[136:139],a[4:7]
zt v[150:151],v233 offset:41984
zm a[8:11],v[128:131],a[144:147],a[8:11]
zm a[8:11],v[132:135],a[148:151],a[8:11]
s_waitcnt lgkmcnt(4)
s_add_u32 s48, s48, 0x1000
zt v[152:153],v233 offset:32776
s_and_b32 m0, s48, 0xffff
v_mul_f32 v32, s58, v32
v_mul_f32 v33, s58, v33
s_bitset1_b32 m0, 16
zt v[154:155],v233 offset:33800
v_mul_f32 v34, s58, v34
zt v[156:157],v233 offset:40968
zl v212,s[36:39],s48 offen offset:0 nt lds
v_mul_f32 v35, s58, v35
v_mul_f32 v48, s58, v48
zm a[24:27],v[140:143],a[148:151],a[24:27]
zt v[158:159],v233 offset:41992
v_mul_f32 v49, s58, v49
zm a[24:27],v[136:139],a[144:147],a[24:27]
v_mul_f32 v50, s58, v50
v_mul_f32 v51, s58, v51
zm a[20:23],v[136:139],a[136:139],a[20:23]
zt v[160:161],v234 offset:32768
zc v32,v32,v33
zm a[20:23],v[140:143],a[140:143],a[20:23]
zt v[162:163],v234 offset:33792
zc v34,v34,v35
zm a[16:19],v[140:143],a[132:135],a[16:19]
zc v48,v48,v49
zc v50,v50,v51
zm a[16:19],v[136:139],a[128:131],a[16:19]
s_waitcnt lgkmcnt(8)
s_add_u32 s48, s48, 0x1000
zt v[164:165],v234 offset:40960
zw a196,v32
zm a[32:35],v[144:147],a[128:131],a[32:35]
s_waitcnt lgkmcnt(7)
zt v[166:167],v234 offset:41984
buffer_load_dword v254, v214, s[40:43], s70 offen offset:0
zm a[32:35],v[148:151],a[132:135],a[32:35]
zw a197,v34
zw a198,v48
zm a[36:39],v[148:151],a[140:143],a[36:39]
zt v[168:169],v234 offset:32776
zw a199,v50
zm a[36:39],v[144:147],a[136:139],a[36:39]
v_mul_f32 v36, s58, v36
v_mul_f32 v37, s58, v37
zm a[40:43],v[144:147],a[144:147],a[40:43]
zt v[170:171],v234 offset:33800
v_mul_f32 v38, s58, v38
zm a[40:43],v[148:151],a[148:151],a[40:43]
s_waitcnt lgkmcnt(6)
zt v[172:173],v234 offset:40968
v_mul_f32 v39, s58, v39
v_mul_f32 v52, s58, v52
v_mul_f32 v53, s58, v53
zt v[174:175],v234 offset:41992
buffer_load_dword v254, v214, s[40:43], s70 offen offset:1024
v_mul_f32 v54, s58, v54
v_mul_f32 v55, s58, v55
zt v[176:177],v235 offset:32768
zc v36,v36,v37
zm a[56:59],v[156:159],a[148:151],a[56:59]
zt v[178:179],v235 offset:33792
zc v38,v38,v39
zm a[56:59],v[152:155],a[144:147],a[56:59]
zc v52,v52,v53
zc v54,v54,v55
zm a[52:55],v[152:155],a[136:139],a[52:55]
zt v[180:181],v235 offset:40960
zw a212,v36
zm a[52:55],v[156:159],a[140:143],a[52:55]
zt v[182:183],v235 offset:41984
zw a213,v38
zm a[48:51],v[156:159],a[132:135],a[48:51]
zw a214,v52
zw a215,v54
zm a[48:51],v[152:155],a[128:131],a[48:51]
s_waitcnt lgkmcnt(10)
zt v[184:185],v235 offset:32776
buffer_load_dword v254, v214, s[40:43], s70 offen offset:2048
zm a[64:67],v[160:163],a[128:131],a[64:67]
s_waitcnt lgkmcnt(9)
v_mul_f32 v40, s58, v40
v_mul_f32 v41, s58, v41
zm a[64:67],v[164:167],a[132:135],a[64:67]
zt v[186:187],v235 offset:33800
v_mul_f32 v42, s58, v42
zm a[68:71],v[164:167],a[140:143],a[68:71]
zt v[188:189],v235 offset:40968
v_mul_f32 v43, s58, v43
zm a[68:71],v[160:163],a[136:139],a[68:71]
v_mul_f32 v56, s58, v56
v_mul_f32 v57, s58, v57
zm a[72:75],v[160:163],a[144:147],a[72:75]
zt v[190:191],v235 offset:41992
v_mul_f32 v58, s58, v58
zm a[72:75],v[164:167],a[148:151],a[72:75]
s_waitcnt lgkmcnt(8)
v_mul_f32 v59, s58, v59
zc v40,v40,v41
buffer_load_dword v254, v214, s[40:43], s70 offen offset:3072
zc v42,v42,v43
zc v56,v56,v57
zc v58,v58,v59
zw a228,v40
zw a229,v42
zw a230,v56
zm a[88:91],v[172:175],a[148:151],a[88:91]
zw a231,v58
zm a[88:91],v[168:171],a[144:147],a[88:91]
zm a[84:87],v[168:171],a[136:139],a[84:87]
zm a[84:87],v[172:175],a[140:143],a[84:87]
zm a[80:83],v[172:175],a[132:135],a[80:83]
zm a[80:83],v[168:171],a[128:131],a[80:83]
s_waitcnt vmcnt(17) lgkmcnt(0)
zm a[96:99],v[176:179],a[128:131],a[96:99]
s_cselect_b32 s71, 1, 0
s_add_u32 s60, s60, 0x46
L_pz15%=:
s_memrealtime s[66:67]
s_waitcnt lgkmcnt(0)
s_sub_i32 s62, s60, s66
s_cmp_gt_i32 s62, 0
s_cbranch_scc1 L_pz15%=
s_add_u32 s62, s62, 6
s_min_i32 s62, s62, 0
s_sub_u32 s60, s60, s62
s_cmp_lg_u32 s71, 0
s_barrier
zm a[96:99],v[180:183],a[132:135],a[96:99]
zm a[100:103],v[180:183],a[140:143],a[100:103]
s_and_b32 m0, s48, 0xffff
zt v[128:129],v232 offset:49152
zm a[100:103],v[176:179],a[136:139],a[100:103]
s_bitset1_b32 m0, 16
zt v[130:131],v232 offset:50176
zm a[104:107],v[176:179],a[144:147],a[104:107]
zl v212,s[36:39],s48 offen offset:0 nt lds
zm a[104:107],v[180:183],a[148:151],a[104:107]
zt v[132:133],v232 offset:57344
zt v[134:135],v232 offset:58368
v_mul_f32 v64, s58, v64
v_mul_f32 v65, s58, v65
zt v[136:137],v232 offset:49160
v_mul_f32 v66, s58, v66
v_mul_f32 v67, s58, v67
v_mul_f32 v80, s58, v80
zm a[120:123],v[188:191],a[148:151],a[120:123]
zt v[138:139],v232 offset:50184
v_mul_f32 v81, s58, v81
zm a[120:123],v[184:187],a[144:147],a[120:123]
zt v[140:141],v232 offset:57352
v_mul_f32 v82, s58, v82
zm a[116:119],v[184:187],a[136:139],a[116:119]
v_mul_f32 v83, s58, v83
zc v64,v64,v65
zm a[116:119],v[188:191],a[140:143],a[116:119]
zt v[142:143],v232 offset:58376
zc v66,v66,v67
zm a[112:115],v[188:191],a[132:135],a[112:115]
s_add_u32 s48, s48, 0x1000
zc v80,v80,v81
zc v82,v82,v83
zm a[112:115],v[184:187],a[128:131],a[112:115]
s_waitcnt lgkmcnt(6)
s_and_b32 m0, s48, 0xffff
zt v[144:145],v233 offset:49152
zw a200,v64
zm a[0:3],v[128:131],a[160:163],a[0:3]
s_waitcnt lgkmcnt(5)
s_bitset1_b32 m0, 16
zt v[146:147],v233 offset:50176
zw a201,v66
zm a[0:3],v[132:135],a[164:167],a[0:3]
zl v212,s[36:39],s48 offen offset:0 nt lds
zw a202,v80
zm a[4:7],v[132:135],a[172:175],a[4:7]
zt v[148:149],v233 offset:57344
zw a203,v82
zm a[4:7],v[128:131],a[168:171],a[4:7]
zt v[150:151],v233 offset:58368
v_mul_f32 v68, s58, v68
zm a[8:11],v[128:131],a[176:179],a[8:11]
v_mul_f32 v69, s58, v69
v_mul_f32 v70, s58, v70
zm a[8:11],v[132:135],a[180:183],a[8:11]
s_waitcnt lgkmcnt(4)
zt v[152:153],v233 offset:49160
v_mul_f32 v71, s58, v71
v_mul_f32 v84, s58, v84
v_mul_f32 v85, s58, v85
zt v[154:155],v233 offset:50184
v_mul_f32 v86, s58, v86
zt v[156:157],v233 offset:57352
v_mul_f32 v87, s58, v87
zc v68,v68,v69
zc v70,v70,v71
zm a[24:27],v[140:143],a[180:183],a[24:27]
zt v[158:159],v233 offset:58376
zc v84,v84,v85
zm a[24:27],v[136:139],a[176:179],a[24:27]
zc v86,v86,v87
zw a216,v68
zm a[20:23],v[136:139],a[168:171],a[20:23]
zt v[160:161],v234 offset:49152
zw a217,v70
zm a[20:23],v[140:143],a[172:175],a[20:23]
s_add_u32 s48, s48, 0x1000
zt v[162:163],v234 offset:50176
zw a218,v84
zm a[16:19],v[140:143],a[164:167],a[16:19]
s_and_b32 m0, s48, 0xffff
zw a219,v86
v_mul_f32 v72, s58, v72
zm a[16:19],v[136:139],a[160:163],a[16:19]
s_waitcnt lgkmcnt(8)
s_bitset1_b32 m0, 16
zt v[164:165],v234 offset:57344
v_mul_f32 v73, s58, v73
zm a[32:35],v[144:147],a[160:163],a[32:35]
s_waitcnt lgkmcnt(7)
zt v[166:167],v234 offset:58368
zl v212,s[36:39],s48 offen offset:0 nt lds
zm a[32:35],v[148:151],a[164:167],a[32:35]
v_mul_f32 v74, s58, v74
v_mul_f32 v75, s58, v75
zm a[36:39],v[148:151],a[172:175],a[36:39]
zt v[168:169],v234 offset:49160
v_mul_f32 v88, s58, v88
zm a[36:39],v[144:147],a[168:171],a[36:39]
v_mul_f32 v89, s58, v89
v_mul_f32 v90, s58, v90
zm a[40:43],v[144:147],a[176:179],a[40:43]
zt v[170:171],v234 offset:50184
v_mul_f32 v91, s58, v91
zm a[40:43],v[148:151],a[180:183],a[40:43]
s_waitcnt lgkmcnt(6)
zt v[172:173],v234 offset:57352
zc v72,v72,v73
zc v74,v74,v75
zc v88,v88,v89
zt v[174:175],v234 offset:58376
zc v90,v90,v91
zw a232,v72
zw a233,v74
zt v[176:177],v235 offset:49152
zw a234,v88
zm a[56:59],v[156:159],a[180:183],a[56:59]
zt v[178:179],v235 offset:50176
zw a235,v90
zm a[56:59],v[152:155],a[176:179],a[56:59]
zm a[52:55],v[152:155],a[168:171],a[52:55]
s_add_u32 s48, s48, 0x1000
zt v[180:181],v235 offset:57344
zm a[52:55],v[156:159],a[172:175],a[52:55]
s_and_b32 m0, s48, 0xffff
zt v[182:183],v235 offset:58368
zm a[48:51],v[156:159],a[164:167],a[48:51]
s_bitset1_b32 m0, 16
zm a[48:51],v[152:155],a[160:163],a[48:51]
s_waitcnt lgkmcnt(10)
zt v[184:185],v235 offset:49160
zl v212,s[36:39],s48 offen offset:0 nt lds
zm a[64:67],v[160:163],a[160:163],a[64:67]
s_waitcnt lgkmcnt(9)
s_add_u32 s48, s48, 0x1000
zm a[64:67],v[164:167],a[164:167],a[64:67]
zt v[186:187],v235 offset:50184
zm a[68:71],v[164:167],a[172:175],a[68:71]
zt v[188:189],v235 offset:57352
zm a[68:71],v[160:163],a[168:171],a[68:71]
zm a[72:75],v[160:163],a[176:179],a[72:75]
zt v[190:191],v235 offset:58376
zm a[72:75],v[164:167],a[180:183],a[72:75]
s_waitcnt lgkmcnt(8)
v_mul_f32 v96, s58, v96
v_mul_f32 v97, s58, v97
v_mul_f32 v98, s58, v98
zk a[128:131],v196 offset:0
v_mul_f32 v99, s58, v99
zm a[88:91],v[172:175],a[180:183],a[88:91]
v_mul_f32 v112, s58, v112
v_mul_f32 v113, s58, v113
zm a[88:91],v[168:171],a[176:179],a[88:91]
zk a[132:135],v197 offset:0
v_mul_f32 v114, s58, v114
zm a[84:87],v[168:171],a[168:171],a[84:87]
v_mul_f32 v115, s58, v115
zc v96,v96,v97
zm a[84:87],v[172:175],a[172:175],a[84:87]
zk a[136:139],v198 offset:0
zc v98,v98,v99
zm a[80:83],v[172:175],a[164:167],a[80:83]
zc v112,v112,v113
zc v114,v114,v115
zm a[80:83],v[168:171],a[160:163],a[80:83]
s_waitcnt lgkmcnt(9)
zk a[140:143],v199 offset:0
zw a204,v96
zm a[96:99],v[176:179],a[160:163],a[96:99]
s_waitcnt lgkmcnt(8)
zw a205,v98
zw a206,v112
zm a[96:99],v[180:183],a[164:167],a[96:99]
zk a[144:147],v196 offset:4096
zw a207,v114
zm a[100:103],v[180:183],a[172:175],a[100:103]
v_mul_f32 v100, s58, v100
v_mul_f32 v101, s58, v101
zm a[100:103],v[176:179],a[168:171],a[100:103]
zk a[148:151],v197 offset:4096
v_mul_f32 v102, s58, v102
zm a[104:107],v[176:179],a[176:179],a[104:107]
v_mul_f32 v103, s58, v103
v_mul_f32 v116, s58, v116
zm a[104:107],v[180:183],a[180:183],a[104:107]
s_waitcnt lgkmcnt(6)
zk a[152:155],v198 offset:4096
v_mul_f32 v117, s58, v117
v_mul_f32 v118, s58, v118
v_mul_f32 v119, s58, v119
zk a[156:159],v199 offset:4096
zc v100,v100,v101
zc v102,v102,v103
zc v116,v116,v117
zc v118,v118,v119
zm a[120:123],v[188:191],a[180:183],a[120:123]
zw a220,v100
zw a221,v102
zm a[120:123],v[184:187],a[176:179],a[120:123]
zw a222,v116
zm a[116:119],v[184:187],a[168:171],a[116:119]
zw a223,v118
v_mul_f32 v104, s58, v104
zm a[116:119],v[188:191],a[172:175],a[116:119]
v_mul_f32 v105, s58, v105
zm a[112:115],v[188:191],a[164:167],a[112:115]
v_mul_f32 v106, s58, v106
v_mul_f32 v107, s58, v107
zm a[112:115],v[184:187],a[160:163],a[112:115]
v_mul_f32 v120, s58, v120
v_mul_f32 v121, s58, v121
v_mul_f32 v122, s58, v122
v_mul_f32 v123, s58, v123
zc v104,v104,v105
zc v106,v106,v107
zc v120,v120,v121
zc v122,v122,v123
zw a236,v104
zw a237,v106
zw a238,v120
zw a239,v122
.p2align 3
s_memtime s[56:57]
s_memrealtime s[66:67]
s_waitcnt lgkmcnt(0)
s_sub_u32 s61, s66, s61
s_sub_u32 s79, s56, s79
s_mov_b32 s72, s79
s_mul_i32 s62, s61, 0xa0
s_mul_i32 s71, s79, 10
s_cmp_lt_u32 s71, s62
s_cselect_b32 s71, 0, 1
v_cvt_f32_u32 v12, s61
v_cvt_f32_u32 v13, s79
v_rcp_f32 v13, v13
s_nop 0
v_mul_f32 v13, 0x46ea6000, v13
v_add_f32 v13, 1.0, v13
v_mul_f32 v12, v12, v13
v_mul_f32 v12, 0xbd124925, v12
v_add_f32 v12, 0x436e55e2, v12
v_cvt_i32_f32 v12, v12
s_nop 0
v_readfirstlane_b32 s49, v12
s_nop 0
s_min_i32 s49, s49, 0x92
s_max_i32 s49, s49, 0x64
s_mov_b32 s40, s20
s_and_b32 s41, s21, 0xffff
s_mov_b32 s42, 0x20000
s_mov_b32 s43, 0x20000
s_mov_b32 s24, s14
s_and_b32 s25, s15, 0xffff
s_mov_b32 s26, 0xffd00
s_mov_b32 s27, 0x20000
s_mov_b32 s28, s18
s_and_b32 s29, s19, 0xffff
s_mov_b32 s30, 0x77840
s_mov_b32 s31, 0x20000
s_nop 1
zm v[0:3],a[128:131],a[192:195],0
v_add_u32 v216, 1, v216
zr v236,a0
zm v[0:3],a[132:135],a[196:199],v[0:3]
zr v237,a1
zm v[0:3],a[136:139],a[200:203],v[0:3]
zr v238,a2
zr v239,a3
zm v[0:3],a[140:143],a[204:207],v[0:3]
zr v240,a16
zm v[4:7],a[156:159],a[204:207],0
zr v241,a17
zr v242,a18
zm v[4:7],a[152:155],a[200:203],v[4:7]
zr v243,a19
zm v[4:7],a[148:151],a[196:199],v[4:7]
v_mul_f32 v236, s58, v236
v_mul_f32 v237, s58, v237
zm v[4:7],a[144:147],a[192:195],v[4:7]
v_mul_f32 v238, s58, v238
zm v[12:15],a[144:147],a[208:211],0
v_mul_f32 v239, s58, v239
v_mul_f32 v240, s58, v240
zm v[12:15],a[148:151],a[212:215],v[12:15]
v_mul_f32 v241, s58, v241
zm v[12:15],a[152:155],a[216:219],v[12:15]
v_mul_f32 v242, s58, v242
v_mul_f32 v243, s58, v243
zm v[12:15],a[156:159],a[220:223],v[12:15]
zc v128,v236,v237
zm v[8:11],a[140:143],a[220:223],0
zc v129,v238,v239
zc v130,v240,v241
zm v[8:11],a[136:139],a[216:219],v[8:11]
zc v131,v242,v243
zm v[8:11],a[132:135],a[212:215],v[8:11]
zr v244,a4
zr v245,a5
zm v[8:11],a[128:131],a[208:211],v[8:11]
zr v246,a6
zm v[16:19],a[128:131],a[224:227],0
zr v247,a7
zr v248,a20
zm v[16:19],a[132:135],a[228:231],v[16:19]
zr v249,a21
zm v[16:19],a[136:139],a[232:235],v[16:19]
zr v250,a22
zr v251,a23
zm v[16:19],a[140:143],a[236:239],v[16:19]
v_mul_f32 v244, s58, v244
zm v[20:23],a[156:159],a[236:239],0
v_mul_f32 v245, s58, v245
v_mul_f32 v246, s58, v246
zm v[20:23],a[152:155],a[232:235],v[20:23]
v_mul_f32 v247, s58, v247
zm v[20:23],a[148:151],a[228:231],v[20:23]
v_mul_f32 v248, s58, v248
v_mul_f32 v249, s58, v249
zm v[20:23],a[144:147],a[224:227],v[20:23]
v_mul_f32 v250, s58, v250
v_mul_f32 v251, s58, v251
zc v132,v244,v245
zc v133,v246,v247
zc v134,v248,v249
zc v135,v250,v251
zk a[128:131],v196 offset:8192
zk a[132:135],v197 offset:8192
zk a[136:139],v198 offset:8192
zk a[140:143],v199 offset:8192
zk a[144:147],v196 offset:12288
zk a[148:151],v197 offset:12288
zk a[152:155],v198 offset:12288
zk a[156:159],v199 offset:12288
.p2align 3
zr v236,a8
zr v237,a9
zr v238,a10
zr v239,a11
zr v240,a24
zr v241,a25
zr v242,a26
zr v243,a27
zp v[236:237],v[236:237],s[58:59]
zp v[238:239],v[238:239],s[58:59]
zp v[240:241],v[240:241],s[58:59]
zp v[242:243],v[242:243],s[58:59]
zc v136,v236,v237
zc v137,v238,v239
zc v138,v240,v241
zc v139,v242,v243
zr v236,a32
zr v237,a33
zr v238,a34
zr v239,a35
zr v240,a48
zr v241,a49
zr v242,a50
zr v243,a51
zp v[236:237],v[236:237],s[58:59]
zp v[238:239],v[238:239],s[58:59]
zp v[240:241],v[240:241],s[58:59]
zp v[242:243],v[242:243],s[58:59]
zc v144,v236,v237
zc v145,v238,v239
zc v146,v240,v241
zc v147,v242,v243
zr v244,a36
zr v245,a37
zr v246,a38
zr v247,a39
zr v248,a52
zr v249,a53
zr v250,a54
zr v251,a55
zp v[244:245],v[244:245],s[58:59]
zp v[246:247],v[246:247],s[58:59]
zp v[248:249],v[248:249],s[58:59]
zp v[250:251],v[250:251],s[58:59]
zc v148,v244,v245
zc v149,v246,v247
zc v150,v248,v249
zc v151,v250,v251
zr v236,a40
zr v237,a41
zr v238,a42
zr v239,a43
zr v240,a56
zr v241,a57
zr v242,a58
zr v243,a59
zp v[236:237],v[236:237],s[58:59]
zp v[238:239],v[238:239],s[58:59]
zp v[240:241],v[240:241],s[58:59]
zp v[242:243],v[242:243],s[58:59]
zc v152,v236,v237
zc v153,v238,v239
zc v154,v240,v241
zc v155,v242,v243
zr v236,a64
zr v237,a65
zr v238,a66
zr v239,a67
zr v240,a80
zr v241,a81
zr v242,a82
zr v243,a83
zp v[236:237],v[236:237],s[58:59]
zp v[238:239],v[238:239],s[58:59]
zp v[240:241],v[240:241],s[58:59]
zp v[242:243],v[242:243],s[58:59]
zc v160,v236,v237
zc v161,v238,v239
zc v162,v240,v241
zc v163,v242,v243
zr v244,a68
zr v245,a69
zr v246,a70
zr v247,a71
zr v248,a84
zr v249,a85
zr v250,a86
zr v251,a87
zp v[244:245],v[244:245],s[58:59]
zp v[246:247],v[246:247],s[58:59]
zp v[248:249],v[248:249],s[58:59]
zp v[250:251],v[250:251],s[58:59]
zc v164,v244,v245
zc v165,v246,v247
zc v166,v248,v249
zc v167,v250,v251
zr v236,a72
zr v237,a73
zr v238,a74
zr v239,a75
zr v240,a88
zr v241,a89
zr v242,a90
zr v243,a91
zp v[236:237],v[236:237],s[58:59]
zp v[238:239],v[238:239],s[58:59]
zp v[240:241],v[240:241],s[58:59]
zp v[242:243],v[242:243],s[58:59]
zc v168,v236,v237
zc v169,v238,v239
zc v170,v240,v241
zc v171,v242,v243
zr v236,a96
zr v237,a97
zr v238,a98
zr v239,a99
zr v240,a112
zr v241,a113
zr v242,a114
zr v243,a115
zp v[236:237],v[236:237],s[58:59]
zp v[238:239],v[238:239],s[58:59]
zp v[240:241],v[240:241],s[58:59]
zp v[242:243],v[242:243],s[58:59]
zc v176,v236,v237
zc v177,v238,v239
zc v178,v240,v241
zc v179,v242,v243
zr v244,a100
zr v245,a101
zr v246,a102
zr v247,a103
zr v248,a116
zr v249,a117
zr v250,a118
zr v251,a119
zp v[244:245],v[244:245],s[58:59]
zp v[246:247],v[246:247],s[58:59]
zp v[248:249],v[248:249],s[58:59]
zp v[250:251],v[250:251],s[58:59]
zc v180,v244,v245
zc v181,v246,v247
zc v182,v248,v249
zc v183,v250,v251
zr v236,a104
zr v237,a105
zr v238,a106
zr v239,a107
zr v240,a120
zr v241,a121
zr v242,a122
zr v243,a123
zp v[236:237],v[236:237],s[58:59]
zp v[238:239],v[238:239],s[58:59]
zp v[240:241],v[240:241],s[58:59]
zp v[242:243],v[242:243],s[58:59]
zc v184,v236,v237
zc v185,v238,v239
zc v186,v240,v241
zc v187,v242,v243
v_mov_b32 v218, 0xff800000
v_mov_b32 v219, 0xff800000
v_mov_b32 v220, 0xff800000
v_mov_b32 v221, 0xff800000
v_mov_b32 v222, 0xff800000
v_mov_b32 v223, 0xff800000
v_mov_b32 v224, 0xff800000
v_mov_b32 v225, 0xff800000
s_waitcnt vmcnt(12) lgkmcnt(0)
s_barrier
s_memrealtime s[66:67]
s_waitcnt lgkmcnt(0)
s_mov_b32 s61, s66
s_mov_b32 s79, s66
s_mov_b32 s70, 0
s_add_u32 s69, s66, 0
s_mov_b32 s46, 2
s_mov_b32 s68, s49
s_cmp_lg_u32 s76, 0
s_cbranch_scc1 WL_safe%=
s_nop 7
WL_start%=:
.p2align 3
v_max3_f32 v236, v0, v1, v218
v_max3_f32 v237, v8, v9, v219
v_max3_f32 v238, v16, v17, v220
v_max3_f32 v239, v24, v25, v221
v_max3_f32 v236, v236, v2, v3
v_max3_f32 v237, v237, v10, v11
v_max3_f32 v238, v238, v18, v19
v_max3_f32 v239, v239, v26, v27
v_max3_f32 v236, v236, v4, v5
v_max3_f32 v237, v237, v12, v13
v_max3_f32 v238, v238, v20, v21
v_max3_f32 v239, v239, v28, v29
v_max3_f32 v236, v236, v6, v7
v_max3_f32 v237, v237, v14, v15
v_max3_f32 v238, v238, v22, v23
v_max3_f32 v239, v239, v30, v31
v_mov_b32 v240, v236
v_mov_b32 v241, v237
v_mov_b32 v242, v238
v_mov_b32 v243, v239
v_mov_b32 v80, 0
v_permlane16_swap_b32 v236, v240
v_permlane16_swap_b32 v237, v241
v_permlane16_swap_b32 v238, v242
v_permlane16_swap_b32 v239, v243
v_max_f32 v236, v236, v240
v_max_f32 v237, v237, v241
v_max_f32 v238, v238, v242
v_max_f32 v239, v239, v243
v_mov_b32 v240, v236
v_mov_b32 v241, v237
v_mov_b32 v242, v238
v_mov_b32 v243, v239
v_mov_b32 v81, 0
v_permlane32_swap_b32 v236, v240
v_permlane32_swap_b32 v237, v241
v_permlane32_swap_b32 v238, v242
v_permlane32_swap_b32 v239, v243
v_max_f32 v236, v236, v240
v_max_f32 v237, v237, v241
v_max_f32 v238, v238, v242
v_max_f32 v239, v239, v243
.p2align 3
zp v[64:65],v[236:237],-1.0 op_sel:[0,0] op_sel_hi:[0,0]
zp v[66:67],v[236:237],-1.0 op_sel:[0,0] op_sel_hi:[0,0]
zp v[68:69],v[236:237],-1.0 op_sel:[1,0] op_sel_hi:[1,0]
zp v[70:71],v[236:237],-1.0 op_sel:[1,0] op_sel_hi:[1,0]
zp v[72:73],v[238:239],-1.0 op_sel:[0,0] op_sel_hi:[0,0]
zp v[74:75],v[238:239],-1.0 op_sel:[0,0] op_sel_hi:[0,0]
zp v[76:77],v[238:239],-1.0 op_sel:[1,0] op_sel_hi:[1,0]
zp v[78:79],v[238:239],-1.0 op_sel:[1,0] op_sel_hi:[1,0]
v_pk_add_f32 v[0:1], v[0:1], v[64:65]
v_pk_add_f32 v[2:3], v[2:3], v[64:65]
v_pk_add_f32 v[4:5], v[4:5], v[64:65]
v_pk_add_f32 v[6:7], v[6:7], v[64:65]
v_pk_add_f32 v[8:9], v[8:9], v[68:69]
v_pk_add_f32 v[10:11], v[10:11], v[68:69]
v_pk_add_f32 v[12:13], v[12:13], v[68:69]
v_pk_add_f32 v[14:15], v[14:15], v[68:69]
v_pk_add_f32 v[16:17], v[16:17], v[72:73]
v_pk_add_f32 v[18:19], v[18:19], v[72:73]
v_pk_add_f32 v[20:21], v[20:21], v[72:73]
v_pk_add_f32 v[22:23], v[22:23], v[72:73]
v_pk_add_f32 v[24:25], v[24:25], v[76:77]
v_pk_add_f32 v[26:27], v[26:27], v[76:77]
v_pk_add_f32 v[28:29], v[28:29], v[76:77]
v_pk_add_f32 v[30:31], v[30:31], v[76:77]
ze v0,v0
ze v1,v1
ze v2,v2
ze v3,v3
ze v4,v4
ze v5,v5
ze v6,v6
ze v7,v7
ze v8,v8
ze v9,v9
ze v10,v10
ze v11,v11
ze v12,v12
ze v13,v13
ze v14,v14
ze v15,v15
v_pk_mov_b32 v[82:83], v[80:81], v[80:81]
v_pk_mov_b32 v[84:85], v[80:81], v[80:81]
v_pk_mov_b32 v[86:87], v[80:81], v[80:81]
v_pk_mov_b32 v[218:219], v[222:223], v[222:223] op_sel:[0,1]
v_pk_mov_b32 v[220:221], v[224:225], v[224:225] op_sel:[0,1]
zc v96,v0,v1
zc v97,v2,v3
zc v98,v4,v5
zc v99,v6,v7
zc v100,v8,v9
zc v101,v10,v11
zc v102,v12,v13
zc v103,v14,v15
v_mov_b32 v108, 0
v_mov_b32 v109, 0
v_mov_b32 v110, 0
v_mov_b32 v111, 0
v_mov_b32 v124, 0
v_mov_b32 v125, 0
v_mov_b32 v126, 0
v_mov_b32 v127, 0
zm v[32:35],a[128:131],a[192:195],v[64:67]
ze v16,v16
zm v[32:35],a[132:135],a[196:199],v[32:35]
zt a[160:161],v200 offset:0
zm v[32:35],a[136:139],a[200:203],v[32:35]
ze v17,v17
zm v[32:35],a[140:143],a[204:207],v[32:35]
zt a[162:163],v200 offset:4096
zc v104,v16,v17
zm v[36:39],a[156:159],a[204:207],v[64:67]
s_and_b32 m0, s47, 0xffff
ze v18,v18
zm v[36:39],a[152:155],a[200:203],v[36:39]
zt a[164:165],v201 offset:0
zl v212,s[96:99],s47 offen offset:0 lds
zm v[36:39],a[148:151],a[196:199],v[36:39]
ze v19,v19
zm v[36:39],a[144:147],a[192:195],v[36:39]
zt a[166:167],v201 offset:4096
zc v105,v18,v19
zm v[44:47],a[144:147],a[208:211],v[68:71]
ze v20,v20
zm v[44:47],a[148:151],a[212:215],v[44:47]
zt a[168:169],v202 offset:0
zm v[44:47],a[152:155],a[216:219],v[44:47]
ze v21,v21
zm v[44:47],a[156:159],a[220:223],v[44:47]
s_add_u32 s47, s47, 0x1000
zt a[170:171],v202 offset:4096
zc v106,v20,v21
zm v[40:43],a[140:143],a[220:223],v[68:71]
s_and_b32 m0, s47, 0xffff
ze v22,v22
zm v[40:43],a[136:139],a[216:219],v[40:43]
zt a[172:173],v203 offset:0
zl v212,s[96:99],s47 offen offset:0 lds
zm v[40:43],a[132:135],a[212:215],v[40:43]
ze v23,v23
zm v[40:43],a[128:131],a[208:211],v[40:43]
zt a[174:175],v203 offset:4096
zc v107,v22,v23
zm v[48:51],a[128:131],a[224:227],v[72:75]
zm v[48:51],a[132:135],a[228:231],v[48:51]
zt a[176:177],v204 offset:0
zm v[48:51],a[136:139],a[232:235],v[48:51]
zm v[48:51],a[140:143],a[236:239],v[48:51]
s_add_u32 s47, s47, 0x1000
zt a[178:179],v204 offset:4096
zm v[52:55],a[156:159],a[236:239],v[72:75]
s_and_b32 m0, s47, 0xffff
zm v[52:55],a[152:155],a[232:235],v[52:55]
zt a[180:181],v205 offset:0
zl v212,s[96:99],s47 offen offset:0 nt lds
zm v[52:55],a[148:151],a[228:231],v[52:55]
zm v[52:55],a[144:147],a[224:227],v[52:55]
zt a[182:183],v205 offset:4096
zt a[184:185],v206 offset:0
s_waitcnt lgkmcnt(5)
s_add_u32 s47, s47, 0x1000
zt a[186:187],v206 offset:4096
s_and_b32 m0, s47, 0xffff
zt a[188:189],v207 offset:0
zl v212,s[96:99],s47 offen offset:0 nt lds
s_add_u32 s47, s47, 0x1000
zt a[190:191],v207 offset:4096
zm a[0:3],a[160:163],v[96:99],0
ze v32,v32
zm a[16:19],a[164:167],v[96:99],0
zk a[128:131],v196 offset:16384
zm a[20:23],a[164:167],v[100:103],0
ze v33,v33
zm a[4:7],a[160:163],v[100:103],0
zk a[132:135],v197 offset:16384
zc v112,v32,v33
zm a[8:11],a[160:163],v[104:107],0
ze v34,v34
zm a[24:27],a[164:167],v[104:107],0
zk a[136:139],v198 offset:16384
ze v35,v35
zk a[140:143],v199 offset:16384
zc v113,v34,v35
v_smfmac_f32_16x16x64_bf16 v[80:83], v[192:195], v[96:103], v253
ze v36,v36
v_smfmac_f32_16x16x64_bf16 v[84:87], v[192:195], v[104:111], v253
zk a[144:147],v196 offset:20480
ze v37,v37
zk a[148:151],v197 offset:20480
zc v114,v36,v37
zm a[56:59],a[172:175],v[104:107],0
s_waitcnt lgkmcnt(6)
ze v38,v38
zm a[40:43],a[168:171],v[104:107],0
zk a[152:155],v198 offset:20480
zm a[36:39],a[168:171],v[100:103],0
ze v39,v39
zm a[52:55],a[172:175],v[100:103],0
s_and_b32 m0, s48, 0xffff
zk a[156:159],v199 offset:20480
zc v115,v38,v39
zm a[48:51],a[172:175],v[96:99],0
s_bitset1_b32 m0, 16
ze v40,v40
zm a[32:35],a[168:171],v[96:99],0
zl v212,s[36:39],s48 offen offset:0 nt lds
zm a[64:67],a[176:179],v[96:99],0
s_add_u32 s48, s48, 0x1000
ze v41,v41
zm a[80:83],a[180:183],v[96:99],0
s_and_b32 m0, s48, 0xffff
zc v116,v40,v41
zm a[84:87],a[180:183],v[100:103],0
s_bitset1_b32 m0, 16
ze v42,v42
zm a[68:71],a[176:179],v[100:103],0
zl v212,s[36:39],s48 offen offset:0 nt lds
zm a[72:75],a[176:179],v[104:107],0
s_add_u32 s48, s48, 0x1000
ze v43,v43
zm a[88:91],a[180:183],v[104:107],0
s_and_b32 m0, s48, 0xffff
zc v117,v42,v43
s_bitset1_b32 m0, 16
ze v44,v44
zl v212,s[36:39],s48 offen offset:0 nt lds
s_add_u32 s48, s48, 0x1000
ze v45,v45
s_and_b32 m0, s48, 0xffff
zc v118,v44,v45
zm a[120:123],a[188:191],v[104:107],0
s_bitset1_b32 m0, 16
ze v46,v46
zm a[104:107],a[184:187],v[104:107],0
zl v212,s[36:39],s48 offen offset:0 nt lds
zm a[100:103],a[184:187],v[100:103],0
s_add_u32 s48, s48, 0x1000
ze v47,v47
zm a[116:119],a[188:191],v[100:103],0
zc v119,v46,v47
zm a[112:115],a[188:191],v[96:99],0
s_waitcnt lgkmcnt(0)
zm a[96:99],a[184:187],v[96:99],0
s_nop 7
WL_A1%=:
zm v[0:3],a[128:131],a[192:195],v[64:67]
ze v48,v48
zm v[0:3],a[132:135],a[196:199],v[0:3]
zt a[160:161],v200 offset:8192
zm v[0:3],a[136:139],a[200:203],v[0:3]
ze v49,v49
zm v[0:3],a[140:143],a[204:207],v[0:3]
zt a[162:163],v200 offset:12288
zc v120,v48,v49
zm v[4:7],a[156:159],a[204:207],v[64:67]
ze v50,v50
zm v[4:7],a[152:155],a[200:203],v[4:7]
zt a[164:165],v201 offset:8192
zm v[4:7],a[148:151],a[196:199],v[4:7]
ze v51,v51
zm v[4:7],a[144:147],a[192:195],v[4:7]
zt a[166:167],v201 offset:12288
zc v121,v50,v51
zm v[12:15],a[144:147],a[208:211],v[68:71]
ze v52,v52
zm v[12:15],a[148:151],a[212:215],v[12:15]
zt a[168:169],v202 offset:8192
zm v[12:15],a[152:155],a[216:219],v[12:15]
ze v53,v53
zm v[12:15],a[156:159],a[220:223],v[12:15]
zt a[170:171],v202 offset:12288
zc v122,v52,v53
zm v[8:11],a[140:143],a[220:223],v[68:71]
ze v54,v54
zm v[8:11],a[136:139],a[216:219],v[8:11]
zt a[172:173],v203 offset:8192
zm v[8:11],a[132:135],a[212:215],v[8:11]
ze v55,v55
zm v[8:11],a[128:131],a[208:211],v[8:11]
zt a[174:175],v203 offset:12288
zc v123,v54,v55
zm v[16:19],a[128:131],a[224:227],v[72:75]
zm v[16:19],a[132:135],a[228:231],v[16:19]
zt a[176:177],v204 offset:8192
zm v[16:19],a[136:139],a[232:235],v[16:19]
zm v[16:19],a[140:143],a[236:239],v[16:19]
zt a[178:179],v204 offset:12288
zm v[20:23],a[156:159],a[236:239],v[72:75]
zm v[20:23],a[152:155],a[232:235],v[20:23]
zt a[180:181],v205 offset:8192
zm v[20:23],a[148:151],a[228:231],v[20:23]
zm v[20:23],a[144:147],a[224:227],v[20:23]
zt a[182:183],v205 offset:12288
zt a[184:185],v206 offset:8192
s_waitcnt lgkmcnt(5)
zt a[186:187],v206 offset:12288
zt a[188:189],v207 offset:8192
zt a[190:191],v207 offset:12288
zm a[0:3],a[160:163],v[112:115],a[0:3]
ze v0,v0
zm a[16:19],a[164:167],v[112:115],a[16:19]
zk a[128:131],v196 offset:24576
zm a[20:23],a[164:167],v[116:119],a[20:23]
ze v1,v1
zm a[4:7],a[160:163],v[116:119],a[4:7]
zk a[132:135],v197 offset:24576
zc v96,v0,v1
zm a[8:11],a[160:163],v[120:123],a[8:11]
ze v2,v2
zm a[24:27],a[164:167],v[120:123],a[24:27]
zk a[136:139],v198 offset:24576
ze v3,v3
zk a[140:143],v199 offset:24576
zc v97,v2,v3
v_smfmac_f32_16x16x64_bf16 v[80:83], v[192:195], v[112:119], v253
ze v4,v4
v_smfmac_f32_16x16x64_bf16 v[84:87], v[192:195], v[120:127], v253
zk a[144:147],v196 offset:28672
ze v5,v5
zk a[148:151],v197 offset:28672
zc v98,v4,v5
zm a[56:59],a[172:175],v[120:123],a[56:59]
s_waitcnt lgkmcnt(6)
ze v6,v6
)"
R"(zm a[40:43],a[168:171],v[120:123],a[40:43]
zk a[152:155],v198 offset:28672
zm a[36:39],a[168:171],v[116:119],a[36:39]
ze v7,v7
zm a[52:55],a[172:175],v[116:119],a[52:55]
zk a[156:159],v199 offset:28672
zc v99,v6,v7
zm a[48:51],a[172:175],v[112:115],a[48:51]
ze v8,v8
zm a[32:35],a[168:171],v[112:115],a[32:35]
zm a[64:67],a[176:179],v[112:115],a[64:67]
ze v9,v9
zm a[80:83],a[180:183],v[112:115],a[80:83]
zc v100,v8,v9
zm a[84:87],a[180:183],v[116:119],a[84:87]
ze v10,v10
zm a[68:71],a[176:179],v[116:119],a[68:71]
zm a[72:75],a[176:179],v[120:123],a[72:75]
ze v11,v11
zm a[88:91],a[180:183],v[120:123],a[88:91]
zc v101,v10,v11
ze v12,v12
ze v13,v13
s_nop 0
zc v102,v12,v13
zm a[120:123],a[188:191],v[120:123],a[120:123]
ze v14,v14
zm a[104:107],a[184:187],v[120:123],a[104:107]
zm a[100:103],a[184:187],v[116:119],a[100:103]
ze v15,v15
zm a[116:119],a[188:191],v[116:119],a[116:119]
zc v103,v14,v15
zm a[112:115],a[188:191],v[112:115],a[112:115]
s_waitcnt vmcnt(6) lgkmcnt(0)
zm a[96:99],a[184:187],v[112:115],a[96:99]
s_sub_i32 s62, s79, s69
s_sub_i32 s62, s62, 0xa0
s_max_i32 s62, s62, 0
s_add_u32 s69, s69, s62
s_add_u32 s69, s69, s68
s_add_u32 s61, s79, s49
s_sub_u32 s61, s61, s46
s_sub_i32 s62, s69, s61
s_max_i32 s62, s62, 0
s_add_u32 s61, s61, s62
L_pz16%=:
s_memrealtime s[66:67]
s_waitcnt lgkmcnt(0)
s_sub_i32 s62, s61, s66
s_cmp_gt_i32 s62, 0
s_cbranch_scc1 L_pz16%=
s_barrier
s_memrealtime s[66:67]
s_waitcnt lgkmcnt(0)
s_sub_u32 s49, s49, 2
s_max_u32 s49, s49, s68
s_add_u32 s70, s70, 1
s_sub_u32 s62, s66, s79
s_cmp_gt_u32 s62, 0xa2
s_cselect_b32 s70, s70, 0
s_cmp_ge_u32 s70, 2
s_cselect_b32 s49, 0x9c, s49
s_cmp_ge_u32 s70, 4
s_cselect_b32 s46, 0, s46
s_sub_u32 s79, s66, 4
zm v[32:35],a[128:131],a[192:195],v[64:67]
ze v16,v16
zm v[32:35],a[132:135],a[196:199],v[32:35]
zt a[160:161],v200 offset:16384
zm v[32:35],a[136:139],a[200:203],v[32:35]
ze v17,v17
zm v[32:35],a[140:143],a[204:207],v[32:35]
zt a[162:163],v200 offset:20480
zc v104,v16,v17
zm v[36:39],a[156:159],a[204:207],v[64:67]
s_and_b32 m0, s47, 0xffff
ze v18,v18
zm v[36:39],a[152:155],a[200:203],v[36:39]
zt a[164:165],v201 offset:16384
zl v212,s[96:99],s47 offen offset:0 lds
zm v[36:39],a[148:151],a[196:199],v[36:39]
ze v19,v19
zm v[36:39],a[144:147],a[192:195],v[36:39]
zt a[166:167],v201 offset:20480
zc v105,v18,v19
zm v[44:47],a[144:147],a[208:211],v[68:71]
ze v20,v20
zm v[44:47],a[148:151],a[212:215],v[44:47]
zt a[168:169],v202 offset:16384
zm v[44:47],a[152:155],a[216:219],v[44:47]
ze v21,v21
zm v[44:47],a[156:159],a[220:223],v[44:47]
s_add_u32 s47, s47, 0x1000
zt a[170:171],v202 offset:20480
zc v106,v20,v21
zm v[40:43],a[140:143],a[220:223],v[68:71]
s_and_b32 m0, s47, 0xffff
ze v22,v22
zm v[40:43],a[136:139],a[216:219],v[40:43]
zt a[172:173],v203 offset:16384
zl v212,s[96:99],s47 offen offset:0 lds
zm v[40:43],a[132:135],a[212:215],v[40:43]
ze v23,v23
zm v[40:43],a[128:131],a[208:211],v[40:43]
zt a[174:175],v203 offset:20480
zc v107,v22,v23
zm v[48:51],a[128:131],a[224:227],v[72:75]
zm v[48:51],a[132:135],a[228:231],v[48:51]
zt a[176:177],v204 offset:16384
zm v[48:51],a[136:139],a[232:235],v[48:51]
zm v[48:51],a[140:143],a[236:239],v[48:51]
s_add_u32 s47, s47, 0x1000
zt a[178:179],v204 offset:20480
zm v[52:55],a[156:159],a[236:239],v[72:75]
s_and_b32 m0, s47, 0xffff
zm v[52:55],a[152:155],a[232:235],v[52:55]
zt a[180:181],v205 offset:16384
zl v212,s[96:99],s47 offen offset:0 nt lds
zm v[52:55],a[148:151],a[228:231],v[52:55]
zm v[52:55],a[144:147],a[224:227],v[52:55]
zt a[182:183],v205 offset:20480
zt a[184:185],v206 offset:16384
s_waitcnt lgkmcnt(5)
s_add_u32 s47, s47, 0x1000
zt a[186:187],v206 offset:20480
s_and_b32 m0, s47, 0xffff
zt a[188:189],v207 offset:16384
zl v212,s[96:99],s47 offen offset:0 nt lds
s_add_u32 s47, s47, 0x1000
zt a[190:191],v207 offset:20480
zm a[0:3],a[160:163],v[96:99],a[0:3]
ze v32,v32
zm a[16:19],a[164:167],v[96:99],a[16:19]
zk a[128:131],v196 offset:32768
zm a[20:23],a[164:167],v[100:103],a[20:23]
ze v33,v33
zm a[4:7],a[160:163],v[100:103],a[4:7]
zk a[132:135],v197 offset:32768
zc v112,v32,v33
zm a[8:11],a[160:163],v[104:107],a[8:11]
ze v34,v34
zm a[24:27],a[164:167],v[104:107],a[24:27]
zk a[136:139],v198 offset:32768
ze v35,v35
zk a[140:143],v199 offset:32768
zc v113,v34,v35
v_smfmac_f32_16x16x64_bf16 v[80:83], v[192:195], v[96:103], v253
ze v36,v36
v_smfmac_f32_16x16x64_bf16 v[84:87], v[192:195], v[104:111], v253
zk a[144:147],v196 offset:36864
ze v37,v37
zk a[148:151],v197 offset:36864
zc v114,v36,v37
zm a[56:59],a[172:175],v[104:107],a[56:59]
s_waitcnt lgkmcnt(6)
ze v38,v38
zm a[40:43],a[168:171],v[104:107],a[40:43]
zk a[152:155],v198 offset:36864
zm a[36:39],a[168:171],v[100:103],a[36:39]
ze v39,v39
zm a[52:55],a[172:175],v[100:103],a[52:55]
s_and_b32 m0, s48, 0xffff
zk a[156:159],v199 offset:36864
zc v115,v38,v39
zm a[48:51],a[172:175],v[96:99],a[48:51]
s_bitset1_b32 m0, 16
ze v40,v40
zm a[32:35],a[168:171],v[96:99],a[32:35]
zl v212,s[36:39],s48 offen offset:0 nt lds
zm a[64:67],a[176:179],v[96:99],a[64:67]
s_add_u32 s48, s48, 0x1000
ze v41,v41
zm a[80:83],a[180:183],v[96:99],a[80:83]
s_and_b32 m0, s48, 0xffff
zc v116,v40,v41
zm a[84:87],a[180:183],v[100:103],a[84:87]
s_bitset1_b32 m0, 16
ze v42,v42
zm a[68:71],a[176:179],v[100:103],a[68:71]
zl v212,s[36:39],s48 offen offset:0 nt lds
zm a[72:75],a[176:179],v[104:107],a[72:75]
s_add_u32 s48, s48, 0x1000
ze v43,v43
zm a[88:91],a[180:183],v[104:107],a[88:91]
s_and_b32 m0, s48, 0xffff
zc v117,v42,v43
s_bitset1_b32 m0, 16
ze v44,v44
zl v212,s[36:39],s48 offen offset:0 nt lds
s_add_u32 s48, s48, 0x1000
ze v45,v45
s_and_b32 m0, s48, 0xffff
zc v118,v44,v45
zm a[120:123],a[188:191],v[104:107],a[120:123]
s_bitset1_b32 m0, 16
ze v46,v46
zm a[104:107],a[184:187],v[104:107],a[104:107]
zl v212,s[36:39],s48 offen offset:0 nt lds
zm a[100:103],a[184:187],v[100:103],a[100:103]
s_add_u32 s48, s48, 0x1000
ze v47,v47
zm a[116:119],a[188:191],v[100:103],a[116:119]
zc v119,v46,v47
zm a[112:115],a[188:191],v[96:99],a[112:115]
s_waitcnt lgkmcnt(0)
zm a[96:99],a[184:187],v[96:99],a[96:99]
zm v[0:3],a[128:131],a[192:195],v[64:67]
ze v48,v48
zm v[0:3],a[132:135],a[196:199],v[0:3]
zt a[160:161],v200 offset:24576
global_load_dword v254, v255, s[52:53]
zm v[0:3],a[136:139],a[200:203],v[0:3]
ze v49,v49
zm v[0:3],a[140:143],a[204:207],v[0:3]
zt a[162:163],v200 offset:28672
zc v120,v48,v49
zm v[4:7],a[156:159],a[204:207],v[64:67]
ze v50,v50
zm v[4:7],a[152:155],a[200:203],v[4:7]
zt a[164:165],v201 offset:24576
zm v[4:7],a[148:151],a[196:199],v[4:7]
ze v51,v51
zm v[4:7],a[144:147],a[192:195],v[4:7]
zt a[166:167],v201 offset:28672
zc v121,v50,v51
zm v[12:15],a[144:147],a[208:211],v[68:71]
ze v52,v52
zm v[12:15],a[148:151],a[212:215],v[12:15]
zt a[168:169],v202 offset:24576
zm v[12:15],a[152:155],a[216:219],v[12:15]
ze v53,v53
zm v[12:15],a[156:159],a[220:223],v[12:15]
zt a[170:171],v202 offset:28672
zc v122,v52,v53
zm v[8:11],a[140:143],a[220:223],v[68:71]
ze v54,v54
zm v[8:11],a[136:139],a[216:219],v[8:11]
zt a[172:173],v203 offset:24576
zm v[8:11],a[132:135],a[212:215],v[8:11]
ze v55,v55
zm v[8:11],a[128:131],a[208:211],v[8:11]
zt a[174:175],v203 offset:28672
zc v123,v54,v55
zm v[16:19],a[128:131],a[224:227],v[72:75]
zm v[16:19],a[132:135],a[228:231],v[16:19]
zt a[176:177],v204 offset:24576
zm v[16:19],a[136:139],a[232:235],v[16:19]
zm v[16:19],a[140:143],a[236:239],v[16:19]
zt a[178:179],v204 offset:28672
zm v[20:23],a[156:159],a[236:239],v[72:75]
zm v[20:23],a[152:155],a[232:235],v[20:23]
zt a[180:181],v205 offset:24576
zm v[20:23],a[148:151],a[228:231],v[20:23]
zm v[20:23],a[144:147],a[224:227],v[20:23]
zt a[182:183],v205 offset:28672
zt a[184:185],v206 offset:24576
s_waitcnt lgkmcnt(5)
zt a[186:187],v206 offset:28672
zt a[188:189],v207 offset:24576
zt a[190:191],v207 offset:28672
zm a[0:3],a[160:163],v[112:115],a[0:3]
ze v0,v0
zm a[16:19],a[164:167],v[112:115],a[16:19]
zk a[128:131],v196 offset:40960
zm a[20:23],a[164:167],v[116:119],a[20:23]
ze v1,v1
zm a[4:7],a[160:163],v[116:119],a[4:7]
zk a[132:135],v197 offset:40960
zc v96,v0,v1
zm a[8:11],a[160:163],v[120:123],a[8:11]
ze v2,v2
zm a[24:27],a[164:167],v[120:123],a[24:27]
zk a[136:139],v198 offset:40960
ze v3,v3
zk a[140:143],v199 offset:40960
zc v97,v2,v3
v_smfmac_f32_16x16x64_bf16 v[80:83], v[192:195], v[112:119], v253
ze v4,v4
v_smfmac_f32_16x16x64_bf16 v[84:87], v[192:195], v[120:127], v253
zk a[144:147],v196 offset:45056
ze v5,v5
zk a[148:151],v197 offset:45056
zc v98,v4,v5
zm a[56:59],a[172:175],v[120:123],a[56:59]
s_waitcnt lgkmcnt(6)
ze v6,v6
zm a[40:43],a[168:171],v[120:123],a[40:43]
zk a[152:155],v198 offset:45056
zm a[36:39],a[168:171],v[116:119],a[36:39]
ze v7,v7
zm a[52:55],a[172:175],v[116:119],a[52:55]
zk a[156:159],v199 offset:45056
zc v99,v6,v7
zm a[48:51],a[172:175],v[112:115],a[48:51]
ze v8,v8
zm a[32:35],a[168:171],v[112:115],a[32:35]
zm a[64:67],a[176:179],v[112:115],a[64:67]
ze v9,v9
zm a[80:83],a[180:183],v[112:115],a[80:83]
zc v100,v8,v9
zm a[84:87],a[180:183],v[116:119],a[84:87]
ze v10,v10
zm a[68:71],a[176:179],v[116:119],a[68:71]
zm a[72:75],a[176:179],v[120:123],a[72:75]
ze v11,v11
zm a[88:91],a[180:183],v[120:123],a[88:91]
zc v101,v10,v11
ze v12,v12
ze v13,v13
s_nop 0
zc v102,v12,v13
zm a[120:123],a[188:191],v[120:123],a[120:123]
ze v14,v14
zm a[104:107],a[184:187],v[120:123],a[104:107]
zm a[100:103],a[184:187],v[116:119],a[100:103]
ze v15,v15
zm a[116:119],a[188:191],v[116:119],a[116:119]
zc v103,v14,v15
zm a[112:115],a[188:191],v[112:115],a[112:115]
s_waitcnt vmcnt(6) lgkmcnt(0)
zm a[96:99],a[184:187],v[112:115],a[96:99]
s_sub_i32 s62, s79, s69
s_sub_i32 s62, s62, 0xa0
s_max_i32 s62, s62, 0
s_add_u32 s69, s69, s62
s_add_u32 s69, s69, s68
s_add_u32 s61, s79, s49
s_sub_u32 s61, s61, s46
s_sub_i32 s62, s69, s61
s_max_i32 s62, s62, 0
s_add_u32 s61, s61, s62
L_pz17%=:
s_memrealtime s[66:67]
s_waitcnt lgkmcnt(0)
s_sub_i32 s62, s61, s66
s_cmp_gt_i32 s62, 0
s_cbranch_scc1 L_pz17%=
s_barrier
s_memrealtime s[66:67]
s_waitcnt lgkmcnt(0)
s_sub_u32 s49, s49, 2
s_max_u32 s49, s49, s68
s_add_u32 s70, s70, 1
s_sub_u32 s62, s66, s79
s_cmp_gt_u32 s62, 0xa2
s_cselect_b32 s70, s70, 0
s_cmp_ge_u32 s70, 2
s_cselect_b32 s49, 0x9c, s49
s_cmp_ge_u32 s70, 4
s_cselect_b32 s46, 0, s46
s_sub_u32 s79, s66, 4
zm v[32:35],a[128:131],a[192:195],v[64:67]
ze v16,v16
zm v[32:35],a[132:135],a[196:199],v[32:35]
zt a[160:161],v200 offset:32768
zm v[32:35],a[136:139],a[200:203],v[32:35]
ze v17,v17
zm v[32:35],a[140:143],a[204:207],v[32:35]
zt a[162:163],v200 offset:36864
zc v104,v16,v17
zm v[36:39],a[156:159],a[204:207],v[64:67]
s_and_b32 m0, s47, 0xffff
ze v18,v18
zm v[36:39],a[152:155],a[200:203],v[36:39]
zt a[164:165],v201 offset:32768
zl v212,s[96:99],s47 offen offset:0 lds
zm v[36:39],a[148:151],a[196:199],v[36:39]
ze v19,v19
zm v[36:39],a[144:147],a[192:195],v[36:39]
zt a[166:167],v201 offset:36864
zc v105,v18,v19
zm v[44:47],a[144:147],a[208:211],v[68:71]
ze v20,v20
zm v[44:47],a[148:151],a[212:215],v[44:47]
zt a[168:169],v202 offset:32768
zm v[44:47],a[152:155],a[216:219],v[44:47]
ze v21,v21
zm v[44:47],a[156:159],a[220:223],v[44:47]
s_add_u32 s47, s47, 0x1000
zt a[170:171],v202 offset:36864
zc v106,v20,v21
zm v[40:43],a[140:143],a[220:223],v[68:71]
s_and_b32 m0, s47, 0xffff
ze v22,v22
zm v[40:43],a[136:139],a[216:219],v[40:43]
zt a[172:173],v203 offset:32768
zl v212,s[96:99],s47 offen offset:0 lds
zm v[40:43],a[132:135],a[212:215],v[40:43]
ze v23,v23
zm v[40:43],a[128:131],a[208:211],v[40:43]
zt a[174:175],v203 offset:36864
zc v107,v22,v23
zm v[48:51],a[128:131],a[224:227],v[72:75]
zm v[48:51],a[132:135],a[228:231],v[48:51]
zt a[176:177],v204 offset:32768
zm v[48:51],a[136:139],a[232:235],v[48:51]
zm v[48:51],a[140:143],a[236:239],v[48:51]
s_add_u32 s47, s47, 0x1000
zt a[178:179],v204 offset:36864
zm v[52:55],a[156:159],a[236:239],v[72:75]
s_and_b32 m0, s47, 0xffff
zm v[52:55],a[152:155],a[232:235],v[52:55]
zt a[180:181],v205 offset:32768
zl v212,s[96:99],s47 offen offset:0 nt lds
zm v[52:55],a[148:151],a[228:231],v[52:55]
zm v[52:55],a[144:147],a[224:227],v[52:55]
zt a[182:183],v205 offset:36864
zt a[184:185],v206 offset:32768
s_waitcnt lgkmcnt(5)
s_add_u32 s47, s47, 0x1000
zt a[186:187],v206 offset:36864
s_and_b32 m0, s47, 0xffff
zt a[188:189],v207 offset:32768
zl v212,s[96:99],s47 offen offset:0 nt lds
s_add_u32 s47, s47, 0x1000
zt a[190:191],v207 offset:36864
zm a[0:3],a[160:163],v[96:99],a[0:3]
ze v32,v32
zm a[16:19],a[164:167],v[96:99],a[16:19]
zk a[128:131],v196 offset:49152
zm a[20:23],a[164:167],v[100:103],a[20:23]
ze v33,v33
zm a[4:7],a[160:163],v[100:103],a[4:7]
zk a[132:135],v197 offset:49152
zc v112,v32,v33
zm a[8:11],a[160:163],v[104:107],a[8:11]
ze v34,v34
zm a[24:27],a[164:167],v[104:107],a[24:27]
zk a[136:139],v198 offset:49152
ze v35,v35
zk a[140:143],v199 offset:49152
zc v113,v34,v35
v_smfmac_f32_16x16x64_bf16 v[80:83], v[192:195], v[96:103], v253
ze v36,v36
v_smfmac_f32_16x16x64_bf16 v[84:87], v[192:195], v[104:111], v253
zk a[144:147],v196 offset:53248
ze v37,v37
zk a[148:151],v197 offset:53248
zc v114,v36,v37
zm a[56:59],a[172:175],v[104:107],a[56:59]
s_waitcnt lgkmcnt(6)
ze v38,v38
zm a[40:43],a[168:171],v[104:107],a[40:43]
zk a[152:155],v198 offset:53248
zm a[36:39],a[168:171],v[100:103],a[36:39]
ze v39,v39
zm a[52:55],a[172:175],v[100:103],a[52:55]
s_and_b32 m0, s48, 0xffff
zk a[156:159],v199 offset:53248
zc v115,v38,v39
zm a[48:51],a[172:175],v[96:99],a[48:51]
s_bitset1_b32 m0, 16
ze v40,v40
zm a[32:35],a[168:171],v[96:99],a[32:35]
zl v212,s[36:39],s48 offen offset:0 nt lds
zm a[64:67],a[176:179],v[96:99],a[64:67]
s_add_u32 s48, s48, 0x1000
ze v41,v41
zm a[80:83],a[180:183],v[96:99],a[80:83]
s_and_b32 m0, s48, 0xffff
zc v116,v40,v41
zm a[84:87],a[180:183],v[100:103],a[84:87]
s_bitset1_b32 m0, 16
ze v42,v42
zm a[68:71],a[176:179],v[100:103],a[68:71]
zl v212,s[36:39],s48 offen offset:0 nt lds
zm a[72:75],a[176:179],v[104:107],a[72:75]
s_add_u32 s48, s48, 0x1000
ze v43,v43
zm a[88:91],a[180:183],v[104:107],a[88:91]
s_and_b32 m0, s48, 0xffff
zc v117,v42,v43
s_bitset1_b32 m0, 16
ze v44,v44
zl v212,s[36:39],s48 offen offset:0 nt lds
s_add_u32 s48, s48, 0x1000
ze v45,v45
s_and_b32 m0, s48, 0xffff
zc v118,v44,v45
zm a[120:123],a[188:191],v[104:107],a[120:123]
s_bitset1_b32 m0, 16
ze v46,v46
zm a[104:107],a[184:187],v[104:107],a[104:107]
zl v212,s[36:39],s48 offen offset:0 nt lds
zm a[100:103],a[184:187],v[100:103],a[100:103]
s_add_u32 s48, s48, 0x1000
ze v47,v47
zm a[116:119],a[188:191],v[100:103],a[116:119]
zc v119,v46,v47
zm a[112:115],a[188:191],v[96:99],a[112:115]
s_waitcnt lgkmcnt(0)
zm a[96:99],a[184:187],v[96:99],a[96:99]
zm v[0:3],a[128:131],a[192:195],v[64:67]
ze v48,v48
zm v[0:3],a[132:135],a[196:199],v[0:3]
zt a[160:161],v200 offset:40960
zm v[0:3],a[136:139],a[200:203],v[0:3]
ze v49,v49
zm v[0:3],a[140:143],a[204:207],v[0:3]
zt a[162:163],v200 offset:45056
zc v120,v48,v49
zm v[4:7],a[156:159],a[204:207],v[64:67]
ze v50,v50
zm v[4:7],a[152:155],a[200:203],v[4:7]
zt a[164:165],v201 offset:40960
zm v[4:7],a[148:151],a[196:199],v[4:7]
ze v51,v51
zm v[4:7],a[144:147],a[192:195],v[4:7]
zt a[166:167],v201 offset:45056
zc v121,v50,v51
zm v[12:15],a[144:147],a[208:211],v[68:71]
ze v52,v52
zm v[12:15],a[148:151],a[212:215],v[12:15]
zt a[168:169],v202 offset:40960
zm v[12:15],a[152:155],a[216:219],v[12:15]
ze v53,v53
zm v[12:15],a[156:159],a[220:223],v[12:15]
zt a[170:171],v202 offset:45056
zc v122,v52,v53
zm v[8:11],a[140:143],a[220:223],v[68:71]
ze v54,v54
zm v[8:11],a[136:139],a[216:219],v[8:11]
zt a[172:173],v203 offset:40960
zm v[8:11],a[132:135],a[212:215],v[8:11]
ze v55,v55
zm v[8:11],a[128:131],a[208:211],v[8:11]
zt a[174:175],v203 offset:45056
zc v123,v54,v55
zm v[16:19],a[128:131],a[224:227],v[72:75]
zm v[16:19],a[132:135],a[228:231],v[16:19]
zt a[176:177],v204 offset:40960
zm v[16:19],a[136:139],a[232:235],v[16:19]
zm v[16:19],a[140:143],a[236:239],v[16:19]
zt a[178:179],v204 offset:45056
zm v[20:23],a[156:159],a[236:239],v[72:75]
zm v[20:23],a[152:155],a[232:235],v[20:23]
zt a[180:181],v205 offset:40960
zm v[20:23],a[148:151],a[228:231],v[20:23]
zm v[20:23],a[144:147],a[224:227],v[20:23]
zt a[182:183],v205 offset:45056
zt a[184:185],v206 offset:40960
s_waitcnt lgkmcnt(5)
zt a[186:187],v206 offset:45056
zt a[188:189],v207 offset:40960
zt a[190:191],v207 offset:45056
zm a[0:3],a[160:163],v[112:115],a[0:3]
ze v0,v0
zm a[16:19],a[164:167],v[112:115],a[16:19]
zk a[128:131],v196 offset:57344
zm a[20:23],a[164:167],v[116:119],a[20:23]
ze v1,v1
zm a[4:7],a[160:163],v[116:119],a[4:7]
zk a[132:135],v197 offset:57344
zc v96,v0,v1
zm a[8:11],a[160:163],v[120:123],a[8:11]
ze v2,v2
zm a[24:27],a[164:167],v[120:123],a[24:27]
zk a[136:139],v198 offset:57344
ze v3,v3
zk a[140:143],v199 offset:57344
zc v97,v2,v3
v_smfmac_f32_16x16x64_bf16 v[80:83], v[192:195], v[112:119], v253
ze v4,v4
v_smfmac_f32_16x16x64_bf16 v[84:87], v[192:195], v[120:127], v253
zk a[144:147],v196 offset:61440
ze v5,v5
zk a[148:151],v197 offset:61440
zc v98,v4,v5
zm a[56:59],a[172:175],v[120:123],a[56:59]
s_waitcnt lgkmcnt(6)
ze v6,v6
zm a[40:43],a[168:171],v[120:123],a[40:43]
zk a[152:155],v198 offset:61440
zm a[36:39],a[168:171],v[116:119],a[36:39]
ze v7,v7
zm a[52:55],a[172:175],v[116:119],a[52:55]
zk a[156:159],v199 offset:61440
zc v99,v6,v7
zm a[48:51],a[172:175],v[112:115],a[48:51]
ze v8,v8
zm a[32:35],a[168:171],v[112:115],a[32:35]
zm a[64:67],a[176:179],v[112:115],a[64:67]
ze v9,v9
zm a[80:83],a[180:183],v[112:115],a[80:83]
zc v100,v8,v9
zm a[84:87],a[180:183],v[116:119],a[84:87]
ze v10,v10
zm a[68:71],a[176:179],v[116:119],a[68:71]
zm a[72:75],a[176:179],v[120:123],a[72:75]
ze v11,v11
zm a[88:91],a[180:183],v[120:123],a[88:91]
zc v101,v10,v11
ze v12,v12
ze v13,v13
s_nop 0
zc v102,v12,v13
zm a[120:123],a[188:191],v[120:123],a[120:123]
ze v14,v14
zm a[104:107],a[184:187],v[120:123],a[104:107]
zm a[100:103],a[184:187],v[116:119],a[100:103]
ze v15,v15
zm a[116:119],a[188:191],v[116:119],a[116:119]
zc v103,v14,v15
zm a[112:115],a[188:191],v[112:115],a[112:115]
s_waitcnt vmcnt(6) lgkmcnt(0)
zm a[96:99],a[184:187],v[112:115],a[96:99]
s_sub_i32 s62, s79, s69
s_sub_i32 s62, s62, 0xa0
s_max_i32 s62, s62, 0
s_add_u32 s69, s69, s62
s_add_u32 s69, s69, s68
s_add_u32 s61, s79, s49
s_sub_u32 s61, s61, s46
s_sub_i32 s62, s69, s61
s_max_i32 s62, s62, 0
s_add_u32 s61, s61, s62
L_pz18%=:
s_memrealtime s[66:67]
s_waitcnt lgkmcnt(0)
s_sub_i32 s62, s61, s66
s_cmp_gt_i32 s62, 0
s_cbranch_scc1 L_pz18%=
s_barrier
s_memrealtime s[66:67]
s_waitcnt lgkmcnt(0)
s_sub_u32 s49, s49, 2
s_max_u32 s49, s49, s68
s_add_u32 s70, s70, 1
s_sub_u32 s62, s66, s79
s_cmp_gt_u32 s62, 0xa2
s_cselect_b32 s70, s70, 0
s_cmp_ge_u32 s70, 2
s_cselect_b32 s49, 0x9c, s49
s_cmp_ge_u32 s70, 4
s_cselect_b32 s46, 0, s46
s_sub_u32 s79, s66, 4
zm v[32:35],a[128:131],a[192:195],v[64:67]
ze v16,v16
zm v[32:35],a[132:135],a[196:199],v[32:35]
zt a[160:161],v200 offset:49152
zm v[32:35],a[136:139],a[200:203],v[32:35]
ze v17,v17
zm v[32:35],a[140:143],a[204:207],v[32:35]
zt a[162:163],v200 offset:53248
zc v104,v16,v17
zm v[36:39],a[156:159],a[204:207],v[64:67]
s_and_b32 m0, s47, 0xffff
ze v18,v18
zm v[36:39],a[152:155],a[200:203],v[36:39]
zt a[164:165],v201 offset:49152
zl v212,s[96:99],s47 offen offset:0 lds
zm v[36:39],a[148:151],a[196:199],v[36:39]
ze v19,v19
zm v[36:39],a[144:147],a[192:195],v[36:39]
zt a[166:167],v201 offset:53248
zc v105,v18,v19
zm v[44:47],a[144:147],a[208:211],v[68:71]
ze v20,v20
zm v[44:47],a[148:151],a[212:215],v[44:47]
zt a[168:169],v202 offset:49152
zm v[44:47],a[152:155],a[216:219],v[44:47]
ze v21,v21
zm v[44:47],a[156:159],a[220:223],v[44:47]
s_add_u32 s47, s47, 0x1000
zt a[170:171],v202 offset:53248
zc v106,v20,v21
zm v[40:43],a[140:143],a[220:223],v[68:71]
s_and_b32 m0, s47, 0xffff
ze v22,v22
zm v[40:43],a[136:139],a[216:219],v[40:43]
zt a[172:173],v203 offset:49152
zl v212,s[96:99],s47 offen offset:0 lds
zm v[40:43],a[132:135],a[212:215],v[40:43]
ze v23,v23
zm v[40:43],a[128:131],a[208:211],v[40:43]
zt a[174:175],v203 offset:53248
zc v107,v22,v23
zm v[48:51],a[128:131],a[224:227],v[72:75]
zm v[48:51],a[132:135],a[228:231],v[48:51]
zt a[176:177],v204 offset:49152
zm v[48:51],a[136:139],a[232:235],v[48:51]
zm v[48:51],a[140:143],a[236:239],v[48:51]
s_add_u32 s47, s47, 0x1000
zt a[178:179],v204 offset:53248
zm v[52:55],a[156:159],a[236:239],v[72:75]
s_and_b32 m0, s47, 0xffff
zm v[52:55],a[152:155],a[232:235],v[52:55]
zt a[180:181],v205 offset:49152
zl v212,s[96:99],s47 offen offset:0 nt lds
zm v[52:55],a[148:151],a[228:231],v[52:55]
zm v[52:55],a[144:147],a[224:227],v[52:55]
zt a[182:183],v205 offset:53248
zt a[184:185],v206 offset:49152
s_waitcnt lgkmcnt(5)
s_add_u32 s47, s47, 0x1000
zt a[186:187],v206 offset:53248
s_and_b32 m0, s47, 0xffff
zt a[188:189],v207 offset:49152
zl v212,s[96:99],s47 offen offset:0 nt lds
s_add_u32 s47, s47, 0x1000
zt a[190:191],v207 offset:53248
zm a[0:3],a[160:163],v[96:99],a[0:3]
ze v32,v32
zm a[16:19],a[164:167],v[96:99],a[16:19]
zk a[128:131],v196 offset:0
zm a[20:23],a[164:167],v[100:103],a[20:23]
s_add_u32 s51, s51, 1
ze v33,v33
zm a[4:7],a[160:163],v[100:103],a[4:7]
zk a[132:135],v197 offset:0
zc v112,v32,v33
zm a[8:11],a[160:163],v[104:107],a[8:11]
ze v34,v34
zm a[24:27],a[164:167],v[104:107],a[24:27]
zk a[136:139],v198 offset:0
ze v35,v35
zk a[140:143],v199 offset:0
zc v113,v34,v35
v_smfmac_f32_16x16x64_bf16 v[80:83], v[192:195], v[96:103], v253
ze v36,v36
v_smfmac_f32_16x16x64_bf16 v[84:87], v[192:195], v[104:111], v253
zk a[144:147],v196 offset:4096
ze v37,v37
zk a[148:151],v197 offset:4096
zc v114,v36,v37
zm a[56:59],a[172:175],v[104:107],a[56:59]
s_waitcnt lgkmcnt(6)
ze v38,v38
zm a[40:43],a[168:171],v[104:107],a[40:43]
zk a[152:155],v198 offset:4096
zm a[36:39],a[168:171],v[100:103],a[36:39]
ze v39,v39
zm a[52:55],a[172:175],v[100:103],a[52:55]
s_and_b32 m0, s48, 0xffff
zk a[156:159],v199 offset:4096
zc v115,v38,v39
zm a[48:51],a[172:175],v[96:99],a[48:51]
s_bitset1_b32 m0, 16
ze v40,v40
zm a[32:35],a[168:171],v[96:99],a[32:35]
zl v212,s[36:39],s48 offen offset:0 nt lds
zm a[64:67],a[176:179],v[96:99],a[64:67]
s_add_u32 s48, s48, 0x1000
ze v41,v41
zm a[80:83],a[180:183],v[96:99],a[80:83]
s_and_b32 m0, s48, 0xffff
zc v116,v40,v41
zm a[84:87],a[180:183],v[100:103],a[84:87]
s_bitset1_b32 m0, 16
ze v42,v42
zm a[68:71],a[176:179],v[100:103],a[68:71]
zl v212,s[36:39],s48 offen offset:0 nt lds
zm a[72:75],a[176:179],v[104:107],a[72:75]
s_add_u32 s48, s48, 0x1000
ze v43,v43
zm a[88:91],a[180:183],v[104:107],a[88:91]
s_and_b32 m0, s48, 0xffff
zc v117,v42,v43
s_bitset1_b32 m0, 16
ze v44,v44
zl v212,s[36:39],s48 offen offset:0 nt lds
s_add_u32 s48, s48, 0x1000
ze v45,v45
s_and_b32 m0, s48, 0xffff
zc v118,v44,v45
zm a[120:123],a[188:191],v[104:107],a[120:123]
s_bitset1_b32 m0, 16
ze v46,v46
zm a[104:107],a[184:187],v[104:107],a[104:107]
zl v212,s[36:39],s48 offen offset:0 nt lds
zm a[100:103],a[184:187],v[100:103],a[100:103]
s_add_u32 s48, s48, 0x1000
ze v47,v47
zm a[116:119],a[188:191],v[100:103],a[116:119]
zc v119,v46,v47
zm a[112:115],a[188:191],v[96:99],a[112:115]
s_cmp_eq_u32 s51, 4
s_waitcnt lgkmcnt(0)
zm a[96:99],a[184:187],v[96:99],a[96:99]
s_nop 7
s_cbranch_scc1 WL_seam%=
s_nop 7
zm v[0:3],a[128:131],a[192:195],v[64:67]
ze v48,v48
zm v[0:3],a[132:135],a[196:199],v[0:3]
zt a[160:161],v200 offset:57344
zm v[0:3],a[136:139],a[200:203],v[0:3]
ze v49,v49
zm v[0:3],a[140:143],a[204:207],v[0:3]
zt a[162:163],v200 offset:61440
zc v120,v48,v49
zm v[4:7],a[156:159],a[204:207],v[64:67]
ze v50,v50
zm v[4:7],a[152:155],a[200:203],v[4:7]
zt a[164:165],v201 offset:57344
zm v[4:7],a[148:151],a[196:199],v[4:7]
ze v51,v51
zm v[4:7],a[144:147],a[192:195],v[4:7]
zt a[166:167],v201 offset:61440
zc v121,v50,v51
zm v[12:15],a[144:147],a[208:211],v[68:71]
ze v52,v52
zm v[12:15],a[148:151],a[212:215],v[12:15]
zt a[168:169],v202 offset:57344
zm v[12:15],a[152:155],a[216:219],v[12:15]
ze v53,v53
zm v[12:15],a[156:159],a[220:223],v[12:15]
zt a[170:171],v202 offset:61440
zc v122,v52,v53
zm v[8:11],a[140:143],a[220:223],v[68:71]
ze v54,v54
zm v[8:11],a[136:139],a[216:219],v[8:11]
zt a[172:173],v203 offset:57344
zm v[8:11],a[132:135],a[212:215],v[8:11]
ze v55,v55
zm v[8:11],a[128:131],a[208:211],v[8:11]
zt a[174:175],v203 offset:61440
zc v123,v54,v55
zm v[16:19],a[128:131],a[224:227],v[72:75]
zm v[16:19],a[132:135],a[228:231],v[16:19]
zt a[176:177],v204 offset:57344
zm v[16:19],a[136:139],a[232:235],v[16:19]
zm v[16:19],a[140:143],a[236:239],v[16:19]
zt a[178:179],v204 offset:61440
zm v[20:23],a[156:159],a[236:239],v[72:75]
zm v[20:23],a[152:155],a[232:235],v[20:23]
zt a[180:181],v205 offset:57344
zm v[20:23],a[148:151],a[228:231],v[20:23]
zm v[20:23],a[144:147],a[224:227],v[20:23]
zt a[182:183],v205 offset:61440
zt a[184:185],v206 offset:57344
s_waitcnt lgkmcnt(5)
zt a[186:187],v206 offset:61440
zt a[188:189],v207 offset:57344
zt a[190:191],v207 offset:61440
zm a[0:3],a[160:163],v[112:115],a[0:3]
ze v0,v0
zm a[16:19],a[164:167],v[112:115],a[16:19]
zk a[128:131],v196 offset:8192
zm a[20:23],a[164:167],v[116:119],a[20:23]
ze v1,v1
zm a[4:7],a[160:163],v[116:119],a[4:7]
zk a[132:135],v197 offset:8192
zc v96,v0,v1
zm a[8:11],a[160:163],v[120:123],a[8:11]
ze v2,v2
zm a[24:27],a[164:167],v[120:123],a[24:27]
zk a[136:139],v198 offset:8192
ze v3,v3
zk a[140:143],v199 offset:8192
zc v97,v2,v3
v_smfmac_f32_16x16x64_bf16 v[80:83], v[192:195], v[112:119], v253
ze v4,v4
v_smfmac_f32_16x16x64_bf16 v[84:87], v[192:195], v[120:127], v253
zk a[144:147],v196 offset:12288
ze v5,v5
zk a[148:151],v197 offset:12288
zc v98,v4,v5
zm a[56:59],a[172:175],v[120:123],a[56:59]
s_waitcnt lgkmcnt(6)
ze v6,v6
zm a[40:43],a[168:171],v[120:123],a[40:43]
zk a[152:155],v198 offset:12288
zm a[36:39],a[168:171],v[116:119],a[36:39]
ze v7,v7
zm a[52:55],a[172:175],v[116:119],a[52:55]
zk a[156:159],v199 offset:12288
zc v99,v6,v7
zm a[48:51],a[172:175],v[112:115],a[48:51]
ze v8,v8
zm a[32:35],a[168:171],v[112:115],a[32:35]
zm a[64:67],a[176:179],v[112:115],a[64:67]
ze v9,v9
zm a[80:83],a[180:183],v[112:115],a[80:83]
zc v100,v8,v9
zm a[84:87],a[180:183],v[116:119],a[84:87]
ze v10,v10
zm a[68:71],a[176:179],v[116:119],a[68:71]
zm a[72:75],a[176:179],v[120:123],a[72:75]
ze v11,v11
zm a[88:91],a[180:183],v[120:123],a[88:91]
zc v101,v10,v11
ze v12,v12
ze v13,v13
s_nop 0
zc v102,v12,v13
zm a[120:123],a[188:191],v[120:123],a[120:123]
ze v14,v14
zm a[104:107],a[184:187],v[120:123],a[104:107]
zm a[100:103],a[184:187],v[116:119],a[100:103]
ze v15,v15
zm a[116:119],a[188:191],v[116:119],a[116:119]
s_cmp_eq_u32 s51, 7
zc v103,v14,v15
zm a[112:115],a[188:191],v[112:115],a[112:115]
s_waitcnt vmcnt(6) lgkmcnt(0)
zm a[96:99],a[184:187],v[112:115],a[96:99]
s_sub_i32 s62, s79, s69
s_sub_i32 s62, s62, 0xa0
s_max_i32 s62, s62, 0
s_add_u32 s69, s69, s62
s_add_u32 s69, s69, s68
s_add_u32 s61, s79, s49
s_sub_u32 s61, s61, s46
s_sub_i32 s62, s69, s61
s_max_i32 s62, s62, 0
s_add_u32 s61, s61, s62
L_pz19%=:
s_memrealtime s[66:67]
s_waitcnt lgkmcnt(0)
s_sub_i32 s62, s61, s66
s_cmp_gt_i32 s62, 0
s_cbranch_scc1 L_pz19%=
s_barrier
s_memrealtime s[66:67]
s_waitcnt lgkmcnt(0)
s_sub_u32 s49, s49, 2
s_max_u32 s49, s49, s68
s_add_u32 s70, s70, 1
s_sub_u32 s62, s66, s79
s_cmp_gt_u32 s62, 0xa2
s_cselect_b32 s70, s70, 0
s_cmp_ge_u32 s70, 2
s_cselect_b32 s49, 0x9c, s49
s_cmp_ge_u32 s70, 4
s_cselect_b32 s46, 0, s46
s_sub_u32 s79, s66, 4
s_cmp_eq_u32 s51, 7
s_nop 7
s_cbranch_scc1 WL_last%=
s_nop 7
zm v[32:35],a[128:131],a[192:195],v[64:67]
ze v16,v16
zm v[32:35],a[132:135],a[196:199],v[32:35]
zt a[160:161],v200 offset:0
zm v[32:35],a[136:139],a[200:203],v[32:35]
ze v17,v17
zm v[32:35],a[140:143],a[204:207],v[32:35]
zt a[162:163],v200 offset:4096
zc v104,v16,v17
zm v[36:39],a[156:159],a[204:207],v[64:67]
s_and_b32 m0, s47, 0xffff
ze v18,v18
zm v[36:39],a[152:155],a[200:203],v[36:39]
zt a[164:165],v201 offset:0
zl v212,s[96:99],s47 offen offset:0 lds
zm v[36:39],a[148:151],a[196:199],v[36:39]
ze v19,v19
zm v[36:39],a[144:147],a[192:195],v[36:39]
zt a[166:167],v201 offset:4096
zc v105,v18,v19
zm v[44:47],a[144:147],a[208:211],v[68:71]
ze v20,v20
zm v[44:47],a[148:151],a[212:215],v[44:47]
zt a[168:169],v202 offset:0
zm v[44:47],a[152:155],a[216:219],v[44:47]
ze v21,v21
zm v[44:47],a[156:159],a[220:223],v[44:47]
s_add_u32 s47, s47, 0x1000
zt a[170:171],v202 offset:4096
zc v106,v20,v21
zm v[40:43],a[140:143],a[220:223],v[68:71]
s_and_b32 m0, s47, 0xffff
ze v22,v22
zm v[40:43],a[136:139],a[216:219],v[40:43]
zt a[172:173],v203 offset:0
zl v212,s[96:99],s47 offen offset:0 lds
zm v[40:43],a[132:135],a[212:215],v[40:43]
ze v23,v23
zm v[40:43],a[128:131],a[208:211],v[40:43]
zt a[174:175],v203 offset:4096
zc v107,v22,v23
zm v[48:51],a[128:131],a[224:227],v[72:75]
zm v[48:51],a[132:135],a[228:231],v[48:51]
zt a[176:177],v204 offset:0
zm v[48:51],a[136:139],a[232:235],v[48:51]
zm v[48:51],a[140:143],a[236:239],v[48:51]
s_add_u32 s47, s47, 0x1000
zt a[178:179],v204 offset:4096
zm v[52:55],a[156:159],a[236:239],v[72:75]
s_and_b32 m0, s47, 0xffff
zm v[52:55],a[152:155],a[232:235],v[52:55]
zt a[180:181],v205 offset:0
zl v212,s[96:99],s47 offen offset:0 nt lds
zm v[52:55],a[148:151],a[228:231],v[52:55]
zm v[52:55],a[144:147],a[224:227],v[52:55]
zt a[182:183],v205 offset:4096
zt a[184:185],v206 offset:0
s_waitcnt lgkmcnt(5)
s_add_u32 s47, s47, 0x1000
zt a[186:187],v206 offset:4096
s_and_b32 m0, s47, 0xffff
zt a[188:189],v207 offset:0
zl v212,s[96:99],s47 offen offset:0 nt lds
s_add_u32 s47, s47, 0x1000
zt a[190:191],v207 offset:4096
zm a[0:3],a[160:163],v[96:99],a[0:3]
ze v32,v32
zm a[16:19],a[164:167],v[96:99],a[16:19]
zk a[128:131],v196 offset:16384
)"
R"(zm a[20:23],a[164:167],v[100:103],a[20:23]
ze v33,v33
zm a[4:7],a[160:163],v[100:103],a[4:7]
zk a[132:135],v197 offset:16384
zc v112,v32,v33
zm a[8:11],a[160:163],v[104:107],a[8:11]
ze v34,v34
zm a[24:27],a[164:167],v[104:107],a[24:27]
zk a[136:139],v198 offset:16384
ze v35,v35
zk a[140:143],v199 offset:16384
zc v113,v34,v35
v_smfmac_f32_16x16x64_bf16 v[80:83], v[192:195], v[96:103], v253
ze v36,v36
v_smfmac_f32_16x16x64_bf16 v[84:87], v[192:195], v[104:111], v253
zk a[144:147],v196 offset:20480
ze v37,v37
zk a[148:151],v197 offset:20480
zc v114,v36,v37
zm a[56:59],a[172:175],v[104:107],a[56:59]
s_waitcnt lgkmcnt(6)
ze v38,v38
zm a[40:43],a[168:171],v[104:107],a[40:43]
zk a[152:155],v198 offset:20480
zm a[36:39],a[168:171],v[100:103],a[36:39]
ze v39,v39
zm a[52:55],a[172:175],v[100:103],a[52:55]
s_and_b32 m0, s48, 0xffff
zk a[156:159],v199 offset:20480
zc v115,v38,v39
zm a[48:51],a[172:175],v[96:99],a[48:51]
s_bitset1_b32 m0, 16
ze v40,v40
zm a[32:35],a[168:171],v[96:99],a[32:35]
zl v212,s[36:39],s48 offen offset:0 nt lds
zm a[64:67],a[176:179],v[96:99],a[64:67]
s_add_u32 s48, s48, 0x1000
ze v41,v41
zm a[80:83],a[180:183],v[96:99],a[80:83]
s_and_b32 m0, s48, 0xffff
zc v116,v40,v41
zm a[84:87],a[180:183],v[100:103],a[84:87]
s_bitset1_b32 m0, 16
ze v42,v42
zm a[68:71],a[176:179],v[100:103],a[68:71]
zl v212,s[36:39],s48 offen offset:0 nt lds
zm a[72:75],a[176:179],v[104:107],a[72:75]
s_add_u32 s48, s48, 0x1000
ze v43,v43
zm a[88:91],a[180:183],v[104:107],a[88:91]
s_and_b32 m0, s48, 0xffff
zc v117,v42,v43
s_bitset1_b32 m0, 16
ze v44,v44
zl v212,s[36:39],s48 offen offset:0 nt lds
s_add_u32 s48, s48, 0x1000
ze v45,v45
s_and_b32 m0, s48, 0xffff
zc v118,v44,v45
zm a[120:123],a[188:191],v[104:107],a[120:123]
s_bitset1_b32 m0, 16
ze v46,v46
zm a[104:107],a[184:187],v[104:107],a[104:107]
zl v212,s[36:39],s48 offen offset:0 nt lds
zm a[100:103],a[184:187],v[100:103],a[100:103]
s_add_u32 s48, s48, 0x1000
ze v47,v47
zm a[116:119],a[188:191],v[100:103],a[116:119]
zc v119,v46,v47
zm a[112:115],a[188:191],v[96:99],a[112:115]
s_waitcnt lgkmcnt(0)
zm a[96:99],a[184:187],v[96:99],a[96:99]
s_nop 7
s_branch WL_A1%=
WL_seam%=:
zm v[0:3],a[128:131],v[128:131],0
ze v48,v48
zm v[0:3],a[132:135],v[144:147],v[0:3]
zt a[160:161],v200 offset:57344
zw a192,v128
zm v[0:3],a[136:139],v[160:163],v[0:3]
ze v49,v49
zm v[0:3],a[140:143],v[176:179],v[0:3]
zt a[162:163],v200 offset:61440
zc v120,v48,v49
zm v[4:7],a[156:159],v[176:179],0
ze v50,v50
zm v[4:7],a[152:155],v[160:163],v[4:7]
zt a[164:165],v201 offset:57344
zw a193,v129
zm v[4:7],a[148:151],v[144:147],v[4:7]
ze v51,v51
zm v[4:7],a[144:147],v[128:131],v[4:7]
zt a[166:167],v201 offset:61440
zc v121,v50,v51
zm v[12:15],a[144:147],v[132:135],0
ze v52,v52
zm v[12:15],a[148:151],v[148:151],v[12:15]
zt a[168:169],v202 offset:57344
zw a194,v130
zm v[12:15],a[152:155],v[164:167],v[12:15]
ze v53,v53
zm v[12:15],a[156:159],v[180:183],v[12:15]
zt a[170:171],v202 offset:61440
zc v122,v52,v53
zm v[8:11],a[140:143],v[180:183],0
ze v54,v54
zm v[8:11],a[136:139],v[164:167],v[8:11]
zt a[172:173],v203 offset:57344
zw a195,v131
zm v[8:11],a[132:135],v[148:151],v[8:11]
ze v55,v55
zm v[8:11],a[128:131],v[132:135],v[8:11]
zt a[174:175],v203 offset:61440
zc v123,v54,v55
zm v[16:19],a[128:131],v[136:139],0
zm v[16:19],a[132:135],v[152:155],v[16:19]
zt a[176:177],v204 offset:57344
zw a208,v132
zm v[16:19],a[136:139],v[168:171],v[16:19]
zm v[16:19],a[140:143],v[184:187],v[16:19]
zt a[178:179],v204 offset:61440
zm v[20:23],a[156:159],v[184:187],0
zm v[20:23],a[152:155],v[168:171],v[20:23]
zt a[180:181],v205 offset:57344
zw a209,v133
zm v[20:23],a[148:151],v[152:155],v[20:23]
zm v[20:23],a[144:147],v[136:139],v[20:23]
zt a[182:183],v205 offset:61440
zt a[184:185],v206 offset:57344
zw a210,v134
s_waitcnt lgkmcnt(5)
zt a[186:187],v206 offset:61440
zt a[188:189],v207 offset:57344
zw a211,v135
zt a[190:191],v207 offset:61440
zm a[0:3],a[160:163],v[112:115],a[0:3]
zw a224,v136
zw a225,v137
zm a[16:19],a[164:167],v[112:115],a[16:19]
zk a[128:131],v196 offset:8192
zw a226,v138
zm a[20:23],a[164:167],v[116:119],a[20:23]
zw a227,v139
zm a[4:7],a[160:163],v[116:119],a[4:7]
zk a[132:135],v197 offset:8192
zm a[8:11],a[160:163],v[120:123],a[8:11]
zm a[24:27],a[164:167],v[120:123],a[24:27]
zk a[136:139],v198 offset:8192
zw a196,v144
zw a197,v145
zw a198,v146
zk a[140:143],v199 offset:8192
zw a199,v147
v_smfmac_f32_16x16x64_bf16 v[80:83], v[192:195], v[112:119], v253
zw a212,v148
zw a213,v149
v_smfmac_f32_16x16x64_bf16 v[84:87], v[192:195], v[120:127], v253
zk a[144:147],v196 offset:12288
zw a214,v150
zw a215,v151
zw a228,v152
zk a[148:151],v197 offset:12288
zw a229,v153
zm a[56:59],a[172:175],v[120:123],a[56:59]
s_waitcnt lgkmcnt(6)
zw a230,v154
zw a231,v155
zm a[40:43],a[168:171],v[120:123],a[40:43]
zk a[152:155],v198 offset:12288
zm a[36:39],a[168:171],v[116:119],a[36:39]
zm a[52:55],a[172:175],v[116:119],a[52:55]
zk a[156:159],v199 offset:12288
zm a[48:51],a[172:175],v[112:115],a[48:51]
v_add_f32 v228, v80, v81
v_add_f32 v230, v82, v83
zm a[32:35],a[168:171],v[112:115],a[32:35]
v_add_f32 v232, v84, v85
zm a[64:67],a[176:179],v[112:115],a[64:67]
v_add_f32 v234, v86, v87
v_add_f32 v236, v228, v230
zm a[80:83],a[180:183],v[112:115],a[80:83]
v_add_f32 v237, v232, v234
zm a[84:87],a[180:183],v[116:119],a[84:87]
v_add_f32 v236, v236, v237
v_cmp_nge_f32 s[56:57], s55, v236
zm a[68:71],a[176:179],v[116:119],a[68:71]
v_mov_b32 v238, s64
zm a[72:75],a[176:179],v[120:123],a[72:75]
v_lshlrev_b32 v238, 2, v238
v_add_u32 v238, 0x26000, v238
zm a[88:91],a[180:183],v[120:123],a[88:91]
zw a200,v160
zw a201,v161
zw a202,v162
zw a203,v163
zw a216,v164
zw a217,v165
zw a218,v166
zm a[120:123],a[188:191],v[120:123],a[120:123]
s_or_b32 s71, s56, s57
v_mov_b32 v237, s71
zm a[104:107],a[184:187],v[120:123],a[104:107]
ds_write_b32 v238, v237 offset:0
zm a[100:103],a[184:187],v[116:119],a[100:103]
zw a219,v167
zw a232,v168
zm a[116:119],a[188:191],v[116:119],a[116:119]
zw a233,v169
zm a[112:115],a[188:191],v[112:115],a[112:115]
zw a234,v170
zw a235,v171
s_waitcnt vmcnt(6) lgkmcnt(0)
zm a[96:99],a[184:187],v[112:115],a[96:99]
s_sub_i32 s62, s79, s69
s_sub_i32 s62, s62, 0xa0
s_max_i32 s62, s62, 0
s_add_u32 s69, s69, s62
s_add_u32 s69, s69, s68
s_add_u32 s61, s79, s49
s_sub_u32 s61, s61, s46
s_sub_i32 s62, s69, s61
s_max_i32 s62, s62, 0
s_add_u32 s61, s61, s62
L_pz20%=:
s_memrealtime s[66:67]
s_waitcnt lgkmcnt(0)
s_sub_i32 s62, s61, s66
s_cmp_gt_i32 s62, 0
s_cbranch_scc1 L_pz20%=
s_barrier
s_memrealtime s[66:67]
s_waitcnt lgkmcnt(0)
s_sub_u32 s49, s49, 2
s_max_u32 s49, s49, s68
s_add_u32 s70, s70, 1
s_sub_u32 s62, s66, s79
s_cmp_gt_u32 s62, 0xa2
s_cselect_b32 s70, s70, 0
s_cmp_ge_u32 s70, 2
s_cselect_b32 s49, 0x9c, s49
s_cmp_ge_u32 s70, 4
s_cselect_b32 s46, 0, s46
s_sub_u32 s79, s66, 4
.p2align 3
zw a204,v176
zw a205,v177
zw a206,v178
zw a207,v179
zw a220,v180
zw a221,v181
zw a222,v182
zw a223,v183
zw a236,v184
zw a237,v185
zw a238,v186
zw a239,v187
v_mov_b32 v248, 0x26000
zk v[244:247],v248 offset:0
v_max_f32 v228, 0.5, v228
v_max_f32 v230, 0.5, v230
v_max_f32 v232, 0.5, v232
v_max_f32 v234, 0.5, v234
v_rcp_f32 v229, v228
v_rcp_f32 v231, v230
v_rcp_f32 v233, v232
v_rcp_f32 v235, v234
v_rcp_f32 v228, v228
v_rcp_f32 v230, v230
v_rcp_f32 v232, v232
v_rcp_f32 v234, v234
.p2align 3
zr v236,a0
zr v237,a1
zr v238,a2
zr v239,a3
zr v240,a16
zr v241,a17
zr v242,a18
zr v243,a19
s_waitcnt lgkmcnt(0)
v_or3_b32 v244, v244, v245, v246
v_or_b32 v244, v244, v247
zp v[236:237],v[236:237],v[228:229]
zp v[238:239],v[238:239],v[228:229]
zp v[240:241],v[240:241],v[228:229]
zp v[242:243],v[242:243],v[228:229]
v_readfirstlane_b32 s71, v244
zc v128,v236,v237
zc v129,v238,v239
zc v130,v240,v241
zc v131,v242,v243
s_andn2_b32 s71, s71, s76
s_cbranch_scc1 L_fail%=
.p2align 3
zr v236,a4
zr v237,a5
zr v238,a6
zr v239,a7
zr v240,a20
zr v241,a21
zr v242,a22
zr v243,a23
zp v[236:237],v[236:237],v[230:231]
zp v[238:239],v[238:239],v[230:231]
zp v[240:241],v[240:241],v[230:231]
zp v[242:243],v[242:243],v[230:231]
zc v132,v236,v237
zc v133,v238,v239
zc v134,v240,v241
zc v135,v242,v243
.p2align 3
zr v236,a8
zr v237,a9
zr v238,a10
zr v239,a11
zr v240,a24
zr v241,a25
zr v242,a26
zr v243,a27
zp v[236:237],v[236:237],v[232:233]
zp v[238:239],v[238:239],v[232:233]
zp v[240:241],v[240:241],v[232:233]
zp v[242:243],v[242:243],v[232:233]
zc v136,v236,v237
zc v137,v238,v239
zc v138,v240,v241
zc v139,v242,v243
.p2align 3
.p2align 3
zr v236,a32
zr v237,a33
zr v238,a34
zr v239,a35
zr v240,a48
zr v241,a49
zr v242,a50
zr v243,a51
zp v[236:237],v[236:237],v[228:229]
zp v[238:239],v[238:239],v[228:229]
zp v[240:241],v[240:241],v[228:229]
zp v[242:243],v[242:243],v[228:229]
zc v144,v236,v237
zc v145,v238,v239
zc v146,v240,v241
zc v147,v242,v243
.p2align 3
zr v236,a36
zr v237,a37
zr v238,a38
zr v239,a39
zr v240,a52
zr v241,a53
zr v242,a54
zr v243,a55
zp v[236:237],v[236:237],v[230:231]
zp v[238:239],v[238:239],v[230:231]
zp v[240:241],v[240:241],v[230:231]
zp v[242:243],v[242:243],v[230:231]
zc v148,v236,v237
zc v149,v238,v239
zc v150,v240,v241
zc v151,v242,v243
.p2align 3
zr v236,a40
zr v237,a41
zr v238,a42
zr v239,a43
zr v240,a56
zr v241,a57
zr v242,a58
zr v243,a59
zp v[236:237],v[236:237],v[232:233]
zp v[238:239],v[238:239],v[232:233]
zp v[240:241],v[240:241],v[232:233]
zp v[242:243],v[242:243],v[232:233]
zc v152,v236,v237
zc v153,v238,v239
zc v154,v240,v241
zc v155,v242,v243
.p2align 3
.p2align 3
zr v236,a64
zr v237,a65
zr v238,a66
zr v239,a67
zr v240,a80
zr v241,a81
zr v242,a82
zr v243,a83
zp v[236:237],v[236:237],v[228:229]
zp v[238:239],v[238:239],v[228:229]
zp v[240:241],v[240:241],v[228:229]
zp v[242:243],v[242:243],v[228:229]
zc v160,v236,v237
zc v161,v238,v239
zc v162,v240,v241
zc v163,v242,v243
.p2align 3
zr v236,a68
zr v237,a69
zr v238,a70
zr v239,a71
zr v240,a84
zr v241,a85
zr v242,a86
zr v243,a87
zp v[236:237],v[236:237],v[230:231]
zp v[238:239],v[238:239],v[230:231]
zp v[240:241],v[240:241],v[230:231]
zp v[242:243],v[242:243],v[230:231]
zc v164,v236,v237
zc v165,v238,v239
zc v166,v240,v241
zc v167,v242,v243
.p2align 3
zr v236,a72
zr v237,a73
zr v238,a74
zr v239,a75
zr v240,a88
zr v241,a89
zr v242,a90
zr v243,a91
zp v[236:237],v[236:237],v[232:233]
zp v[238:239],v[238:239],v[232:233]
zp v[240:241],v[240:241],v[232:233]
zp v[242:243],v[242:243],v[232:233]
zc v168,v236,v237
zc v169,v238,v239
zc v170,v240,v241
zc v171,v242,v243
.p2align 3
.p2align 3
zr v236,a96
zr v237,a97
zr v238,a98
zr v239,a99
zr v240,a112
zr v241,a113
zr v242,a114
zr v243,a115
zp v[236:237],v[236:237],v[228:229]
zp v[238:239],v[238:239],v[228:229]
zp v[240:241],v[240:241],v[228:229]
zp v[242:243],v[242:243],v[228:229]
zc v176,v236,v237
zc v177,v238,v239
zc v178,v240,v241
zc v179,v242,v243
.p2align 3
zr v236,a100
zr v237,a101
zr v238,a102
zr v239,a103
zr v240,a116
zr v241,a117
zr v242,a118
zr v243,a119
zp v[236:237],v[236:237],v[230:231]
zp v[238:239],v[238:239],v[230:231]
zp v[240:241],v[240:241],v[230:231]
zp v[242:243],v[242:243],v[230:231]
zc v180,v236,v237
zc v181,v238,v239
zc v182,v240,v241
zc v183,v242,v243
.p2align 3
zr v236,a104
zr v237,a105
zr v238,a106
zr v239,a107
zr v240,a120
zr v241,a121
zr v242,a122
zr v243,a123
zp v[236:237],v[236:237],v[232:233]
zp v[238:239],v[238:239],v[232:233]
zp v[240:241],v[240:241],v[232:233]
zp v[242:243],v[242:243],v[232:233]
zc v184,v236,v237
zc v185,v238,v239
zc v186,v240,v241
zc v187,v242,v243
.p2align 3
s_add_u32 s69, s69, 60
s_nop 7
s_branch WL_start%=
WL_last%=:
zm v[32:35],a[128:131],a[192:195],v[64:67]
s_mov_b32 s42, s78
ze v16,v16
zm v[32:35],a[132:135],a[196:199],v[32:35]
s_mov_b32 s51, 8
zt a[160:161],v200 offset:0
zm v[32:35],a[136:139],a[200:203],v[32:35]
ze v17,v17
zm v[32:35],a[140:143],a[204:207],v[32:35]
s_and_b32 m0, s48, 0xffff
zt a[162:163],v200 offset:4096
zc v104,v16,v17
zm v[36:39],a[156:159],a[204:207],v[64:67]
s_bitset1_b32 m0, 16
ze v18,v18
zm v[36:39],a[152:155],a[200:203],v[36:39]
zt a[164:165],v201 offset:0
zl v212,s[36:39],s48 offen offset:0 nt lds
zm v[36:39],a[148:151],a[196:199],v[36:39]
ze v19,v19
zm v[36:39],a[144:147],a[192:195],v[36:39]
zt a[166:167],v201 offset:4096
zc v105,v18,v19
zm v[44:47],a[144:147],a[208:211],v[68:71]
ze v20,v20
zm v[44:47],a[148:151],a[212:215],v[44:47]
zt a[168:169],v202 offset:0
zm v[44:47],a[152:155],a[216:219],v[44:47]
s_add_u32 s48, s48, 0x1000
ze v21,v21
zm v[44:47],a[156:159],a[220:223],v[44:47]
s_and_b32 m0, s48, 0xffff
zt a[170:171],v202 offset:4096
zc v106,v20,v21
zm v[40:43],a[140:143],a[220:223],v[68:71]
s_bitset1_b32 m0, 16
ze v22,v22
zm v[40:43],a[136:139],a[216:219],v[40:43]
zt a[172:173],v203 offset:0
zl v212,s[36:39],s48 offen offset:0 nt lds
zm v[40:43],a[132:135],a[212:215],v[40:43]
ze v23,v23
zm v[40:43],a[128:131],a[208:211],v[40:43]
zt a[174:175],v203 offset:4096
zc v107,v22,v23
zm v[48:51],a[128:131],a[224:227],v[72:75]
zm v[48:51],a[132:135],a[228:231],v[48:51]
zt a[176:177],v204 offset:0
zm v[48:51],a[136:139],a[232:235],v[48:51]
s_add_u32 s48, s48, 0x1000
zm v[48:51],a[140:143],a[236:239],v[48:51]
s_and_b32 m0, s48, 0xffff
zt a[178:179],v204 offset:4096
zm v[52:55],a[156:159],a[236:239],v[72:75]
s_bitset1_b32 m0, 16
zm v[52:55],a[152:155],a[232:235],v[52:55]
zt a[180:181],v205 offset:0
zl v212,s[36:39],s48 offen offset:0 nt lds
zm v[52:55],a[148:151],a[228:231],v[52:55]
zm v[52:55],a[144:147],a[224:227],v[52:55]
zt a[182:183],v205 offset:4096
zt a[184:185],v206 offset:0
s_add_u32 s48, s48, 0x1000
s_waitcnt lgkmcnt(5)
s_and_b32 m0, s48, 0xffff
zt a[186:187],v206 offset:4096
s_bitset1_b32 m0, 16
zt a[188:189],v207 offset:0
zl v212,s[36:39],s48 offen offset:0 nt lds
s_add_u32 s48, s48, 0x1000
zt a[190:191],v207 offset:4096
zm a[0:3],a[160:163],v[96:99],a[0:3]
ze v32,v32
zm a[16:19],a[164:167],v[96:99],a[16:19]
zk a[128:131],v196 offset:16384
zm a[20:23],a[164:167],v[100:103],a[20:23]
ze v33,v33
zm a[4:7],a[160:163],v[100:103],a[4:7]
zk a[132:135],v197 offset:16384
zc v112,v32,v33
zm a[8:11],a[160:163],v[104:107],a[8:11]
ze v34,v34
zm a[24:27],a[164:167],v[104:107],a[24:27]
zk a[136:139],v198 offset:16384
ze v35,v35
zk a[140:143],v199 offset:16384
zc v113,v34,v35
v_smfmac_f32_16x16x64_bf16 v[80:83], v[192:195], v[96:103], v253
ze v36,v36
v_smfmac_f32_16x16x64_bf16 v[84:87], v[192:195], v[104:111], v253
zk a[144:147],v196 offset:20480
ze v37,v37
zk a[148:151],v197 offset:20480
zc v114,v36,v37
zm a[56:59],a[172:175],v[104:107],a[56:59]
s_waitcnt lgkmcnt(6)
ze v38,v38
zm a[40:43],a[168:171],v[104:107],a[40:43]
zk a[152:155],v198 offset:20480
zm a[36:39],a[168:171],v[100:103],a[36:39]
ze v39,v39
zm a[52:55],a[172:175],v[100:103],a[52:55]
zk a[156:159],v199 offset:20480
zc v115,v38,v39
zm a[48:51],a[172:175],v[96:99],a[48:51]
ze v40,v40
zm a[32:35],a[168:171],v[96:99],a[32:35]
zm a[64:67],a[176:179],v[96:99],a[64:67]
ze v41,v41
zm a[80:83],a[180:183],v[96:99],a[80:83]
zc v116,v40,v41
zm a[84:87],a[180:183],v[100:103],a[84:87]
ze v42,v42
zm a[68:71],a[176:179],v[100:103],a[68:71]
zm a[72:75],a[176:179],v[104:107],a[72:75]
ze v43,v43
zm a[88:91],a[180:183],v[104:107],a[88:91]
zc v117,v42,v43
ze v44,v44
ze v45,v45
s_nop 0
zc v118,v44,v45
zm a[120:123],a[188:191],v[104:107],a[120:123]
ze v46,v46
zm a[104:107],a[184:187],v[104:107],a[104:107]
zm a[100:103],a[184:187],v[100:103],a[100:103]
ze v47,v47
zm a[116:119],a[188:191],v[100:103],a[116:119]
zc v119,v46,v47
zm a[112:115],a[188:191],v[96:99],a[112:115]
s_waitcnt lgkmcnt(0)
zm a[96:99],a[184:187],v[96:99],a[96:99]
zm v[0:3],a[128:131],a[192:195],v[64:67]
ze v48,v48
zm v[0:3],a[132:135],a[196:199],v[0:3]
zt a[160:161],v200 offset:8192
zm v[0:3],a[136:139],a[200:203],v[0:3]
ze v49,v49
zm v[0:3],a[140:143],a[204:207],v[0:3]
zt a[162:163],v200 offset:12288
zc v120,v48,v49
zm v[4:7],a[156:159],a[204:207],v[64:67]
ze v50,v50
zm v[4:7],a[152:155],a[200:203],v[4:7]
zt a[164:165],v201 offset:8192
zm v[4:7],a[148:151],a[196:199],v[4:7]
ze v51,v51
zm v[4:7],a[144:147],a[192:195],v[4:7]
zt a[166:167],v201 offset:12288
zc v121,v50,v51
zm v[12:15],a[144:147],a[208:211],v[68:71]
ze v52,v52
zm v[12:15],a[148:151],a[212:215],v[12:15]
zt a[168:169],v202 offset:8192
zm v[12:15],a[152:155],a[216:219],v[12:15]
ze v53,v53
zm v[12:15],a[156:159],a[220:223],v[12:15]
zt a[170:171],v202 offset:12288
zc v122,v52,v53
zm v[8:11],a[140:143],a[220:223],v[68:71]
ze v54,v54
zm v[8:11],a[136:139],a[216:219],v[8:11]
zt a[172:173],v203 offset:8192
zm v[8:11],a[132:135],a[212:215],v[8:11]
ze v55,v55
zm v[8:11],a[128:131],a[208:211],v[8:11]
zt a[174:175],v203 offset:12288
zc v123,v54,v55
zm v[16:19],a[128:131],a[224:227],v[72:75]
zm v[16:19],a[132:135],a[228:231],v[16:19]
zt a[176:177],v204 offset:8192
zm v[16:19],a[136:139],a[232:235],v[16:19]
zm v[16:19],a[140:143],a[236:239],v[16:19]
zt a[178:179],v204 offset:12288
zm v[20:23],a[156:159],a[236:239],v[72:75]
zm v[20:23],a[152:155],a[232:235],v[20:23]
zt a[180:181],v205 offset:8192
zm v[20:23],a[148:151],a[228:231],v[20:23]
zm v[20:23],a[144:147],a[224:227],v[20:23]
zt a[182:183],v205 offset:12288
zt a[184:185],v206 offset:8192
s_waitcnt lgkmcnt(5)
zt a[186:187],v206 offset:12288
zt a[188:189],v207 offset:8192
zt a[190:191],v207 offset:12288
zm a[0:3],a[160:163],v[112:115],a[0:3]
ze v0,v0
zm a[16:19],a[164:167],v[112:115],a[16:19]
zk a[128:131],v196 offset:24576
zm a[20:23],a[164:167],v[116:119],a[20:23]
ze v1,v1
zm a[4:7],a[160:163],v[116:119],a[4:7]
zk a[132:135],v197 offset:24576
zc v96,v0,v1
zm a[8:11],a[160:163],v[120:123],a[8:11]
ze v2,v2
zm a[24:27],a[164:167],v[120:123],a[24:27]
zk a[136:139],v198 offset:24576
ze v3,v3
zk a[140:143],v199 offset:24576
zc v97,v2,v3
v_smfmac_f32_16x16x64_bf16 v[80:83], v[192:195], v[112:119], v253
ze v4,v4
v_smfmac_f32_16x16x64_bf16 v[84:87], v[192:195], v[120:127], v253
zk a[144:147],v196 offset:28672
ze v5,v5
zk a[148:151],v197 offset:28672
zc v98,v4,v5
zm a[56:59],a[172:175],v[120:123],a[56:59]
s_waitcnt lgkmcnt(6)
ze v6,v6
zm a[40:43],a[168:171],v[120:123],a[40:43]
zk a[152:155],v198 offset:28672
zm a[36:39],a[168:171],v[116:119],a[36:39]
ze v7,v7
zm a[52:55],a[172:175],v[116:119],a[52:55]
zk a[156:159],v199 offset:28672
zc v99,v6,v7
zm a[48:51],a[172:175],v[112:115],a[48:51]
ze v8,v8
zm a[32:35],a[168:171],v[112:115],a[32:35]
zm a[64:67],a[176:179],v[112:115],a[64:67]
ze v9,v9
zm a[80:83],a[180:183],v[112:115],a[80:83]
zc v100,v8,v9
zm a[84:87],a[180:183],v[116:119],a[84:87]
ze v10,v10
zm a[68:71],a[176:179],v[116:119],a[68:71]
zm a[72:75],a[176:179],v[120:123],a[72:75]
ze v11,v11
zm a[88:91],a[180:183],v[120:123],a[88:91]
zc v101,v10,v11
ze v12,v12
ze v13,v13
s_nop 0
zc v102,v12,v13
zm a[120:123],a[188:191],v[120:123],a[120:123]
ze v14,v14
zm a[104:107],a[184:187],v[120:123],a[104:107]
zm a[100:103],a[184:187],v[116:119],a[100:103]
ze v15,v15
zm a[116:119],a[188:191],v[116:119],a[116:119]
zc v103,v14,v15
zm a[112:115],a[188:191],v[112:115],a[112:115]
s_waitcnt vmcnt(12) lgkmcnt(0)
zm a[96:99],a[184:187],v[112:115],a[96:99]
s_sub_i32 s62, s79, s69
s_sub_i32 s62, s62, 0xa0
s_max_i32 s62, s62, 0
s_add_u32 s69, s69, s62
s_add_u32 s69, s69, s68
s_add_u32 s61, s79, s49
s_sub_u32 s61, s61, s46
s_sub_i32 s62, s69, s61
s_max_i32 s62, s62, 0
s_add_u32 s61, s61, s62
L_pz21%=:
s_memrealtime s[66:67]
s_waitcnt lgkmcnt(0)
s_sub_i32 s62, s61, s66
s_cmp_gt_i32 s62, 0
s_cbranch_scc1 L_pz21%=
s_barrier
s_memrealtime s[66:67]
s_waitcnt lgkmcnt(0)
s_sub_u32 s49, s49, 2
s_max_u32 s49, s49, s68
s_add_u32 s70, s70, 1
s_sub_u32 s62, s66, s79
s_cmp_gt_u32 s62, 0xa2
s_cselect_b32 s70, s70, 0
s_cmp_ge_u32 s70, 2
s_cselect_b32 s49, 0x9c, s49
s_cmp_ge_u32 s70, 4
s_cselect_b32 s46, 0, s46
s_sub_u32 s79, s66, 4
zm v[32:35],a[128:131],a[192:195],v[64:67]
ze v16,v16
zm v[32:35],a[132:135],a[196:199],v[32:35]
zt a[160:161],v200 offset:16384
zm v[32:35],a[136:139],a[200:203],v[32:35]
ze v17,v17
zm v[32:35],a[140:143],a[204:207],v[32:35]
zt a[162:163],v200 offset:20480
zc v104,v16,v17
zm v[36:39],a[156:159],a[204:207],v[64:67]
ze v18,v18
zm v[36:39],a[152:155],a[200:203],v[36:39]
zt a[164:165],v201 offset:16384
zm v[36:39],a[148:151],a[196:199],v[36:39]
ze v19,v19
zm v[36:39],a[144:147],a[192:195],v[36:39]
zt a[166:167],v201 offset:20480
zc v105,v18,v19
zm v[44:47],a[144:147],a[208:211],v[68:71]
s_add_u32 s60, s88, 0
ze v20,v20
zm v[44:47],a[148:151],a[212:215],v[44:47]
zt a[168:169],v202 offset:16384
buffer_store_dwordx4 v[128:131], v214, s[40:43], s60 offen offset:0 sc1
zm v[44:47],a[152:155],a[216:219],v[44:47]
ze v21,v21
zm v[44:47],a[156:159],a[220:223],v[44:47]
zt a[170:171],v202 offset:20480
zc v106,v20,v21
zm v[40:43],a[140:143],a[220:223],v[68:71]
ze v22,v22
zm v[40:43],a[136:139],a[216:219],v[40:43]
zt a[172:173],v203 offset:16384
zm v[40:43],a[132:135],a[212:215],v[40:43]
ze v23,v23
zm v[40:43],a[128:131],a[208:211],v[40:43]
zt a[174:175],v203 offset:20480
zc v107,v22,v23
zm v[48:51],a[128:131],a[224:227],v[72:75]
zm v[48:51],a[132:135],a[228:231],v[48:51]
zt a[176:177],v204 offset:16384
zm v[48:51],a[136:139],a[232:235],v[48:51]
zm v[48:51],a[140:143],a[236:239],v[48:51]
zt a[178:179],v204 offset:20480
zm v[52:55],a[156:159],a[236:239],v[72:75]
zm v[52:55],a[152:155],a[232:235],v[52:55]
zt a[180:181],v205 offset:16384
zm v[52:55],a[148:151],a[228:231],v[52:55]
zm v[52:55],a[144:147],a[224:227],v[52:55]
zt a[182:183],v205 offset:20480
zt a[184:185],v206 offset:16384
buffer_store_dwordx4 v[132:135], v214, s[40:43], s60 offen offset:1024 sc1
s_waitcnt lgkmcnt(5)
zt a[186:187],v206 offset:20480
zt a[188:189],v207 offset:16384
zt a[190:191],v207 offset:20480
zm a[0:3],a[160:163],v[96:99],a[0:3]
ze v32,v32
zm a[16:19],a[164:167],v[96:99],a[16:19]
zk a[128:131],v196 offset:32768
zm a[20:23],a[164:167],v[100:103],a[20:23]
ze v33,v33
zm a[4:7],a[160:163],v[100:103],a[4:7]
zk a[132:135],v197 offset:32768
zc v112,v32,v33
zm a[8:11],a[160:163],v[104:107],a[8:11]
ze v34,v34
zm a[24:27],a[164:167],v[104:107],a[24:27]
zk a[136:139],v198 offset:32768
ze v35,v35
zk a[140:143],v199 offset:32768
zc v113,v34,v35
v_smfmac_f32_16x16x64_bf16 v[80:83], v[192:195], v[96:103], v253
ze v36,v36
v_smfmac_f32_16x16x64_bf16 v[84:87], v[192:195], v[104:111], v253
zk a[144:147],v196 offset:36864
ze v37,v37
zk a[148:151],v197 offset:36864
zc v114,v36,v37
zm a[56:59],a[172:175],v[104:107],a[56:59]
s_waitcnt lgkmcnt(6)
ze v38,v38
zm a[40:43],a[168:171],v[104:107],a[40:43]
zk a[152:155],v198 offset:36864
zm a[36:39],a[168:171],v[100:103],a[36:39]
ze v39,v39
zm a[52:55],a[172:175],v[100:103],a[52:55]
zk a[156:159],v199 offset:36864
zc v115,v38,v39
zm a[48:51],a[172:175],v[96:99],a[48:51]
ze v40,v40
zm a[32:35],a[168:171],v[96:99],a[32:35]
zm a[64:67],a[176:179],v[96:99],a[64:67]
ze v41,v41
zm a[80:83],a[180:183],v[96:99],a[80:83]
zc v116,v40,v41
zm a[84:87],a[180:183],v[100:103],a[84:87]
ze v42,v42
zm a[68:71],a[176:179],v[100:103],a[68:71]
zm a[72:75],a[176:179],v[104:107],a[72:75]
ze v43,v43
zm a[88:91],a[180:183],v[104:107],a[88:91]
zc v117,v42,v43
ze v44,v44
ze v45,v45
s_nop 0
zc v118,v44,v45
zm a[120:123],a[188:191],v[104:107],a[120:123]
ze v46,v46
zm a[104:107],a[184:187],v[104:107],a[104:107]
zm a[100:103],a[184:187],v[100:103],a[100:103]
ze v47,v47
zm a[116:119],a[188:191],v[100:103],a[116:119]
zc v119,v46,v47
zm a[112:115],a[188:191],v[96:99],a[112:115]
s_waitcnt lgkmcnt(0)
zm a[96:99],a[184:187],v[96:99],a[96:99]
buffer_store_dwordx4 v[136:139], v214, s[40:43], s60 offen offset:2048 sc1
zm v[0:3],a[128:131],a[192:195],v[64:67]
ze v48,v48
zm v[0:3],a[132:135],a[196:199],v[0:3]
zt a[160:161],v200 offset:24576
zm v[0:3],a[136:139],a[200:203],v[0:3]
ze v49,v49
zm v[0:3],a[140:143],a[204:207],v[0:3]
zt a[162:163],v200 offset:28672
zc v120,v48,v49
zm v[4:7],a[156:159],a[204:207],v[64:67]
ze v50,v50
zm v[4:7],a[152:155],a[200:203],v[4:7]
zt a[164:165],v201 offset:24576
zm v[4:7],a[148:151],a[196:199],v[4:7]
ze v51,v51
zm v[4:7],a[144:147],a[192:195],v[4:7]
zt a[166:167],v201 offset:28672
zc v121,v50,v51
zm v[12:15],a[144:147],a[208:211],v[68:71]
ze v52,v52
zm v[12:15],a[148:151],a[212:215],v[12:15]
zt a[168:169],v202 offset:24576
zm v[12:15],a[152:155],a[216:219],v[12:15]
ze v53,v53
zm v[12:15],a[156:159],a[220:223],v[12:15]
zt a[170:171],v202 offset:28672
zc v122,v52,v53
zm v[8:11],a[140:143],a[220:223],v[68:71]
ze v54,v54
zm v[8:11],a[136:139],a[216:219],v[8:11]
zt a[172:173],v203 offset:24576
zm v[8:11],a[132:135],a[212:215],v[8:11]
ze v55,v55
zm v[8:11],a[128:131],a[208:211],v[8:11]
zt a[174:175],v203 offset:28672
zc v123,v54,v55
zm v[16:19],a[128:131],a[224:227],v[72:75]
s_add_u32 s60, s88, 0x1000
zm v[16:19],a[132:135],a[228:231],v[16:19]
zt a[176:177],v204 offset:24576
buffer_store_dwordx4 v[144:147], v214, s[40:43], s60 offen offset:0 sc1
zm v[16:19],a[136:139],a[232:235],v[16:19]
zm v[16:19],a[140:143],a[236:239],v[16:19]
zt a[178:179],v204 offset:28672
zm v[20:23],a[156:159],a[236:239],v[72:75]
zm v[20:23],a[152:155],a[232:235],v[20:23]
zt a[180:181],v205 offset:24576
zm v[20:23],a[148:151],a[228:231],v[20:23]
zm v[20:23],a[144:147],a[224:227],v[20:23]
zt a[182:183],v205 offset:28672
zt a[184:185],v206 offset:24576
buffer_store_dwordx4 v[148:151], v214, s[40:43], s60 offen offset:1024 sc1
s_waitcnt lgkmcnt(5)
zt a[186:187],v206 offset:28672
zt a[188:189],v207 offset:24576
zt a[190:191],v207 offset:28672
zm a[0:3],a[160:163],v[112:115],a[0:3]
ze v0,v0
zm a[16:19],a[164:167],v[112:115],a[16:19]
zk a[128:131],v196 offset:40960
zm a[20:23],a[164:167],v[116:119],a[20:23]
ze v1,v1
zm a[4:7],a[160:163],v[116:119],a[4:7]
zk a[132:135],v197 offset:40960
zc v96,v0,v1
zm a[8:11],a[160:163],v[120:123],a[8:11]
ze v2,v2
zm a[24:27],a[164:167],v[120:123],a[24:27]
zk a[136:139],v198 offset:40960
ze v3,v3
zk a[140:143],v199 offset:40960
zc v97,v2,v3
v_smfmac_f32_16x16x64_bf16 v[80:83], v[192:195], v[112:119], v253
ze v4,v4
v_smfmac_f32_16x16x64_bf16 v[84:87], v[192:195], v[120:127], v253
zk a[144:147],v196 offset:45056
ze v5,v5
zk a[148:151],v197 offset:45056
zc v98,v4,v5
zm a[56:59],a[172:175],v[120:123],a[56:59]
s_waitcnt lgkmcnt(6)
ze v6,v6
zm a[40:43],a[168:171],v[120:123],a[40:43]
zk a[152:155],v198 offset:45056
zm a[36:39],a[168:171],v[116:119],a[36:39]
ze v7,v7
zm a[52:55],a[172:175],v[116:119],a[52:55]
zk a[156:159],v199 offset:45056
zc v99,v6,v7
zm a[48:51],a[172:175],v[112:115],a[48:51]
ze v8,v8
zm a[32:35],a[168:171],v[112:115],a[32:35]
buffer_store_dwordx4 v[152:155], v214, s[40:43], s60 offen offset:2048 sc1
zm a[64:67],a[176:179],v[112:115],a[64:67]
ze v9,v9
zm a[80:83],a[180:183],v[112:115],a[80:83]
zc v100,v8,v9
zm a[84:87],a[180:183],v[116:119],a[84:87]
ze v10,v10
zm a[68:71],a[176:179],v[116:119],a[68:71]
zm a[72:75],a[176:179],v[120:123],a[72:75]
ze v11,v11
zm a[88:91],a[180:183],v[120:123],a[88:91]
zc v101,v10,v11
ze v12,v12
ze v13,v13
s_nop 0
zc v102,v12,v13
zm a[120:123],a[188:191],v[120:123],a[120:123]
ze v14,v14
zm a[104:107],a[184:187],v[120:123],a[104:107]
zm a[100:103],a[184:187],v[116:119],a[100:103]
ze v15,v15
zm a[116:119],a[188:191],v[116:119],a[116:119]
zc v103,v14,v15
zm a[112:115],a[188:191],v[112:115],a[112:115]
s_waitcnt vmcnt(10) lgkmcnt(0)
)"
R"(zm a[96:99],a[184:187],v[112:115],a[96:99]
s_barrier
zm v[32:35],a[128:131],a[192:195],v[64:67]
ze v16,v16
zm v[32:35],a[132:135],a[196:199],v[32:35]
zt a[160:161],v200 offset:32768
zm v[32:35],a[136:139],a[200:203],v[32:35]
ze v17,v17
zm v[32:35],a[140:143],a[204:207],v[32:35]
s_add_u32 s50, s84, 0
zt a[162:163],v200 offset:36864
zc v104,v16,v17
zm v[36:39],a[156:159],a[204:207],v[64:67]
s_add_u32 m0, s44, 0
ze v18,v18
zm v[36:39],a[152:155],a[200:203],v[36:39]
zt a[164:165],v201 offset:32768
zl v208,s[24:27],s50 offen offset:0 lds
zm v[36:39],a[148:151],a[196:199],v[36:39]
ze v19,v19
zm v[36:39],a[144:147],a[192:195],v[36:39]
zt a[166:167],v201 offset:36864
zc v105,v18,v19
zm v[44:47],a[144:147],a[208:211],v[68:71]
s_add_u32 s60, s88, 0x2000
ze v20,v20
zm v[44:47],a[148:151],a[212:215],v[44:47]
zt a[168:169],v202 offset:32768
buffer_store_dwordx4 v[160:163], v214, s[40:43], s60 offen offset:0 sc1
zm v[44:47],a[152:155],a[216:219],v[44:47]
ze v21,v21
zm v[44:47],a[156:159],a[220:223],v[44:47]
zt a[170:171],v202 offset:36864
zc v106,v20,v21
zm v[40:43],a[140:143],a[220:223],v[68:71]
ze v22,v22
zm v[40:43],a[136:139],a[216:219],v[40:43]
zt a[172:173],v203 offset:32768
zl v209,s[24:27],s50 offen offset:1024 lds
zm v[40:43],a[132:135],a[212:215],v[40:43]
ze v23,v23
zm v[40:43],a[128:131],a[208:211],v[40:43]
zt a[174:175],v203 offset:36864
zc v107,v22,v23
zm v[48:51],a[128:131],a[224:227],v[72:75]
zm v[48:51],a[132:135],a[228:231],v[48:51]
zt a[176:177],v204 offset:32768
zm v[48:51],a[136:139],a[232:235],v[48:51]
zm v[48:51],a[140:143],a[236:239],v[48:51]
zt a[178:179],v204 offset:36864
zm v[52:55],a[156:159],a[236:239],v[72:75]
zm v[52:55],a[152:155],a[232:235],v[52:55]
zt a[180:181],v205 offset:32768
zl v210,s[24:27],s50 offen offset:2048 lds
zm v[52:55],a[148:151],a[228:231],v[52:55]
zm v[52:55],a[144:147],a[224:227],v[52:55]
zt a[182:183],v205 offset:36864
zt a[184:185],v206 offset:32768
buffer_store_dwordx4 v[164:167], v214, s[40:43], s60 offen offset:1024 sc1
s_waitcnt lgkmcnt(5)
zt a[186:187],v206 offset:36864
zt a[188:189],v207 offset:32768
zl v211,s[24:27],s50 offen offset:3072 lds
zt a[190:191],v207 offset:36864
zm a[0:3],a[160:163],v[96:99],a[0:3]
ze v32,v32
zm a[16:19],a[164:167],v[96:99],a[16:19]
zk a[128:131],v196 offset:49152
zm a[20:23],a[164:167],v[100:103],a[20:23]
ze v33,v33
zm a[4:7],a[160:163],v[100:103],a[4:7]
zk a[132:135],v197 offset:49152
zc v112,v32,v33
zm a[8:11],a[160:163],v[104:107],a[8:11]
ze v34,v34
zm a[24:27],a[164:167],v[104:107],a[24:27]
zk a[136:139],v198 offset:49152
ze v35,v35
zk a[140:143],v199 offset:49152
zc v113,v34,v35
v_smfmac_f32_16x16x64_bf16 v[80:83], v[192:195], v[96:103], v253
ze v36,v36
v_smfmac_f32_16x16x64_bf16 v[84:87], v[192:195], v[104:111], v253
zk a[144:147],v196 offset:53248
ze v37,v37
zk a[148:151],v197 offset:53248
zc v114,v36,v37
zm a[56:59],a[172:175],v[104:107],a[56:59]
s_waitcnt lgkmcnt(6)
ze v38,v38
zm a[40:43],a[168:171],v[104:107],a[40:43]
zk a[152:155],v198 offset:53248
zm a[36:39],a[168:171],v[100:103],a[36:39]
ze v39,v39
zm a[52:55],a[172:175],v[100:103],a[52:55]
zk a[156:159],v199 offset:53248
zc v115,v38,v39
zm a[48:51],a[172:175],v[96:99],a[48:51]
ze v40,v40
zm a[32:35],a[168:171],v[96:99],a[32:35]
zm a[64:67],a[176:179],v[96:99],a[64:67]
ze v41,v41
zm a[80:83],a[180:183],v[96:99],a[80:83]
zc v116,v40,v41
zm a[84:87],a[180:183],v[100:103],a[84:87]
ze v42,v42
zm a[68:71],a[176:179],v[100:103],a[68:71]
zm a[72:75],a[176:179],v[104:107],a[72:75]
ze v43,v43
zm a[88:91],a[180:183],v[104:107],a[88:91]
zc v117,v42,v43
ze v44,v44
ze v45,v45
s_nop 0
zc v118,v44,v45
zm a[120:123],a[188:191],v[104:107],a[120:123]
ze v46,v46
zm a[104:107],a[184:187],v[104:107],a[104:107]
zm a[100:103],a[184:187],v[100:103],a[100:103]
ze v47,v47
zm a[116:119],a[188:191],v[100:103],a[116:119]
zc v119,v46,v47
zm a[112:115],a[188:191],v[96:99],a[112:115]
s_waitcnt lgkmcnt(0)
zm a[96:99],a[184:187],v[96:99],a[96:99]
buffer_store_dwordx4 v[168:171], v214, s[40:43], s60 offen offset:2048 sc1
zm v[0:3],a[128:131],a[192:195],v[64:67]
ze v48,v48
zm v[0:3],a[132:135],a[196:199],v[0:3]
zt a[160:161],v200 offset:40960
zm v[0:3],a[136:139],a[200:203],v[0:3]
ze v49,v49
zm v[0:3],a[140:143],a[204:207],v[0:3]
s_add_u32 s50, s84, 0x10000
zt a[162:163],v200 offset:45056
zc v120,v48,v49
zm v[4:7],a[156:159],a[204:207],v[64:67]
s_add_u32 m0, s44, 0x4000
ze v50,v50
zm v[4:7],a[152:155],a[200:203],v[4:7]
zt a[164:165],v201 offset:40960
zl v208,s[24:27],s50 offen offset:0 lds
zm v[4:7],a[148:151],a[196:199],v[4:7]
ze v51,v51
zm v[4:7],a[144:147],a[192:195],v[4:7]
zt a[166:167],v201 offset:45056
zc v121,v50,v51
zm v[12:15],a[144:147],a[208:211],v[68:71]
ze v52,v52
zm v[12:15],a[148:151],a[212:215],v[12:15]
zt a[168:169],v202 offset:40960
zm v[12:15],a[152:155],a[216:219],v[12:15]
ze v53,v53
zm v[12:15],a[156:159],a[220:223],v[12:15]
zt a[170:171],v202 offset:45056
zc v122,v52,v53
zm v[8:11],a[140:143],a[220:223],v[68:71]
ze v54,v54
zm v[8:11],a[136:139],a[216:219],v[8:11]
zt a[172:173],v203 offset:40960
zl v209,s[24:27],s50 offen offset:1024 lds
zm v[8:11],a[132:135],a[212:215],v[8:11]
ze v55,v55
zm v[8:11],a[128:131],a[208:211],v[8:11]
zt a[174:175],v203 offset:45056
zc v123,v54,v55
zm v[16:19],a[128:131],a[224:227],v[72:75]
s_add_u32 s60, s88, 0x3000
zm v[16:19],a[132:135],a[228:231],v[16:19]
zt a[176:177],v204 offset:40960
buffer_store_dwordx4 v[176:179], v214, s[40:43], s60 offen offset:0 sc1
zm v[16:19],a[136:139],a[232:235],v[16:19]
zm v[16:19],a[140:143],a[236:239],v[16:19]
zt a[178:179],v204 offset:45056
zm v[20:23],a[156:159],a[236:239],v[72:75]
zm v[20:23],a[152:155],a[232:235],v[20:23]
zt a[180:181],v205 offset:40960
zl v210,s[24:27],s50 offen offset:2048 lds
zm v[20:23],a[148:151],a[228:231],v[20:23]
zm v[20:23],a[144:147],a[224:227],v[20:23]
zt a[182:183],v205 offset:45056
zt a[184:185],v206 offset:40960
buffer_store_dwordx4 v[180:183], v214, s[40:43], s60 offen offset:1024 sc1
s_waitcnt lgkmcnt(5)
zt a[186:187],v206 offset:45056
zt a[188:189],v207 offset:40960
zl v211,s[24:27],s50 offen offset:3072 lds
zt a[190:191],v207 offset:45056
zm a[0:3],a[160:163],v[112:115],a[0:3]
ze v0,v0
zm a[16:19],a[164:167],v[112:115],a[16:19]
zk a[128:131],v196 offset:57344
zm a[20:23],a[164:167],v[116:119],a[20:23]
ze v1,v1
zm a[4:7],a[160:163],v[116:119],a[4:7]
zk a[132:135],v197 offset:57344
zc v96,v0,v1
zm a[8:11],a[160:163],v[120:123],a[8:11]
ze v2,v2
zm a[24:27],a[164:167],v[120:123],a[24:27]
zk a[136:139],v198 offset:57344
ze v3,v3
zk a[140:143],v199 offset:57344
zc v97,v2,v3
v_smfmac_f32_16x16x64_bf16 v[80:83], v[192:195], v[112:119], v253
ze v4,v4
v_smfmac_f32_16x16x64_bf16 v[84:87], v[192:195], v[120:127], v253
zk a[144:147],v196 offset:61440
ze v5,v5
zk a[148:151],v197 offset:61440
zc v98,v4,v5
zm a[56:59],a[172:175],v[120:123],a[56:59]
s_waitcnt lgkmcnt(6)
ze v6,v6
zm a[40:43],a[168:171],v[120:123],a[40:43]
zk a[152:155],v198 offset:61440
zm a[36:39],a[168:171],v[116:119],a[36:39]
ze v7,v7
zm a[52:55],a[172:175],v[116:119],a[52:55]
zk a[156:159],v199 offset:61440
zc v99,v6,v7
zm a[48:51],a[172:175],v[112:115],a[48:51]
ze v8,v8
zm a[32:35],a[168:171],v[112:115],a[32:35]
buffer_store_dwordx4 v[184:187], v214, s[40:43], s60 offen offset:2048 sc1
zm a[64:67],a[176:179],v[112:115],a[64:67]
ze v9,v9
zm a[80:83],a[180:183],v[112:115],a[80:83]
zc v100,v8,v9
zm a[84:87],a[180:183],v[116:119],a[84:87]
ze v10,v10
zm a[68:71],a[176:179],v[116:119],a[68:71]
zm a[72:75],a[176:179],v[120:123],a[72:75]
ze v11,v11
zm a[88:91],a[180:183],v[120:123],a[88:91]
zc v101,v10,v11
ze v12,v12
ze v13,v13
s_nop 0
zc v102,v12,v13
zm a[120:123],a[188:191],v[120:123],a[120:123]
ze v14,v14
zm a[104:107],a[184:187],v[120:123],a[104:107]
zm a[100:103],a[184:187],v[116:119],a[100:103]
ze v15,v15
zm a[116:119],a[188:191],v[116:119],a[116:119]
zc v103,v14,v15
zm a[112:115],a[188:191],v[112:115],a[112:115]
s_waitcnt vmcnt(20) lgkmcnt(0)
zm a[96:99],a[184:187],v[112:115],a[96:99]
s_barrier
zm v[32:35],a[128:131],a[192:195],v[64:67]
ze v16,v16
zm v[32:35],a[132:135],a[196:199],v[32:35]
zt a[160:161],v200 offset:49152
zm v[32:35],a[136:139],a[200:203],v[32:35]
ze v17,v17
zm v[32:35],a[140:143],a[204:207],v[32:35]
s_add_u32 s50, s84, 0x20000
zt a[162:163],v200 offset:53248
zc v104,v16,v17
zm v[36:39],a[156:159],a[204:207],v[64:67]
s_add_u32 m0, s44, 0x8000
ze v18,v18
zm v[36:39],a[152:155],a[200:203],v[36:39]
zt a[164:165],v201 offset:49152
zl v208,s[24:27],s50 offen offset:0 lds
zm v[36:39],a[148:151],a[196:199],v[36:39]
ze v19,v19
zm v[36:39],a[144:147],a[192:195],v[36:39]
zt a[166:167],v201 offset:53248
zc v105,v18,v19
zm v[44:47],a[144:147],a[208:211],v[68:71]
ze v20,v20
zm v[44:47],a[148:151],a[212:215],v[44:47]
zt a[168:169],v202 offset:49152
global_load_dword v254, v255, s[52:53]
zm v[44:47],a[152:155],a[216:219],v[44:47]
ze v21,v21
zm v[44:47],a[156:159],a[220:223],v[44:47]
zt a[170:171],v202 offset:53248
zc v106,v20,v21
zm v[40:43],a[140:143],a[220:223],v[68:71]
ze v22,v22
zm v[40:43],a[136:139],a[216:219],v[40:43]
zt a[172:173],v203 offset:49152
zl v209,s[24:27],s50 offen offset:1024 lds
zm v[40:43],a[132:135],a[212:215],v[40:43]
ze v23,v23
zm v[40:43],a[128:131],a[208:211],v[40:43]
zt a[174:175],v203 offset:53248
zc v107,v22,v23
zm v[48:51],a[128:131],a[224:227],v[72:75]
zm v[48:51],a[132:135],a[228:231],v[48:51]
zt a[176:177],v204 offset:49152
zm v[48:51],a[136:139],a[232:235],v[48:51]
zm v[48:51],a[140:143],a[236:239],v[48:51]
zt a[178:179],v204 offset:53248
zm v[52:55],a[156:159],a[236:239],v[72:75]
zm v[52:55],a[152:155],a[232:235],v[52:55]
zt a[180:181],v205 offset:49152
zl v210,s[24:27],s50 offen offset:2048 lds
zm v[52:55],a[148:151],a[228:231],v[52:55]
zm v[52:55],a[144:147],a[224:227],v[52:55]
zt a[182:183],v205 offset:53248
zt a[184:185],v206 offset:49152
s_waitcnt lgkmcnt(5)
zt a[186:187],v206 offset:53248
zt a[188:189],v207 offset:49152
zl v211,s[24:27],s50 offen offset:3072 lds
zt a[190:191],v207 offset:53248
zm a[0:3],a[160:163],v[96:99],a[0:3]
ze v32,v32
zm a[16:19],a[164:167],v[96:99],a[16:19]
zm a[20:23],a[164:167],v[100:103],a[20:23]
ze v33,v33
zm a[4:7],a[160:163],v[100:103],a[4:7]
zc v112,v32,v33
zm a[8:11],a[160:163],v[104:107],a[8:11]
ze v34,v34
zm a[24:27],a[164:167],v[104:107],a[24:27]
ze v35,v35
s_nop 0
zc v113,v34,v35
v_smfmac_f32_16x16x64_bf16 v[80:83], v[192:195], v[96:103], v253
ze v36,v36
v_smfmac_f32_16x16x64_bf16 v[84:87], v[192:195], v[104:111], v253
ze v37,v37
s_nop 0
zc v114,v36,v37
zm a[56:59],a[172:175],v[104:107],a[56:59]
s_waitcnt lgkmcnt(0)
ze v38,v38
zm a[40:43],a[168:171],v[104:107],a[40:43]
zm a[36:39],a[168:171],v[100:103],a[36:39]
ze v39,v39
zm a[52:55],a[172:175],v[100:103],a[52:55]
s_add_u32 s50, s84, 0x30000
zc v115,v38,v39
zm a[48:51],a[172:175],v[96:99],a[48:51]
s_add_u32 m0, s44, 0xc000
ze v40,v40
zm a[32:35],a[168:171],v[96:99],a[32:35]
zl v208,s[24:27],s50 offen offset:0 lds
zm a[64:67],a[176:179],v[96:99],a[64:67]
ze v41,v41
zm a[80:83],a[180:183],v[96:99],a[80:83]
zc v116,v40,v41
zm a[84:87],a[180:183],v[100:103],a[84:87]
ze v42,v42
zm a[68:71],a[176:179],v[100:103],a[68:71]
zl v209,s[24:27],s50 offen offset:1024 lds
zm a[72:75],a[176:179],v[104:107],a[72:75]
ze v43,v43
zm a[88:91],a[180:183],v[104:107],a[88:91]
zc v117,v42,v43
ze v44,v44
zl v210,s[24:27],s50 offen offset:2048 lds
ze v45,v45
s_nop 0
zc v118,v44,v45
zm a[120:123],a[188:191],v[104:107],a[120:123]
ze v46,v46
zm a[104:107],a[184:187],v[104:107],a[104:107]
zl v211,s[24:27],s50 offen offset:3072 lds
zm a[100:103],a[184:187],v[100:103],a[100:103]
ze v47,v47
zm a[116:119],a[188:191],v[100:103],a[116:119]
zc v119,v46,v47
zm a[112:115],a[188:191],v[96:99],a[112:115]
s_waitcnt vmcnt(9) lgkmcnt(0)
zm a[96:99],a[184:187],v[96:99],a[96:99]
s_mov_b64 exec, 1
buffer_store_dwordx2 v[216:217], off, s[92:95], s77 offset:0 sc1
s_mov_b64 exec, -1
ze v48,v48
zt a[160:161],v200 offset:57344
ze v49,v49
zt a[162:163],v200 offset:61440
zc v120,v48,v49
ze v50,v50
zt a[164:165],v201 offset:57344
ze v51,v51
zt a[166:167],v201 offset:61440
zc v121,v50,v51
ze v52,v52
zt a[168:169],v202 offset:57344
ze v53,v53
zt a[170:171],v202 offset:61440
zc v122,v52,v53
ze v54,v54
zt a[172:173],v203 offset:57344
ze v55,v55
zt a[174:175],v203 offset:61440
zc v123,v54,v55
zt a[176:177],v204 offset:57344
zt a[178:179],v204 offset:61440
zt a[180:181],v205 offset:57344
zt a[182:183],v205 offset:61440
zt a[184:185],v206 offset:57344
s_waitcnt lgkmcnt(5)
zt a[186:187],v206 offset:61440
zt a[188:189],v207 offset:57344
zt a[190:191],v207 offset:61440
s_nop 1
zm a[0:3],a[160:163],v[112:115],a[0:3]
s_lshl_b32 s74, s75, 1
v_xor_b32 v239, 0x10000, v200
zm a[16:19],a[164:167],v[112:115],a[16:19]
zm a[20:23],a[164:167],v[116:119],a[20:23]
s_mov_b32 s42, 0x20000
zm a[4:7],a[160:163],v[116:119],a[4:7]
s_add_u32 s54, s74, 2
zm a[8:11],a[160:163],v[120:123],a[8:11]
s_and_b32 s54, s54, 7
zm a[24:27],a[164:167],v[120:123],a[24:27]
s_lshl_b32 s54, s54, 5
v_xor_b32 v200, s54, v239
s_add_u32 s54, s74, 3
s_and_b32 s54, s54, 7
v_smfmac_f32_16x16x64_bf16 v[80:83], v[192:195], v[112:119], v253
s_lshl_b32 s54, s54, 5
v_xor_b32 v201, s54, v239
v_smfmac_f32_16x16x64_bf16 v[84:87], v[192:195], v[120:127], v253
s_add_u32 s54, s74, 4
s_and_b32 s54, s54, 7
s_lshl_b32 s54, s54, 5
v_xor_b32 v202, s54, v239
zm a[56:59],a[172:175],v[120:123],a[56:59]
s_waitcnt lgkmcnt(0)
s_add_u32 s54, s74, 5
zm a[40:43],a[168:171],v[120:123],a[40:43]
s_and_b32 s54, s54, 7
zm a[36:39],a[168:171],v[116:119],a[36:39]
s_lshl_b32 s54, s54, 5
v_xor_b32 v203, s54, v239
zm a[52:55],a[172:175],v[116:119],a[52:55]
s_add_u32 s54, s74, 6
zm a[48:51],a[172:175],v[112:115],a[48:51]
s_and_b32 s54, s54, 7
v_add_f32 v228, v80, v81
v_add_f32 v230, v82, v83
zm a[32:35],a[168:171],v[112:115],a[32:35]
s_lshl_b32 s54, s54, 5
v_xor_b32 v204, s54, v239
zm a[64:67],a[176:179],v[112:115],a[64:67]
s_add_u32 s54, s74, 7
v_add_f32 v232, v84, v85
v_add_f32 v234, v86, v87
zm a[80:83],a[180:183],v[112:115],a[80:83]
s_and_b32 s54, s54, 7
v_add_f32 v236, v228, v230
zm a[84:87],a[180:183],v[116:119],a[84:87]
s_lshl_b32 s54, s54, 5
v_xor_b32 v205, s54, v239
v_add_f32 v237, v232, v234
zm a[68:71],a[176:179],v[116:119],a[68:71]
s_add_u32 s54, s74, 8
v_add_f32 v236, v236, v237
zm a[72:75],a[176:179],v[120:123],a[72:75]
s_and_b32 s54, s54, 7
v_cmp_nge_f32 s[56:57], s55, v236
v_mov_b32 v238, s64
zm a[88:91],a[180:183],v[120:123],a[88:91]
s_lshl_b32 s54, s54, 5
v_xor_b32 v206, s54, v239
s_add_u32 s54, s74, 9
v_lshlrev_b32 v238, 2, v238
v_add_u32 v238, 0x26000, v238
s_and_b32 s54, s54, 7
s_lshl_b32 s54, s54, 5
v_xor_b32 v207, s54, v239
zm a[120:123],a[188:191],v[120:123],a[120:123]
s_or_b32 s71, s56, s57
v_mov_b32 v237, s71
zm a[104:107],a[184:187],v[120:123],a[104:107]
ds_write_b32 v238, v237 offset:0
zm a[100:103],a[184:187],v[116:119],a[100:103]
zm a[116:119],a[188:191],v[116:119],a[116:119]
zm a[112:115],a[188:191],v[112:115],a[112:115]
s_waitcnt vmcnt(11) lgkmcnt(0)
zm a[96:99],a[184:187],v[112:115],a[96:99]
s_barrier
v_mov_b32 v248, 0x26000
zk v[244:247],v248 offset:0
v_max_f32 v228, 0.5, v228
v_max_f32 v230, 0.5, v230
v_max_f32 v232, 0.5, v232
v_max_f32 v234, 0.5, v234
v_rcp_f32 v229, v228
v_rcp_f32 v231, v230
v_rcp_f32 v233, v232
v_rcp_f32 v235, v234
v_rcp_f32 v228, v228
v_rcp_f32 v230, v230
v_rcp_f32 v232, v232
v_rcp_f32 v234, v234
.p2align 3
zr v236,a0
zr v237,a1
zr v238,a2
zr v239,a3
zr v240,a16
zr v241,a17
zr v242,a18
zr v243,a19
s_waitcnt lgkmcnt(0)
v_or3_b32 v244, v244, v245, v246
v_or_b32 v244, v244, v247
zp v[236:237],v[236:237],v[228:229]
zp v[238:239],v[238:239],v[228:229]
zp v[240:241],v[240:241],v[228:229]
zp v[242:243],v[242:243],v[228:229]
v_readfirstlane_b32 s71, v244
zc v32,v236,v237
zc v33,v238,v239
zc v34,v240,v241
zc v35,v242,v243
s_andn2_b32 s71, s71, s76
s_cbranch_scc1 L_fail%=
.p2align 3
zr v236,a4
zr v237,a5
zr v238,a6
zr v239,a7
zr v240,a20
zr v241,a21
zr v242,a22
zr v243,a23
zp v[236:237],v[236:237],v[230:231]
zp v[238:239],v[238:239],v[230:231]
zp v[240:241],v[240:241],v[230:231]
zp v[242:243],v[242:243],v[230:231]
zc v36,v236,v237
zc v37,v238,v239
zc v38,v240,v241
zc v39,v242,v243
.p2align 3
zr v236,a8
zr v237,a9
zr v238,a10
zr v239,a11
zr v240,a24
zr v241,a25
zr v242,a26
zr v243,a27
zp v[236:237],v[236:237],v[232:233]
zp v[238:239],v[238:239],v[232:233]
zp v[240:241],v[240:241],v[232:233]
zp v[242:243],v[242:243],v[232:233]
zc v40,v236,v237
zc v41,v238,v239
zc v42,v240,v241
zc v43,v242,v243
s_add_u32 s60, s88, 0x4000
buffer_store_dwordx4 v[32:35], v214, s[40:43], s60 offen offset:0 sc1
.p2align 3
zt a[128:129],v200 offset:0
zt a[130:131],v200 offset:4096
zt a[132:133],v200 offset:8192
zt a[134:135],v200 offset:12288
zt a[136:137],v200 offset:16384
zt a[138:139],v200 offset:20480
zt a[140:141],v200 offset:24576
zt a[142:143],v200 offset:28672
zt a[144:145],v201 offset:0
zt a[146:147],v201 offset:4096
zt a[148:149],v201 offset:8192
zt a[150:151],v201 offset:12288
.p2align 3
zr v236,a32
zr v237,a33
zr v238,a34
zr v239,a35
zr v240,a48
zr v241,a49
zr v242,a50
zr v243,a51
zp v[236:237],v[236:237],v[228:229]
zp v[238:239],v[238:239],v[228:229]
zp v[240:241],v[240:241],v[228:229]
zp v[242:243],v[242:243],v[228:229]
zc v48,v236,v237
zc v49,v238,v239
zc v50,v240,v241
zc v51,v242,v243
.p2align 3
zr v236,a36
zr v237,a37
zr v238,a38
zr v239,a39
zr v240,a52
zr v241,a53
zr v242,a54
zr v243,a55
zp v[236:237],v[236:237],v[230:231]
zp v[238:239],v[238:239],v[230:231]
zp v[240:241],v[240:241],v[230:231]
zp v[242:243],v[242:243],v[230:231]
zc v52,v236,v237
zc v53,v238,v239
zc v54,v240,v241
zc v55,v242,v243
buffer_store_dwordx4 v[36:39], v214, s[40:43], s60 offen offset:1024 sc1
s_waitcnt lgkmcnt(10)
zm a[192:195],a[128:131],v[128:131],0
s_waitcnt lgkmcnt(8)
s_add_u32 s50, s85, 0
zt a[152:153],v201 offset:16384
zr v236,a40
zm a[192:195],a[132:135],v[144:147],a[192:195]
s_waitcnt lgkmcnt(7)
s_add_u32 m0, s45, 0
zr v237,a41
zr v238,a42
zm a[192:195],a[136:139],v[160:163],a[192:195]
s_waitcnt lgkmcnt(5)
zt a[154:155],v201 offset:20480
zl v208,s[24:27],s50 offen offset:0 lds
zm a[192:195],a[140:143],v[176:179],a[192:195]
zt a[156:157],v201 offset:24576
buffer_store_dwordx4 v[40:43], v214, s[40:43], s60 offen offset:2048 sc1
zm a[196:199],a[140:143],v[180:183],0
zr v239,a43
zr v240,a56
zm a[196:199],a[136:139],v[164:167],a[196:199]
zt a[158:159],v201 offset:28672
zr v241,a57
zm a[196:199],a[132:135],v[148:151],a[196:199]
zr v242,a58
zr v243,a59
zm a[196:199],a[128:131],v[132:135],a[196:199]
zt a[160:161],v202 offset:0
zl v209,s[24:27],s50 offen offset:1024 lds
zm a[200:203],a[128:131],v[136:139],0
zt a[162:163],v202 offset:4096
v_mul_f32 v236, v232, v236
zm a[200:203],a[132:135],v[152:155],a[200:203]
v_mul_f32 v237, v232, v237
v_mul_f32 v238, v232, v238
zm a[200:203],a[136:139],v[168:171],a[200:203]
zt a[164:165],v202 offset:8192
v_mul_f32 v239, v232, v239
zm a[200:203],a[140:143],v[184:187],a[200:203]
s_waitcnt lgkmcnt(3)
zt a[166:167],v202 offset:12288
v_mul_f32 v240, v232, v240
v_mul_f32 v241, v232, v241
v_mul_f32 v242, v232, v242
zt a[168:169],v202 offset:16384
v_mul_f32 v243, v232, v243
zt a[170:171],v202 offset:20480
zc v56,v236,v237
zl v210,s[24:27],s50 offen offset:2048 lds
zt a[172:173],v202 offset:24576
zc v57,v238,v239
zt a[174:175],v202 offset:28672
zc v58,v240,v241
zc v59,v242,v243
zt a[176:177],v203 offset:0
zm a[216:219],a[156:159],v[184:187],0
zm a[216:219],a[152:155],v[168:171],a[216:219]
zt a[178:179],v203 offset:4096
zm a[216:219],a[148:151],v[152:155],a[216:219]
zt a[180:181],v203 offset:8192
zm a[216:219],a[144:147],v[136:139],a[216:219]
zl v211,s[24:27],s50 offen offset:3072 lds
zm a[212:215],a[144:147],v[132:135],0
zt a[182:183],v203 offset:12288
zm a[212:215],a[148:151],v[148:151],a[212:215]
zt a[184:185],v203 offset:16384
zm a[212:215],a[152:155],v[164:167],a[212:215]
s_add_u32 s60, s88, 0x5000
zm a[212:215],a[156:159],v[180:183],a[212:215]
zt a[186:187],v203 offset:20480
buffer_store_dwordx4 v[48:51], v214, s[40:43], s60 offen offset:0 sc1
zm a[208:211],a[156:159],v[176:179],0
zt a[188:189],v203 offset:24576
zm a[208:211],a[152:155],v[160:163],a[208:211]
s_add_u32 s50, s85, 0x10000
zm a[208:211],a[148:151],v[144:147],a[208:211]
s_add_u32 m0, s45, 0x4000
zt a[190:191],v203 offset:28672
zm a[208:211],a[144:147],v[128:131],a[208:211]
s_waitcnt lgkmcnt(14)
zl v208,s[24:27],s50 offen offset:0 lds
zm a[224:227],a[160:163],v[128:131],0
s_waitcnt lgkmcnt(12)
zm a[224:227],a[164:167],v[144:147],a[224:227]
s_waitcnt lgkmcnt(10)
zm a[224:227],a[168:171],v[160:163],a[224:227]
s_waitcnt lgkmcnt(8)
zr v236,a64
zm a[224:227],a[172:175],v[176:179],a[224:227]
zr v237,a65
zm a[228:231],a[172:175],v[180:183],0
zr v238,a66
zr v239,a67
zm a[228:231],a[168:171],v[164:167],a[228:231]
zr v240,a80
zm a[228:231],a[164:167],v[148:151],a[228:231]
zl v209,s[24:27],s50 offen offset:1024 lds
zr v241,a81
zm a[228:231],a[160:163],v[132:135],a[228:231]
buffer_store_dwordx4 v[52:55], v214, s[40:43], s60 offen offset:1024 sc1
zr v242,a82
zm a[232:235],a[160:163],v[136:139],0
zr v243,a83
v_mul_f32 v236, v228, v236
zm a[232:235],a[164:167],v[152:155],a[232:235]
v_mul_f32 v237, v228, v237
zm a[232:235],a[168:171],v[168:171],a[232:235]
v_mul_f32 v238, v228, v238
v_mul_f32 v239, v228, v239
zm a[232:235],a[172:175],v[184:187],a[232:235]
s_waitcnt lgkmcnt(0)
v_mul_f32 v240, v228, v240
v_mul_f32 v241, v228, v241
v_mul_f32 v242, v228, v242
v_mul_f32 v243, v228, v243
zt a[128:129],v204 offset:0
zl v210,s[24:27],s50 offen offset:2048 lds
zt a[130:131],v204 offset:4096
zc v96,v236,v237
zc v97,v238,v239
zc v98,v240,v241
zt a[132:133],v204 offset:8192
zc v99,v242,v243
zt a[134:135],v204 offset:12288
zr v236,a68
buffer_store_dwordx4 v[56:59], v214, s[40:43], s60 offen offset:2048 sc1
zr v237,a69
zm a[248:251],a[188:191],v[184:187],0
zt a[136:137],v204 offset:16384
zr v238,a70
zm a[248:251],a[184:187],v[168:171],a[248:251]
zt a[138:139],v204 offset:20480
zr v239,a71
zm a[248:251],a[180:183],v[152:155],a[248:251]
zl v211,s[24:27],s50 offen offset:3072 lds
zr v240,a84
zm a[248:251],a[176:179],v[136:139],a[248:251]
zt a[140:141],v204 offset:24576
zr v241,a85
zm a[244:247],a[176:179],v[132:135],0
zt a[142:143],v204 offset:28672
zr v242,a86
zm a[244:247],a[180:183],v[148:151],a[244:247]
zr v243,a87
v_mul_f32 v236, v230, v236
zm a[244:247],a[184:187],v[164:167],a[244:247]
zt a[144:145],v205 offset:0
v_mul_f32 v237, v230, v237
zm a[244:247],a[188:191],v[180:183],a[244:247]
v_mul_f32 v238, v230, v238
v_mul_f32 v239, v230, v239
zm a[240:243],a[188:191],v[176:179],0
zt a[146:147],v205 offset:4096
v_mul_f32 v240, v230, v240
zm a[240:243],a[184:187],v[160:163],a[240:243]
zt a[148:149],v205 offset:8192
v_mul_f32 v241, v230, v241
zm a[240:243],a[180:183],v[144:147],a[240:243]
v_mul_f32 v242, v230, v242
v_mul_f32 v243, v230, v243
zm a[240:243],a[176:179],v[128:131],a[240:243]
s_waitcnt lgkmcnt(9)
zt a[150:151],v205 offset:12288
zm a[0:3],a[128:131],v[128:131],0
s_waitcnt lgkmcnt(8)
zt a[152:153],v205 offset:16384
buffer_load_dwordx2 v[226:227], v215, s[92:95], 0 offen offset:0 sc1
zm a[0:3],a[132:135],v[144:147],a[0:3]
s_waitcnt lgkmcnt(7)
s_mov_b32 s96, s22
zc v100,v236,v237
zc v101,v238,v239
zm a[0:3],a[136:139],v[160:163],a[0:3]
s_waitcnt lgkmcnt(5)
s_and_b32 s97, s23, 0xffff
zt a[154:155],v205 offset:20480
zc v102,v240,v241
zm a[0:3],a[140:143],v[176:179],a[0:3]
s_mov_b32 s98, 0x7a000
zt a[156:157],v205 offset:24576
zc v103,v242,v243
zm a[4:7],a[140:143],v[180:183],0
s_mov_b32 s99, 0x20000
zr v236,a72
zr v237,a73
zm a[4:7],a[136:139],v[164:167],a[4:7]
s_add_u32 s50, s86, 0x10000
zt a[158:159],v205 offset:28672
zr v238,a74
zm a[4:7],a[132:135],v[148:151],a[4:7]
s_add_u32 m0, s45, 0xc000
zr v239,a75
zr v240,a88
zm a[4:7],a[128:131],v[132:135],a[4:7]
zt a[160:161],v206 offset:0
zl v208,s[24:27],s50 offen offset:0 lds
zm a[8:11],a[128:131],v[136:139],0
zt a[162:163],v206 offset:4096
zr v241,a89
zm a[8:11],a[132:135],v[152:155],a[8:11]
zr v242,a90
zr v243,a91
zm a[8:11],a[136:139],v[168:171],a[8:11]
s_add_u32 s60, s88, 0x6000
zt a[164:165],v206 offset:8192
v_mul_f32 v236, v232, v236
zm a[8:11],a[140:143],v[184:187],a[8:11]
s_waitcnt lgkmcnt(3)
zt a[166:167],v206 offset:12288
buffer_store_dwordx4 v[96:99], v214, s[40:43], s60 offen offset:0 sc1
v_mul_f32 v237, v232, v237
v_mul_f32 v238, v232, v238
zt a[168:169],v206 offset:16384
zl v209,s[24:27],s50 offen offset:1024 lds
zt a[170:171],v206 offset:20480
v_mul_f32 v239, v232, v239
v_mul_f32 v240, v232, v240
v_mul_f32 v241, v232, v241
zt a[172:173],v206 offset:24576
v_mul_f32 v242, v232, v242
zt a[174:175],v206 offset:28672
v_mul_f32 v243, v232, v243
zc v104,v236,v237
zc v105,v238,v239
zt a[176:177],v207 offset:0
zc v106,v240,v241
zm a[24:27],a[156:159],v[184:187],0
zl v210,s[24:27],s50 offen offset:2048 lds
zc v107,v242,v243
zm a[24:27],a[152:155],v[168:171],a[24:27]
zt a[178:179],v207 offset:4096
zm a[24:27],a[148:151],v[152:155],a[24:27]
zt a[180:181],v207 offset:8192
zm a[24:27],a[144:147],v[136:139],a[24:27]
buffer_store_dwordx4 v[100:103], v214, s[40:43], s60 offen offset:1024 sc1
zm a[20:23],a[144:147],v[132:135],0
zt a[182:183],v207 offset:12288
zm a[20:23],a[148:151],v[148:151],a[20:23]
zt a[184:185],v207 offset:16384
zm a[20:23],a[152:155],v[164:167],a[20:23]
zm a[20:23],a[156:159],v[180:183],a[20:23]
zt a[186:187],v207 offset:20480
zl v211,s[24:27],s50 offen offset:3072 lds
zm a[16:19],a[156:159],v[176:179],0
zt a[188:189],v207 offset:24576
zm a[16:19],a[152:155],v[160:163],a[16:19]
zm a[16:19],a[148:151],v[144:147],a[16:19]
zt a[190:191],v207 offset:28672
zm a[16:19],a[144:147],v[128:131],a[16:19]
s_waitcnt lgkmcnt(14)
zm a[32:35],a[160:163],v[128:131],0
s_waitcnt lgkmcnt(12)
s_add_u32 s50, s86, 0
zm a[32:35],a[164:167],v[144:147],a[32:35]
s_waitcnt lgkmcnt(10)
s_add_u32 m0, s45, 0x8000
zm a[32:35],a[168:171],v[160:163],a[32:35]
s_waitcnt lgkmcnt(8)
zl v208,s[24:27],s50 offen offset:0 lds
zm a[32:35],a[172:175],v[176:179],a[32:35]
buffer_store_dwordx4 v[104:107], v214, s[40:43], s60 offen offset:2048 sc1
zm a[36:39],a[172:175],v[180:183],0
zm a[36:39],a[168:171],v[164:167],a[36:39]
zr v236,a96
zm a[36:39],a[164:167],v[148:151],a[36:39]
zr v237,a97
zr v238,a98
zm a[36:39],a[160:163],v[132:135],a[36:39]
zr v239,a99
zm a[40:43],a[160:163],v[136:139],0
zr v240,a112
zr v241,a113
zm a[40:43],a[164:167],v[152:155],a[40:43]
zl v209,s[24:27],s50 offen offset:1024 lds
zr v242,a114
zm a[40:43],a[168:171],v[168:171],a[40:43]
zr v243,a115
v_mul_f32 v236, v228, v236
zm a[40:43],a[172:175],v[184:187],a[40:43]
s_waitcnt vmcnt(25) lgkmcnt(0)
v_mul_f32 v237, v228, v237
s_barrier
v_mul_f32 v238, v228, v238
v_mul_f32 v239, v228, v239
v_mul_f32 v240, v228, v240
zt a[128:129],v200 offset:32768
v_mul_f32 v241, v228, v241
zt a[130:131],v200 offset:36864
zl v210,s[24:27],s50 offen offset:2048 lds
v_mul_f32 v242, v228, v242
zt a[132:133],v200 offset:40960
v_mul_f32 v243, v228, v243
zt a[134:135],v200 offset:45056
zc v112,v236,v237
zc v113,v238,v239
zc v114,v240,v241
zm a[56:59],a[188:191],v[184:187],0
zt a[136:137],v201 offset:32768
zc v115,v242,v243
zm a[56:59],a[184:187],v[168:171],a[56:59]
zt a[138:139],v201 offset:36864
zr v236,a100
zm a[56:59],a[180:183],v[152:155],a[56:59]
zr v237,a101
zr v238,a102
zm a[56:59],a[176:179],v[136:139],a[56:59]
zt a[140:141],v201 offset:40960
zl v211,s[24:27],s50 offen offset:3072 lds
zm a[52:55],a[176:179],v[132:135],0
zt a[142:143],v201 offset:45056
zr v239,a103
zm a[52:55],a[180:183],v[148:151],a[52:55]
zr v240,a116
zr v241,a117
zm a[52:55],a[184:187],v[164:167],a[52:55]
s_add_u32 s60, s88, 0x7000
zt a[144:145],v202 offset:32768
zr v242,a118
zm a[52:55],a[188:191],v[180:183],a[52:55]
buffer_store_dwordx4 v[112:115], v214, s[40:43], s60 offen offset:0 sc1
zr v243,a119
zm a[48:51],a[188:191],v[176:179],0
zt a[146:147],v202 offset:36864
v_mul_f32 v236, v230, v236
s_waitcnt vmcnt(12)
WL_pc0%=:
v_cmp_ne_u64 vcc, v[226:227], v[216:217]
s_nop 1
s_cbranch_vccnz WL_ps0%=
WL_pd0%=:
zm a[48:51],a[184:187],v[160:163],a[48:51]
zt a[148:149],v202 offset:40960
v_mul_f32 v237, v230, v237
zm a[48:51],a[180:183],v[144:147],a[48:51]
v_mul_f32 v238, v230, v238
v_mul_f32 v239, v230, v239
zm a[48:51],a[176:179],v[128:131],a[48:51]
s_waitcnt lgkmcnt(9)
zt a[150:151],v202 offset:45056
v_mul_f32 v240, v230, v240
zm a[192:195],a[128:131],v[32:35],a[192:195]
s_waitcnt lgkmcnt(8)
s_add_u32 s50, s87, 0
zt a[152:153],v203 offset:32768
v_mul_f32 v241, v230, v241
zm a[192:195],a[132:135],v[48:51],a[192:195]
s_add_u32 m0, s44, 0
v_mul_f32 v242, v230, v242
v_mul_f32 v243, v230, v243
zm a[196:199],a[132:135],v[52:55],a[196:199]
zt a[154:155],v203 offset:36864
zl v208,s[24:27],s50 offen offset:0 lds
zm a[196:199],a[128:131],v[36:39],a[196:199]
zt a[156:157],v203 offset:40960
zc v116,v236,v237
zm a[200:203],a[128:131],v[40:43],a[200:203]
zc v117,v238,v239
zc v118,v240,v241
zm a[200:203],a[132:135],v[56:59],a[200:203]
s_waitcnt lgkmcnt(7)
zt a[158:159],v203 offset:45056
zc v119,v242,v243
zl v209,s[24:27],s50 offen offset:1024 lds
zr v236,a104
zt a[160:161],v204 offset:32768
buffer_store_dwordx4 v[116:119], v214, s[40:43], s60 offen offset:1024 sc1
zt a[162:163],v204 offset:36864
zr v237,a105
zr v238,a106
zr v239,a107
zm a[216:219],a[140:143],v[56:59],a[216:219]
zt a[164:165],v204 offset:40960
zr v240,a120
zm a[216:219],a[136:139],v[40:43],a[216:219]
zt a[166:167],v204 offset:45056
zr v241,a121
zm a[212:215],a[136:139],v[36:39],a[212:215]
zl v210,s[24:27],s50 offen offset:2048 lds
zr v242,a122
zm a[212:215],a[140:143],v[52:55],a[212:215]
zt a[168:169],v205 offset:32768
zr v243,a123
zm a[208:211],a[140:143],v[48:51],a[208:211]
zt a[170:171],v205 offset:36864
v_mul_f32 v236, v232, v236
zm a[208:211],a[136:139],v[32:35],a[208:211]
s_waitcnt lgkmcnt(12)
v_mul_f32 v237, v232, v237
v_mul_f32 v238, v232, v238
zm a[224:227],a[144:147],v[32:35],a[224:227]
s_waitcnt lgkmcnt(10)
zt a[172:173],v205 offset:40960
v_mul_f32 v239, v232, v239
zm a[224:227],a[148:151],v[48:51],a[224:227]
zt a[174:175],v205 offset:45056
)"
R"(v_mul_f32 v240, v232, v240
zm a[228:231],a[148:151],v[52:55],a[228:231]
zl v211,s[24:27],s50 offen offset:3072 lds
v_mul_f32 v241, v232, v241
zm a[228:231],a[144:147],v[36:39],a[228:231]
zt a[176:177],v206 offset:32768
v_mul_f32 v242, v232, v242
zm a[232:235],a[144:147],v[40:43],a[232:235]
v_mul_f32 v243, v232, v243
zc v120,v236,v237
zm a[232:235],a[148:151],v[56:59],a[232:235]
s_waitcnt lgkmcnt(9)
zt a[178:179],v206 offset:36864
zc v121,v238,v239
zt a[180:181],v206 offset:40960
zc v122,v240,v241
s_add_u32 s72, s89, 0
zc v123,v242,v243
zt a[182:183],v206 offset:45056
zl v[0:3],v214,s[40:43],s72 offen offset:0 sc1
zt a[184:185],v207 offset:32768
buffer_store_dwordx4 v[120:123], v214, s[40:43], s60 offen offset:2048 sc1
zm a[248:251],a[156:159],v[56:59],a[248:251]
zm a[248:251],a[152:155],v[40:43],a[248:251]
zt a[186:187],v207 offset:36864
zm a[244:247],a[152:155],v[36:39],a[244:247]
zt a[188:189],v207 offset:40960
zm a[244:247],a[156:159],v[52:55],a[244:247]
zm a[240:243],a[156:159],v[48:51],a[240:243]
zt a[190:191],v207 offset:45056
zm a[240:243],a[152:155],v[32:35],a[240:243]
s_waitcnt lgkmcnt(14)
zl v[4:7],v214,s[40:43],s72 offen offset:1024 sc1
zm a[0:3],a[160:163],v[32:35],a[0:3]
s_waitcnt lgkmcnt(12)
zm a[0:3],a[164:167],v[48:51],a[0:3]
zm a[4:7],a[164:167],v[52:55],a[4:7]
zm a[4:7],a[160:163],v[36:39],a[4:7]
zm a[8:11],a[160:163],v[40:43],a[8:11]
zm a[8:11],a[164:167],v[56:59],a[8:11]
s_waitcnt lgkmcnt(8)
zl v[8:11],v214,s[40:43],s72 offen offset:2048 sc1
zm a[24:27],a[172:175],v[56:59],a[24:27]
zm a[24:27],a[168:171],v[40:43],a[24:27]
zm a[20:23],a[168:171],v[36:39],a[20:23]
zm a[20:23],a[172:175],v[52:55],a[20:23]
zm a[16:19],a[172:175],v[48:51],a[16:19]
zt a[128:129],v200 offset:49152
zm a[16:19],a[168:171],v[32:35],a[16:19]
s_waitcnt lgkmcnt(7)
zt a[130:131],v200 offset:53248
zm a[32:35],a[176:179],v[32:35],a[32:35]
s_waitcnt lgkmcnt(6)
s_add_u32 s72, s89, 0x1000
zm a[32:35],a[180:183],v[48:51],a[32:35]
zt a[132:133],v200 offset:57344
zl v[16:19],v214,s[40:43],s72 offen offset:0 sc1
zm a[36:39],a[180:183],v[52:55],a[36:39]
zt a[134:135],v200 offset:61440
zm a[36:39],a[176:179],v[36:39],a[36:39]
zm a[40:43],a[176:179],v[40:43],a[40:43]
zt a[136:137],v201 offset:49152
zm a[40:43],a[180:183],v[56:59],a[40:43]
s_waitcnt lgkmcnt(5)
zt a[138:139],v201 offset:53248
zt a[140:141],v201 offset:57344
zl v[20:23],v214,s[40:43],s72 offen offset:1024 sc1
zt a[142:143],v201 offset:61440
zm a[56:59],a[188:191],v[56:59],a[56:59]
zt a[144:145],v202 offset:49152
zm a[56:59],a[184:187],v[40:43],a[56:59]
zm a[52:55],a[184:187],v[36:39],a[52:55]
zt a[146:147],v202 offset:53248
zm a[52:55],a[188:191],v[52:55],a[52:55]
zt a[148:149],v202 offset:57344
zm a[48:51],a[188:191],v[48:51],a[48:51]
zm a[48:51],a[184:187],v[32:35],a[48:51]
s_waitcnt lgkmcnt(9)
zt a[150:151],v202 offset:61440
zm a[192:195],a[128:131],v[96:99],a[192:195]
s_waitcnt lgkmcnt(8)
s_add_u32 s50, s87, 0x10000
zt a[152:153],v203 offset:49152
zm a[192:195],a[132:135],v[112:115],a[192:195]
s_add_u32 m0, s44, 0x4000
zm a[196:199],a[132:135],v[116:119],a[196:199]
zt a[154:155],v203 offset:53248
zl v208,s[24:27],s50 offen offset:0 lds
zm a[196:199],a[128:131],v[100:103],a[196:199]
zt a[156:157],v203 offset:57344
zm a[200:203],a[128:131],v[104:107],a[200:203]
zl v209,s[24:27],s50 offen offset:1024 lds
zm a[200:203],a[132:135],v[120:123],a[200:203]
s_waitcnt lgkmcnt(7)
zt a[158:159],v203 offset:61440
zt a[160:161],v204 offset:49152
zt a[162:163],v204 offset:53248
zl v210,s[24:27],s50 offen offset:2048 lds
zm a[216:219],a[140:143],v[120:123],a[216:219]
zt a[164:165],v204 offset:57344
zm a[216:219],a[136:139],v[104:107],a[216:219]
zt a[166:167],v204 offset:61440
zm a[212:215],a[136:139],v[100:103],a[212:215]
zm a[212:215],a[140:143],v[116:119],a[212:215]
zt a[168:169],v205 offset:49152
zl v211,s[24:27],s50 offen offset:3072 lds
zm a[208:211],a[140:143],v[112:115],a[208:211]
zt a[170:171],v205 offset:53248
zm a[208:211],a[136:139],v[96:99],a[208:211]
s_waitcnt lgkmcnt(12)
zm a[224:227],a[144:147],v[96:99],a[224:227]
s_waitcnt lgkmcnt(10)
zt a[172:173],v205 offset:57344
zm a[224:227],a[148:151],v[112:115],a[224:227]
zt a[174:175],v205 offset:61440
zl v[24:27],v214,s[40:43],s72 offen offset:2048 sc1
zm a[228:231],a[148:151],v[116:119],a[228:231]
zm a[228:231],a[144:147],v[100:103],a[228:231]
zt a[176:177],v206 offset:49152
zm a[232:235],a[144:147],v[104:107],a[232:235]
zm a[232:235],a[148:151],v[120:123],a[232:235]
s_waitcnt lgkmcnt(9)
zt a[178:179],v206 offset:53248
zt a[180:181],v206 offset:57344
zt a[182:183],v206 offset:61440
s_add_u32 s72, s89, 0x2000
zt a[184:185],v207 offset:49152
zm a[248:251],a[156:159],v[120:123],a[248:251]
zl v[64:67],v214,s[40:43],s72 offen offset:0 sc1
zm a[248:251],a[152:155],v[104:107],a[248:251]
zt a[186:187],v207 offset:53248
zm a[244:247],a[152:155],v[100:103],a[244:247]
zt a[188:189],v207 offset:57344
zm a[244:247],a[156:159],v[116:119],a[244:247]
zm a[240:243],a[156:159],v[112:115],a[240:243]
zt a[190:191],v207 offset:61440
zm a[240:243],a[152:155],v[96:99],a[240:243]
s_waitcnt lgkmcnt(14)
zl v[68:71],v214,s[40:43],s72 offen offset:1024 sc1
zm a[0:3],a[160:163],v[96:99],a[0:3]
s_waitcnt lgkmcnt(12)
zm a[0:3],a[164:167],v[112:115],a[0:3]
zm a[4:7],a[164:167],v[116:119],a[4:7]
zm a[4:7],a[160:163],v[100:103],a[4:7]
zl v[72:75],v214,s[40:43],s72 offen offset:2048 sc1
v_xor_b32 v200, 0x10000, v200
zm a[8:11],a[160:163],v[104:107],a[8:11]
v_xor_b32 v201, 0x10000, v201
zm a[8:11],a[164:167],v[120:123],a[8:11]
s_waitcnt lgkmcnt(8)
v_xor_b32 v202, 0x10000, v202
v_xor_b32 v203, 0x10000, v203
v_xor_b32 v204, 0x10000, v204
v_xor_b32 v205, 0x10000, v205
v_xor_b32 v206, 0x10000, v206
zm a[24:27],a[172:175],v[120:123],a[24:27]
v_xor_b32 v207, 0x10000, v207
zm a[24:27],a[168:171],v[104:107],a[24:27]
s_waitcnt vmcnt(32) lgkmcnt(0)
zm a[20:23],a[168:171],v[100:103],a[20:23]
s_barrier
s_add_u32 s72, s89, 0x3000
zm a[20:23],a[172:175],v[116:119],a[20:23]
zl v[80:83],v214,s[40:43],s72 offen offset:0 sc1
zm a[16:19],a[172:175],v[112:115],a[16:19]
zt a[128:129],v200 offset:0
zm a[16:19],a[168:171],v[96:99],a[16:19]
zt a[130:131],v200 offset:4096
zm a[32:35],a[176:179],v[96:99],a[32:35]
zl v[84:87],v214,s[40:43],s72 offen offset:1024 sc1
zm a[32:35],a[180:183],v[112:115],a[32:35]
zt a[132:133],v200 offset:8192
zm a[36:39],a[180:183],v[116:119],a[36:39]
zt a[134:135],v200 offset:12288
zm a[36:39],a[176:179],v[100:103],a[36:39]
zm a[40:43],a[176:179],v[104:107],a[40:43]
zt a[136:137],v201 offset:0
s_waitcnt vmcnt(14)
s_mov_b64 exec, 1
buffer_store_dwordx2 v[216:217], off, s[92:95], s77 offset:1024 sc1
s_mov_b64 exec, -1
zm a[40:43],a[180:183],v[120:123],a[40:43]
zt a[138:139],v201 offset:4096
zl v[88:91],v214,s[40:43],s72 offen offset:2048 sc1
zt a[140:141],v201 offset:8192
zt a[142:143],v201 offset:12288
zm a[56:59],a[188:191],v[120:123],a[56:59]
zt a[144:145],v202 offset:0
zm a[56:59],a[184:187],v[104:107],a[56:59]
zm a[52:55],a[184:187],v[100:103],a[52:55]
zt a[146:147],v202 offset:4096
zm a[52:55],a[188:191],v[116:119],a[52:55]
zt a[148:149],v202 offset:8192
zm a[48:51],a[188:191],v[112:115],a[48:51]
zm a[48:51],a[184:187],v[96:99],a[48:51]
s_waitcnt lgkmcnt(9)
s_waitcnt vmcnt(7)
zt a[150:151],v202 offset:12288
zm a[192:195],a[128:131],v[0:3],a[192:195]
s_waitcnt lgkmcnt(8)
s_add_u32 s50, s85, 0x20000
zt a[152:153],v203 offset:0
zm a[192:195],a[132:135],v[16:19],a[192:195]
s_add_u32 m0, s44, 0x8000
zm a[196:199],a[132:135],v[20:23],a[196:199]
zt a[154:155],v203 offset:4096
zl v208,s[24:27],s50 offen offset:0 lds
zm a[196:199],a[128:131],v[4:7],a[196:199]
zt a[156:157],v203 offset:8192
zl v209,s[24:27],s50 offen offset:1024 lds
zm a[200:203],a[128:131],v[8:11],a[200:203]
zm a[200:203],a[132:135],v[24:27],a[200:203]
s_waitcnt lgkmcnt(7)
zt a[158:159],v203 offset:12288
zt a[160:161],v204 offset:0
zl v210,s[24:27],s50 offen offset:2048 lds
zt a[162:163],v204 offset:4096
zm a[216:219],a[140:143],v[24:27],a[216:219]
zt a[164:165],v204 offset:8192
zm a[216:219],a[136:139],v[8:11],a[216:219]
zt a[166:167],v204 offset:12288
zl v211,s[24:27],s50 offen offset:3072 lds
zm a[212:215],a[136:139],v[4:7],a[212:215]
zm a[212:215],a[140:143],v[20:23],a[212:215]
zt a[168:169],v205 offset:0
zm a[208:211],a[140:143],v[16:19],a[208:211]
s_add_u32 s72, s90, 0
zt a[170:171],v205 offset:4096
zm a[208:211],a[136:139],v[0:3],a[208:211]
s_waitcnt lgkmcnt(12)
zl a[64:67],v214,s[40:43],s72 offen offset:0 sc1
zm a[224:227],a[144:147],v[0:3],a[224:227]
s_waitcnt lgkmcnt(10)
zt a[172:173],v205 offset:8192
zm a[224:227],a[148:151],v[16:19],a[224:227]
zt a[174:175],v205 offset:12288
zm a[228:231],a[148:151],v[20:23],a[228:231]
zm a[228:231],a[144:147],v[4:7],a[228:231]
zt a[176:177],v206 offset:0
zl a[68:71],v214,s[40:43],s72 offen offset:1024 sc1
zm a[232:235],a[144:147],v[8:11],a[232:235]
zm a[232:235],a[148:151],v[24:27],a[232:235]
s_waitcnt lgkmcnt(9)
zt a[178:179],v206 offset:4096
zt a[180:181],v206 offset:8192
zl a[72:75],v214,s[40:43],s72 offen offset:2048 sc1
zt a[182:183],v206 offset:12288
zt a[184:185],v207 offset:0
zm a[248:251],a[156:159],v[24:27],a[248:251]
zm a[248:251],a[152:155],v[8:11],a[248:251]
zt a[186:187],v207 offset:4096
zm a[244:247],a[152:155],v[4:7],a[244:247]
zt a[188:189],v207 offset:8192
zm a[244:247],a[156:159],v[20:23],a[244:247]
zm a[240:243],a[156:159],v[16:19],a[240:243]
s_add_u32 s72, s90, 0x1000
zt a[190:191],v207 offset:12288
zm a[240:243],a[152:155],v[0:3],a[240:243]
s_waitcnt lgkmcnt(14)
zl a[80:83],v214,s[40:43],s72 offen offset:0 sc1
zm a[0:3],a[160:163],v[0:3],a[0:3]
s_waitcnt lgkmcnt(12)
zm a[0:3],a[164:167],v[16:19],a[0:3]
zm a[4:7],a[164:167],v[20:23],a[4:7]
zl a[84:87],v214,s[40:43],s72 offen offset:1024 sc1
zm a[4:7],a[160:163],v[4:7],a[4:7]
zm a[8:11],a[160:163],v[8:11],a[8:11]
zm a[8:11],a[164:167],v[24:27],a[8:11]
s_waitcnt lgkmcnt(8)
zl a[88:91],v214,s[40:43],s72 offen offset:2048 sc1
zm a[24:27],a[172:175],v[24:27],a[24:27]
zm a[24:27],a[168:171],v[8:11],a[24:27]
zm a[20:23],a[168:171],v[4:7],a[20:23]
zm a[20:23],a[172:175],v[20:23],a[20:23]
s_add_u32 s72, s90, 0x2000
zm a[16:19],a[172:175],v[16:19],a[16:19]
zt a[128:129],v200 offset:16384
zl a[96:99],v214,s[40:43],s72 offen offset:0 sc1
zm a[16:19],a[168:171],v[0:3],a[16:19]
s_waitcnt lgkmcnt(7)
zt a[130:131],v200 offset:20480
zm a[32:35],a[176:179],v[0:3],a[32:35]
s_waitcnt lgkmcnt(6)
zm a[32:35],a[180:183],v[16:19],a[32:35]
zt a[132:133],v200 offset:24576
zm a[36:39],a[180:183],v[20:23],a[36:39]
zt a[134:135],v200 offset:28672
zl a[100:103],v214,s[40:43],s72 offen offset:1024 sc1
zm a[36:39],a[176:179],v[4:7],a[36:39]
zm a[40:43],a[176:179],v[8:11],a[40:43]
zt a[136:137],v201 offset:16384
zm a[40:43],a[180:183],v[24:27],a[40:43]
s_waitcnt lgkmcnt(5)
zt a[138:139],v201 offset:20480
zl a[104:107],v214,s[40:43],s72 offen offset:2048 sc1
zt a[140:141],v201 offset:24576
zt a[142:143],v201 offset:28672
zm a[56:59],a[188:191],v[24:27],a[56:59]
zt a[144:145],v202 offset:16384
zm a[56:59],a[184:187],v[8:11],a[56:59]
zm a[52:55],a[184:187],v[4:7],a[52:55]
zt a[146:147],v202 offset:20480
zm a[52:55],a[188:191],v[20:23],a[52:55]
zt a[148:149],v202 offset:24576
zm a[48:51],a[188:191],v[16:19],a[48:51]
zm a[48:51],a[184:187],v[0:3],a[48:51]
s_waitcnt lgkmcnt(9)
s_waitcnt vmcnt(13)
zt a[150:151],v202 offset:28672
zm a[192:195],a[128:131],v[64:67],a[192:195]
s_waitcnt lgkmcnt(8)
s_add_u32 s50, s85, 0x30000
zt a[152:153],v203 offset:16384
zm a[192:195],a[132:135],v[80:83],a[192:195]
s_add_u32 m0, s44, 0xc000
zm a[196:199],a[132:135],v[84:87],a[196:199]
zt a[154:155],v203 offset:20480
zl v208,s[24:27],s50 offen offset:0 lds
zm a[196:199],a[128:131],v[68:71],a[196:199]
zt a[156:157],v203 offset:24576
zl v209,s[24:27],s50 offen offset:1024 lds
zm a[200:203],a[128:131],v[72:75],a[200:203]
zm a[200:203],a[132:135],v[88:91],a[200:203]
s_waitcnt lgkmcnt(7)
zt a[158:159],v203 offset:28672
zt a[160:161],v204 offset:16384
zl v210,s[24:27],s50 offen offset:2048 lds
zt a[162:163],v204 offset:20480
zm a[216:219],a[140:143],v[88:91],a[216:219]
zt a[164:165],v204 offset:24576
zm a[216:219],a[136:139],v[72:75],a[216:219]
zt a[166:167],v204 offset:28672
zl v211,s[24:27],s50 offen offset:3072 lds
zm a[212:215],a[136:139],v[68:71],a[212:215]
zm a[212:215],a[140:143],v[84:87],a[212:215]
zt a[168:169],v205 offset:16384
zm a[208:211],a[140:143],v[80:83],a[208:211]
s_add_u32 s72, s90, 0x3000
zt a[170:171],v205 offset:20480
zm a[208:211],a[136:139],v[64:67],a[208:211]
s_waitcnt lgkmcnt(12)
zl a[112:115],v214,s[40:43],s72 offen offset:0 sc1
zm a[224:227],a[144:147],v[64:67],a[224:227]
s_waitcnt lgkmcnt(10)
zt a[172:173],v205 offset:24576
zm a[224:227],a[148:151],v[80:83],a[224:227]
zt a[174:175],v205 offset:28672
zm a[228:231],a[148:151],v[84:87],a[228:231]
zm a[228:231],a[144:147],v[68:71],a[228:231]
zt a[176:177],v206 offset:16384
zl a[116:119],v214,s[40:43],s72 offen offset:1024 sc1
zm a[232:235],a[144:147],v[72:75],a[232:235]
zm a[232:235],a[148:151],v[88:91],a[232:235]
s_waitcnt lgkmcnt(9)
zt a[178:179],v206 offset:20480
zt a[180:181],v206 offset:24576
zl a[120:123],v214,s[40:43],s72 offen offset:2048 sc1
zt a[182:183],v206 offset:28672
zt a[184:185],v207 offset:16384
zm a[248:251],a[156:159],v[88:91],a[248:251]
zm a[248:251],a[152:155],v[72:75],a[248:251]
zt a[186:187],v207 offset:20480
zm a[244:247],a[152:155],v[68:71],a[244:247]
zt a[188:189],v207 offset:24576
zm a[244:247],a[156:159],v[84:87],a[244:247]
zm a[240:243],a[156:159],v[80:83],a[240:243]
s_add_u32 s72, s91, 0
zt a[190:191],v207 offset:28672
zm a[240:243],a[152:155],v[64:67],a[240:243]
s_waitcnt lgkmcnt(14)
zl v[128:131],v214,s[40:43],s72 offen offset:0 sc1
zm a[0:3],a[160:163],v[64:67],a[0:3]
s_waitcnt lgkmcnt(12)
zm a[0:3],a[164:167],v[80:83],a[0:3]
zm a[4:7],a[164:167],v[84:87],a[4:7]
zl v[132:135],v214,s[40:43],s72 offen offset:1024 sc1
zm a[4:7],a[160:163],v[68:71],a[4:7]
zm a[8:11],a[160:163],v[72:75],a[8:11]
zm a[8:11],a[164:167],v[88:91],a[8:11]
s_waitcnt lgkmcnt(8)
zl v[136:139],v214,s[40:43],s72 offen offset:2048 sc1
zm a[24:27],a[172:175],v[88:91],a[24:27]
zm a[24:27],a[168:171],v[72:75],a[24:27]
s_waitcnt vmcnt(47) lgkmcnt(0)
zm a[20:23],a[168:171],v[68:71],a[20:23]
s_barrier
zm a[20:23],a[172:175],v[84:87],a[20:23]
s_add_u32 s72, s91, 0x1000
zm a[16:19],a[172:175],v[80:83],a[16:19]
zt a[128:129],v200 offset:32768
zl v[144:147],v214,s[40:43],s72 offen offset:0 sc1
zm a[16:19],a[168:171],v[64:67],a[16:19]
zt a[130:131],v200 offset:36864
zm a[32:35],a[176:179],v[64:67],a[32:35]
zm a[32:35],a[180:183],v[80:83],a[32:35]
zt a[132:133],v200 offset:40960
zm a[36:39],a[180:183],v[84:87],a[36:39]
zt a[134:135],v200 offset:45056
zl v[148:151],v214,s[40:43],s72 offen offset:1024 sc1
zm a[36:39],a[176:179],v[68:71],a[36:39]
zm a[40:43],a[176:179],v[72:75],a[40:43]
zt a[136:137],v200 offset:49152
zm a[40:43],a[180:183],v[88:91],a[40:43]
zt a[138:139],v200 offset:53248
zl v[152:155],v214,s[40:43],s72 offen offset:2048 sc1
zt a[140:141],v200 offset:57344
zt a[142:143],v200 offset:61440
zm a[56:59],a[188:191],v[88:91],a[56:59]
zt a[144:145],v201 offset:32768
zm a[56:59],a[184:187],v[72:75],a[56:59]
zm a[52:55],a[184:187],v[68:71],a[52:55]
zt a[146:147],v201 offset:36864
zm a[52:55],a[188:191],v[84:87],a[52:55]
zt a[148:149],v201 offset:40960
zm a[48:51],a[188:191],v[80:83],a[48:51]
zm a[48:51],a[184:187],v[64:67],a[48:51]
s_waitcnt lgkmcnt(9)
s_waitcnt vmcnt(6)
zt a[150:151],v201 offset:45056
zm a[192:195],a[128:131],a[64:67],a[192:195]
s_waitcnt lgkmcnt(8)
zt a[152:153],v201 offset:49152
buffer_load_dwordx2 v[226:227], v215, s[92:95], 0 offen offset:1024 sc1
zm a[192:195],a[132:135],a[80:83],a[192:195]
s_waitcnt lgkmcnt(7)
zm a[192:195],a[136:139],a[96:99],a[192:195]
s_waitcnt lgkmcnt(5)
s_add_u32 s50, s86, 0x20000
zt a[154:155],v201 offset:53248
zm a[192:195],a[140:143],a[112:115],a[192:195]
s_add_u32 m0, s45, 0
zt a[156:157],v201 offset:57344
zm a[196:199],a[140:143],a[116:119],a[196:199]
zl v208,s[24:27],s50 offen offset:0 lds
zm a[196:199],a[136:139],a[100:103],a[196:199]
zt a[158:159],v201 offset:61440
zm a[196:199],a[132:135],a[84:87],a[196:199]
zm a[196:199],a[128:131],a[68:71],a[196:199]
zt a[160:161],v202 offset:32768
zm a[200:203],a[128:131],a[72:75],a[200:203]
zt a[162:163],v202 offset:36864
zm a[200:203],a[132:135],a[88:91],a[200:203]
zl v209,s[24:27],s50 offen offset:1024 lds
zm a[200:203],a[136:139],a[104:107],a[200:203]
zt a[164:165],v202 offset:40960
zm a[200:203],a[140:143],a[120:123],a[200:203]
s_waitcnt lgkmcnt(3)
zt a[166:167],v202 offset:45056
zt a[168:169],v202 offset:49152
zt a[170:171],v202 offset:53248
zl v210,s[24:27],s50 offen offset:2048 lds
zt a[172:173],v202 offset:57344
zt a[174:175],v202 offset:61440
zt a[176:177],v203 offset:32768
zl v211,s[24:27],s50 offen offset:3072 lds
zm a[216:219],a[156:159],a[120:123],a[216:219]
zm a[216:219],a[152:155],a[104:107],a[216:219]
zt a[178:179],v203 offset:36864
zm a[216:219],a[148:151],a[88:91],a[216:219]
s_add_u32 s72, s91, 0x2000
zt a[180:181],v203 offset:40960
zm a[216:219],a[144:147],a[72:75],a[216:219]
zl v[160:163],v214,s[40:43],s72 offen offset:0 sc1
zm a[212:215],a[144:147],a[68:71],a[212:215]
zt a[182:183],v203 offset:45056
zm a[212:215],a[148:151],a[84:87],a[212:215]
zt a[184:185],v203 offset:49152
zm a[212:215],a[152:155],a[100:103],a[212:215]
zm a[212:215],a[156:159],a[116:119],a[212:215]
zt a[186:187],v203 offset:53248
zm a[208:211],a[156:159],a[112:115],a[208:211]
zt a[188:189],v203 offset:57344
zl v[164:167],v214,s[40:43],s72 offen offset:1024 sc1
zm a[208:211],a[152:155],a[96:99],a[208:211]
zm a[208:211],a[148:151],a[80:83],a[208:211]
zt a[190:191],v203 offset:61440
zm a[208:211],a[144:147],a[64:67],a[208:211]
s_waitcnt lgkmcnt(14)
zm a[224:227],a[160:163],a[64:67],a[224:227]
s_waitcnt lgkmcnt(12)
zm a[224:227],a[164:167],a[80:83],a[224:227]
s_waitcnt lgkmcnt(10)
zl v[168:171],v214,s[40:43],s72 offen offset:2048 sc1
zm a[224:227],a[168:171],a[96:99],a[224:227]
s_waitcnt lgkmcnt(8)
zm a[224:227],a[172:175],a[112:115],a[224:227]
zm a[228:231],a[172:175],a[116:119],a[228:231]
zm a[228:231],a[168:171],a[100:103],a[228:231]
zm a[228:231],a[164:167],a[84:87],a[228:231]
zm a[228:231],a[160:163],a[68:71],a[228:231]
zm a[232:235],a[160:163],a[72:75],a[232:235]
zm a[232:235],a[164:167],a[88:91],a[232:235]
s_add_u32 s72, s91, 0x3000
zm a[232:235],a[168:171],a[104:107],a[232:235]
zl v[176:179],v214,s[40:43],s72 offen offset:0 sc1
zm a[232:235],a[172:175],a[120:123],a[232:235]
s_waitcnt lgkmcnt(0)
zt a[128:129],v204 offset:32768
zt a[130:131],v204 offset:36864
zl v[180:183],v214,s[40:43],s72 offen offset:1024 sc1
zt a[132:133],v204 offset:40960
zt a[134:135],v204 offset:45056
zm a[248:251],a[188:191],a[120:123],a[248:251]
zt a[136:137],v204 offset:49152
zl v[184:187],v214,s[40:43],s72 offen offset:2048 sc1
zm a[248:251],a[184:187],a[104:107],a[248:251]
zt a[138:139],v204 offset:53248
zm a[248:251],a[180:183],a[88:91],a[248:251]
zm a[248:251],a[176:179],a[72:75],a[248:251]
zt a[140:141],v204 offset:57344
zm a[244:247],a[176:179],a[68:71],a[244:247]
zt a[142:143],v204 offset:61440
zm a[244:247],a[180:183],a[84:87],a[244:247]
zm a[244:247],a[184:187],a[100:103],a[244:247]
zt a[144:145],v205 offset:32768
s_waitcnt vmcnt(10)
WL_pc1%=:
v_cmp_ne_u64 vcc, v[226:227], v[216:217]
s_nop 1
s_cbranch_vccnz WL_ps1%=
WL_pd1%=:
zm a[244:247],a[188:191],a[116:119],a[244:247]
zm a[240:243],a[188:191],a[112:115],a[240:243]
zt a[146:147],v205 offset:36864
zm a[240:243],a[184:187],a[96:99],a[240:243]
zt a[148:149],v205 offset:40960
zm a[240:243],a[180:183],a[80:83],a[240:243]
zm a[240:243],a[176:179],a[64:67],a[240:243]
s_waitcnt lgkmcnt(9)
zt a[150:151],v205 offset:45056
zm a[0:3],a[128:131],a[64:67],a[0:3]
s_waitcnt lgkmcnt(8)
s_add_u32 s50, s86, 0x30000
zt a[152:153],v205 offset:49152
zm a[0:3],a[132:135],a[80:83],a[0:3]
s_waitcnt lgkmcnt(7)
s_add_u32 m0, s45, 0x4000
zm a[0:3],a[136:139],a[96:99],a[0:3]
s_waitcnt lgkmcnt(5)
zt a[154:155],v205 offset:53248
zl v208,s[24:27],s50 offen offset:0 lds
zm a[0:3],a[140:143],a[112:115],a[0:3]
zt a[156:157],v205 offset:57344
zm a[4:7],a[140:143],a[116:119],a[4:7]
zl v209,s[24:27],s50 offen offset:1024 lds
zm a[4:7],a[136:139],a[100:103],a[4:7]
zt a[158:159],v205 offset:61440
zm a[4:7],a[132:135],a[84:87],a[4:7]
zm a[4:7],a[128:131],a[68:71],a[4:7]
zt a[160:161],v206 offset:32768
zm a[8:11],a[128:131],a[72:75],a[8:11]
zt a[162:163],v206 offset:36864
zl v210,s[24:27],s50 offen offset:2048 lds
zm a[8:11],a[132:135],a[88:91],a[8:11]
zm a[8:11],a[136:139],a[104:107],a[8:11]
zt a[164:165],v206 offset:40960
zm a[8:11],a[140:143],a[120:123],a[8:11]
s_waitcnt lgkmcnt(3)
zt a[166:167],v206 offset:45056
zt a[168:169],v206 offset:49152
zl v211,s[24:27],s50 offen offset:3072 lds
zt a[170:171],v206 offset:53248
s_add_u32 s72, s89, 0x4000
zt a[172:173],v206 offset:57344
zt a[174:175],v206 offset:61440
zl v[32:35],v214,s[40:43],s72 offen offset:0 sc1
zt a[176:177],v207 offset:32768
zm a[24:27],a[156:159],a[120:123],a[24:27]
zm a[24:27],a[152:155],a[104:107],a[24:27]
zt a[178:179],v207 offset:36864
zm a[24:27],a[148:151],a[88:91],a[24:27]
zt a[180:181],v207 offset:40960
zl v[36:39],v214,s[40:43],s72 offen offset:1024 sc1
zm a[24:27],a[144:147],a[72:75],a[24:27]
zm a[20:23],a[144:147],a[68:71],a[20:23]
zt a[182:183],v207 offset:45056
zm a[20:23],a[148:151],a[84:87],a[20:23]
zt a[184:185],v207 offset:49152
zm a[20:23],a[152:155],a[100:103],a[20:23]
zl v[40:43],v214,s[40:43],s72 offen offset:2048 sc1
zm a[20:23],a[156:159],a[116:119],a[20:23]
zt a[186:187],v207 offset:53248
zm a[16:19],a[156:159],a[112:115],a[16:19]
zt a[188:189],v207 offset:57344
zm a[16:19],a[152:155],a[96:99],a[16:19]
zm a[16:19],a[148:151],a[80:83],a[16:19]
zt a[190:191],v207 offset:61440
zm a[16:19],a[144:147],a[64:67],a[16:19]
s_waitcnt lgkmcnt(14)
zm a[32:35],a[160:163],a[64:67],a[32:35]
s_waitcnt lgkmcnt(12)
zm a[32:35],a[164:167],a[80:83],a[32:35]
s_waitcnt lgkmcnt(10)
zm a[32:35],a[168:171],a[96:99],a[32:35]
s_waitcnt lgkmcnt(8)
s_add_u32 s72, s89, 0x5000
zm a[32:35],a[172:175],a[112:115],a[32:35]
zl v[48:51],v214,s[40:43],s72 offen offset:0 sc1
v_xor_b32 v200, 0x10000, v200
zm a[36:39],a[172:175],a[116:119],a[36:39]
v_xor_b32 v201, 0x10000, v201
zm a[36:39],a[168:171],a[100:103],a[36:39]
v_xor_b32 v202, 0x10000, v202
zm a[36:39],a[164:167],a[84:87],a[36:39]
v_xor_b32 v203, 0x10000, v203
zm a[36:39],a[160:163],a[68:71],a[36:39]
zl v[52:55],v214,s[40:43],s72 offen offset:1024 sc1
v_xor_b32 v204, 0x10000, v204
zm a[40:43],a[160:163],a[72:75],a[40:43]
v_xor_b32 v205, 0x10000, v205
zm a[40:43],a[164:167],a[88:91],a[40:43]
v_xor_b32 v206, 0x10000, v206
zm a[40:43],a[168:171],a[104:107],a[40:43]
v_xor_b32 v207, 0x10000, v207
zm a[40:43],a[172:175],a[120:123],a[40:43]
s_waitcnt vmcnt(54) lgkmcnt(0)
s_barrier
zl v[56:59],v214,s[40:43],s72 offen offset:2048 sc1
zt a[128:129],v200 offset:0
zt a[130:131],v200 offset:4096
zt a[132:133],v200 offset:8192
zt a[134:135],v200 offset:12288
zm a[56:59],a[188:191],a[120:123],a[56:59]
s_add_u32 s72, s89, 0x6000
zt a[136:137],v200 offset:16384
zm a[56:59],a[184:187],a[104:107],a[56:59]
zt a[138:139],v200 offset:20480
zl v[96:99],v214,s[40:43],s72 offen offset:0 sc1
zm a[56:59],a[180:183],a[88:91],a[56:59]
zm a[56:59],a[176:179],a[72:75],a[56:59]
zt a[140:141],v200 offset:24576
zm a[52:55],a[176:179],a[68:71],a[52:55]
zt a[142:143],v200 offset:28672
zm a[52:55],a[180:183],a[84:87],a[52:55]
zl v[100:103],v214,s[40:43],s72 offen offset:1024 sc1
zm a[52:55],a[184:187],a[100:103],a[52:55]
zt a[144:145],v201 offset:0
zm a[52:55],a[188:191],a[116:119],a[52:55]
zm a[48:51],a[188:191],a[112:115],a[48:51]
zt a[146:147],v201 offset:4096
zm a[48:51],a[184:187],a[96:99],a[48:51]
zt a[148:149],v201 offset:8192
zm a[48:51],a[180:183],a[80:83],a[48:51]
zm a[48:51],a[176:179],a[64:67],a[48:51]
s_waitcnt lgkmcnt(9)
s_waitcnt vmcnt(12)
zt a[150:151],v201 offset:12288
zm a[192:195],a[128:131],v[128:131],a[192:195]
s_waitcnt lgkmcnt(8)
s_add_u32 s50, s87, 0x20000
zt a[152:153],v201 offset:16384
zm a[192:195],a[132:135],v[144:147],a[192:195]
s_waitcnt lgkmcnt(7)
s_add_u32 m0, s45, 0x8000
zm a[192:195],a[136:139],v[160:163],a[192:195]
s_waitcnt lgkmcnt(5)
zt a[154:155],v201 offset:20480
zl v208,s[24:27],s50 offen offset:0 lds
zm a[192:195],a[140:143],v[176:179],a[192:195]
zt a[156:157],v201 offset:24576
zm a[196:199],a[140:143],v[180:183],a[196:199]
zl v209,s[24:27],s50 offen offset:1024 lds
zm a[196:199],a[136:139],v[164:167],a[196:199]
zt a[158:159],v201 offset:28672
zm a[196:199],a[132:135],v[148:151],a[196:199]
zm a[196:199],a[128:131],v[132:135],a[196:199]
zt a[160:161],v202 offset:0
zm a[200:203],a[128:131],v[136:139],a[200:203]
zt a[162:163],v202 offset:4096
zl v210,s[24:27],s50 offen offset:2048 lds
zm a[200:203],a[132:135],v[152:155],a[200:203]
zm a[200:203],a[136:139],v[168:171],a[200:203]
zt a[164:165],v202 offset:8192
zm a[200:203],a[140:143],v[184:187],a[200:203]
s_waitcnt lgkmcnt(3)
zt a[166:167],v202 offset:12288
zt a[168:169],v202 offset:16384
zl v211,s[24:27],s50 offen offset:3072 lds
zt a[170:171],v202 offset:20480
zt a[172:173],v202 offset:24576
zt a[174:175],v202 offset:28672
zl v[104:107],v214,s[40:43],s72 offen offset:2048 sc1
zt a[176:177],v203 offset:0
zm a[216:219],a[156:159],v[184:187],a[216:219]
zm a[216:219],a[152:155],v[168:171],a[216:219]
zt a[178:179],v203 offset:4096
zm a[216:219],a[148:151],v[152:155],a[216:219]
zt a[180:181],v203 offset:8192
zm a[216:219],a[144:147],v[136:139],a[216:219]
zm a[212:215],a[144:147],v[132:135],a[212:215]
zt a[182:183],v203 offset:12288
zm a[212:215],a[148:151],v[148:151],a[212:215]
s_add_u32 s72, s89, 0x7000
zt a[184:185],v203 offset:16384
zm a[212:215],a[152:155],v[164:167],a[212:215]
zl v[112:115],v214,s[40:43],s72 offen offset:0 sc1
zm a[212:215],a[156:159],v[180:183],a[212:215]
zt a[186:187],v203 offset:20480
zm a[208:211],a[156:159],v[176:179],a[208:211]
zt a[188:189],v203 offset:24576
zm a[208:211],a[152:155],v[160:163],a[208:211]
zm a[208:211],a[148:151],v[144:147],a[208:211]
zt a[190:191],v203 offset:28672
zm a[208:211],a[144:147],v[128:131],a[208:211]
s_waitcnt lgkmcnt(14)
zl v[116:119],v214,s[40:43],s72 offen offset:1024 sc1
zm a[224:227],a[160:163],v[128:131],a[224:227]
s_waitcnt lgkmcnt(12)
zm a[224:227],a[164:167],v[144:147],a[224:227]
s_waitcnt lgkmcnt(10)
zm a[224:227],a[168:171],v[160:163],a[224:227]
s_waitcnt lgkmcnt(8)
zm a[224:227],a[172:175],v[176:179],a[224:227]
zl v[120:123],v214,s[40:43],s72 offen offset:2048 sc1
zm a[228:231],a[172:175],v[180:183],a[228:231]
zm a[228:231],a[168:171],v[164:167],a[228:231]
zm a[228:231],a[164:167],v[148:151],a[228:231]
zm a[228:231],a[160:163],v[132:135],a[228:231]
zm a[232:235],a[160:163],v[136:139],a[232:235]
zm a[232:235],a[164:167],v[152:155],a[232:235]
zm a[232:235],a[168:171],v[168:171],a[232:235]
zm a[232:235],a[172:175],v[184:187],a[232:235]
s_waitcnt lgkmcnt(0)
s_add_u32 s72, s90, 0x4000
zl v[0:3],v214,s[40:43],s72 offen offset:0 sc1
zt a[128:129],v204 offset:0
zt a[130:131],v204 offset:4096
zl v[4:7],v214,s[40:43],s72 offen offset:1024 sc1
zt a[132:133],v204 offset:8192
zt a[134:135],v204 offset:12288
zm a[248:251],a[188:191],v[184:187],a[248:251]
zt a[136:137],v204 offset:16384
zm a[248:251],a[184:187],v[168:171],a[248:251]
zt a[138:139],v204 offset:20480
zl v[8:11],v214,s[40:43],s72 offen offset:2048 sc1
zm a[248:251],a[180:183],v[152:155],a[248:251]
zm a[248:251],a[176:179],v[136:139],a[248:251]
zt a[140:141],v204 offset:24576
zm a[244:247],a[176:179],v[132:135],a[244:247]
zt a[142:143],v204 offset:28672
zm a[244:247],a[180:183],v[148:151],a[244:247]
zm a[244:247],a[184:187],v[164:167],a[244:247]
zt a[144:145],v205 offset:0
zm a[244:247],a[188:191],v[180:183],a[244:247]
zm a[240:243],a[188:191],v[176:179],a[240:243]
zt a[146:147],v205 offset:4096
zm a[240:243],a[184:187],v[160:163],a[240:243]
zt a[148:149],v205 offset:8192
zm a[240:243],a[180:183],v[144:147],a[240:243]
zm a[240:243],a[176:179],v[128:131],a[240:243]
s_waitcnt lgkmcnt(9)
zt a[150:151],v205 offset:12288
zm a[0:3],a[128:131],v[128:131],a[0:3]
s_waitcnt lgkmcnt(8)
s_add_u32 s50, s87, 0x30000
zt a[152:153],v205 offset:16384
zm a[0:3],a[132:135],v[144:147],a[0:3]
s_waitcnt lgkmcnt(7)
s_add_u32 m0, s45, 0xc000
zm a[0:3],a[136:139],v[160:163],a[0:3]
s_waitcnt lgkmcnt(5)
zt a[154:155],v205 offset:20480
zl v208,s[24:27],s50 offen offset:0 lds
zm a[0:3],a[140:143],v[176:179],a[0:3]
zt a[156:157],v205 offset:24576
zl v209,s[24:27],s50 offen offset:1024 lds
zm a[4:7],a[140:143],v[180:183],a[4:7]
zm a[4:7],a[136:139],v[164:167],a[4:7]
zt a[158:159],v205 offset:28672
zm a[4:7],a[132:135],v[148:151],a[4:7]
zl v210,s[24:27],s50 offen offset:2048 lds
zm a[4:7],a[128:131],v[132:135],a[4:7]
zt a[160:161],v206 offset:0
zm a[8:11],a[128:131],v[136:139],a[8:11]
zt a[162:163],v206 offset:4096
zm a[8:11],a[132:135],v[152:155],a[8:11]
zm a[8:11],a[136:139],v[168:171],a[8:11]
zt a[164:165],v206 offset:8192
zl v211,s[24:27],s50 offen offset:3072 lds
zm a[8:11],a[140:143],v[184:187],a[8:11]
s_waitcnt lgkmcnt(3)
zt a[166:167],v206 offset:12288
s_add_u32 s72, s90, 0x5000
zt a[168:169],v206 offset:16384
zl v[16:19],v214,s[40:43],s72 offen offset:0 sc1
zt a[170:171],v206 offset:20480
zt a[172:173],v206 offset:24576
zt a[174:175],v206 offset:28672
zl v[20:23],v214,s[40:43],s72 offen offset:1024 sc1
zt a[176:177],v207 offset:0
zm a[24:27],a[156:159],v[184:187],a[24:27]
zl v[24:27],v214,s[40:43],s72 offen offset:2048 sc1
zm a[24:27],a[152:155],v[168:171],a[24:27]
zt a[178:179],v207 offset:4096
zm a[24:27],a[148:151],v[152:155],a[24:27]
zt a[180:181],v207 offset:8192
zm a[24:27],a[144:147],v[136:139],a[24:27]
zm a[20:23],a[144:147],v[132:135],a[20:23]
zt a[182:183],v207 offset:12288
zm a[20:23],a[148:151],v[148:151],a[20:23]
zt a[184:185],v207 offset:16384
zm a[20:23],a[152:155],v[164:167],a[20:23]
s_add_u32 s72, s90, 0x6000
zm a[20:23],a[156:159],v[180:183],a[20:23]
zt a[186:187],v207 offset:20480
zl v[64:67],v214,s[40:43],s72 offen offset:0 sc1
zm a[16:19],a[156:159],v[176:179],a[16:19]
zt a[188:189],v207 offset:24576
zm a[16:19],a[152:155],v[160:163],a[16:19]
zm a[16:19],a[148:151],v[144:147],a[16:19]
zt a[190:191],v207 offset:28672
zm a[16:19],a[144:147],v[128:131],a[16:19]
s_waitcnt lgkmcnt(14)
zl v[68:71],v214,s[40:43],s72 offen offset:1024 sc1
zm a[32:35],a[160:163],v[128:131],a[32:35]
s_waitcnt lgkmcnt(12)
zm a[32:35],a[164:167],v[144:147],a[32:35]
s_waitcnt lgkmcnt(10)
zm a[32:35],a[168:171],v[160:163],a[32:35]
s_waitcnt lgkmcnt(8)
zl v[72:75],v214,s[40:43],s72 offen offset:2048 sc1
zm a[32:35],a[172:175],v[176:179],a[32:35]
zm a[36:39],a[172:175],v[180:183],a[36:39]
zm a[36:39],a[168:171],v[164:167],a[36:39]
zm a[36:39],a[164:167],v[148:151],a[36:39]
zm a[36:39],a[160:163],v[132:135],a[36:39]
zm a[40:43],a[160:163],v[136:139],a[40:43]
s_add_u32 s72, s90, 0x7000
zm a[40:43],a[164:167],v[152:155],a[40:43]
zl v[80:83],v214,s[40:43],s72 offen offset:0 sc1
zm a[40:43],a[168:171],v[168:171],a[40:43]
zm a[40:43],a[172:175],v[184:187],a[40:43]
s_waitcnt vmcnt(54) lgkmcnt(0)
s_barrier
zl v[84:87],v214,s[40:43],s72 offen offset:1024 sc1
zt a[128:129],v200 offset:32768
zt a[130:131],v200 offset:36864
zl v[88:91],v214,s[40:43],s72 offen offset:2048 sc1
zt a[132:133],v200 offset:40960
zt a[134:135],v200 offset:45056
zm a[56:59],a[188:191],v[184:187],a[56:59]
zt a[136:137],v200 offset:49152
zm a[56:59],a[184:187],v[168:171],a[56:59]
zt a[138:139],v200 offset:53248
zm a[56:59],a[180:183],v[152:155],a[56:59]
s_add_u32 s72, s91, 0x4000
zm a[56:59],a[176:179],v[136:139],a[56:59]
zt a[140:141],v200 offset:57344
zl a[64:67],v214,s[40:43],s72 offen offset:0 sc1
zm a[52:55],a[176:179],v[132:135],a[52:55]
zt a[142:143],v200 offset:61440
zm a[52:55],a[180:183],v[148:151],a[52:55]
zm a[52:55],a[184:187],v[164:167],a[52:55]
zt a[144:145],v201 offset:32768
zl a[68:71],v214,s[40:43],s72 offen offset:1024 sc1
zm a[52:55],a[188:191],v[180:183],a[52:55]
zm a[48:51],a[188:191],v[176:179],a[48:51]
zt a[146:147],v201 offset:36864
zm a[48:51],a[184:187],v[160:163],a[48:51]
zt a[148:149],v201 offset:40960
zm a[48:51],a[180:183],v[144:147],a[48:51]
zm a[48:51],a[176:179],v[128:131],a[48:51]
s_waitcnt lgkmcnt(9)
s_waitcnt vmcnt(18)
zt a[150:151],v201 offset:45056
zm a[192:195],a[128:131],v[32:35],a[192:195]
s_waitcnt lgkmcnt(8)
zt a[152:153],v201 offset:49152
zl a[72:75],v214,s[40:43],s72 offen offset:2048 sc1
zm a[192:195],a[132:135],v[48:51],a[192:195]
s_waitcnt lgkmcnt(7)
zm a[192:195],a[136:139],v[96:99],a[192:195]
s_waitcnt lgkmcnt(5)
zt a[154:155],v201 offset:53248
zm a[192:195],a[140:143],v[112:115],a[192:195]
zt a[156:157],v201 offset:57344
zm a[196:199],a[140:143],v[116:119],a[196:199]
zm a[196:199],a[136:139],v[100:103],a[196:199]
zt a[158:159],v201 offset:61440
zm a[196:199],a[132:135],v[52:55],a[196:199]
zm a[196:199],a[128:131],v[36:39],a[196:199]
s_add_u32 s72, s91, 0x5000
zt a[160:161],v202 offset:32768
zm a[200:203],a[128:131],v[40:43],a[200:203]
zt a[162:163],v202 offset:36864
zl a[80:83],v214,s[40:43],s72 offen offset:0 sc1
zm a[200:203],a[132:135],v[56:59],a[200:203]
zm a[200:203],a[136:139],v[104:107],a[200:203]
zt a[164:165],v202 offset:40960
zm a[200:203],a[140:143],v[120:123],a[200:203]
s_waitcnt lgkmcnt(3)
zt a[166:167],v202 offset:45056
zt a[168:169],v202 offset:49152
zl a[84:87],v214,s[40:43],s72 offen offset:1024 sc1
zt a[170:171],v202 offset:53248
zt a[172:173],v202 offset:57344
zt a[174:175],v202 offset:61440
zl a[88:91],v214,s[40:43],s72 offen offset:2048 sc1
zt a[176:177],v203 offset:32768
zm a[216:219],a[156:159],v[120:123],a[216:219]
zm a[216:219],a[152:155],v[104:107],a[216:219]
zt a[178:179],v203 offset:36864
zm a[216:219],a[148:151],v[56:59],a[216:219]
zt a[180:181],v203 offset:40960
zm a[216:219],a[144:147],v[40:43],a[216:219]
zm a[212:215],a[144:147],v[36:39],a[212:215]
zt a[182:183],v203 offset:45056
zm a[212:215],a[148:151],v[52:55],a[212:215]
s_add_u32 s72, s91, 0x6000
zt a[184:185],v203 offset:49152
zm a[212:215],a[152:155],v[100:103],a[212:215]
zl a[96:99],v214,s[40:43],s72 offen offset:0 sc1
zm a[212:215],a[156:159],v[116:119],a[212:215]
zt a[186:187],v203 offset:53248
zm a[208:211],a[156:159],v[112:115],a[208:211]
zt a[188:189],v203 offset:57344
zm a[208:211],a[152:155],v[96:99],a[208:211]
zm a[208:211],a[148:151],v[48:51],a[208:211]
zt a[190:191],v203 offset:61440
zm a[208:211],a[144:147],v[32:35],a[208:211]
s_waitcnt lgkmcnt(14)
zl a[100:103],v214,s[40:43],s72 offen offset:1024 sc1
zm a[224:227],a[160:163],v[32:35],a[224:227]
s_waitcnt lgkmcnt(12)
zm a[224:227],a[164:167],v[48:51],a[224:227]
s_waitcnt lgkmcnt(10)
zm a[224:227],a[168:171],v[96:99],a[224:227]
s_waitcnt lgkmcnt(8)
zm a[224:227],a[172:175],v[112:115],a[224:227]
zl a[104:107],v214,s[40:43],s72 offen offset:2048 sc1
zm a[228:231],a[172:175],v[116:119],a[228:231]
zm a[228:231],a[168:171],v[100:103],a[228:231]
zm a[228:231],a[164:167],v[52:55],a[228:231]
zm a[228:231],a[160:163],v[36:39],a[228:231]
zm a[232:235],a[160:163],v[40:43],a[232:235]
zm a[232:235],a[164:167],v[56:59],a[232:235]
zm a[232:235],a[168:171],v[104:107],a[232:235]
zm a[232:235],a[172:175],v[120:123],a[232:235]
s_waitcnt lgkmcnt(0)
s_add_u32 s72, s91, 0x7000
zl a[112:115],v214,s[40:43],s72 offen offset:0 sc1
zt a[128:129],v204 offset:32768
zt a[130:131],v204 offset:36864
zl a[116:119],v214,s[40:43],s72 offen offset:1024 sc1
zt a[132:133],v204 offset:40960
zt a[134:135],v204 offset:45056
zm a[248:251],a[188:191],v[120:123],a[248:251]
zt a[136:137],v204 offset:49152
zm a[248:251],a[184:187],v[104:107],a[248:251]
zt a[138:139],v204 offset:53248
zl a[120:123],v214,s[40:43],s72 offen offset:2048 sc1
zm a[248:251],a[180:183],v[56:59],a[248:251]
zm a[248:251],a[176:179],v[40:43],a[248:251]
zt a[140:141],v204 offset:57344
zm a[244:247],a[176:179],v[36:39],a[244:247]
zt a[142:143],v204 offset:61440
zm a[244:247],a[180:183],v[52:55],a[244:247]
zm a[244:247],a[184:187],v[100:103],a[244:247]
zt a[144:145],v205 offset:32768
zm a[244:247],a[188:191],v[116:119],a[244:247]
zm a[240:243],a[188:191],v[112:115],a[240:243]
zt a[146:147],v205 offset:36864
zm a[240:243],a[184:187],v[96:99],a[240:243]
zt a[148:149],v205 offset:40960
zm a[240:243],a[180:183],v[48:51],a[240:243]
zm a[240:243],a[176:179],v[32:35],a[240:243]
s_waitcnt lgkmcnt(9)
zt a[150:151],v205 offset:45056
zm a[0:3],a[128:131],v[32:35],a[0:3]
s_waitcnt lgkmcnt(8)
zt a[152:153],v205 offset:49152
zm a[0:3],a[132:135],v[48:51],a[0:3]
s_waitcnt lgkmcnt(7)
zm a[0:3],a[136:139],v[96:99],a[0:3]
s_waitcnt lgkmcnt(5)
zt a[154:155],v205 offset:53248
zm a[0:3],a[140:143],v[112:115],a[0:3]
zt a[156:157],v205 offset:57344
zm a[4:7],a[140:143],v[116:119],a[4:7]
zm a[4:7],a[136:139],v[100:103],a[4:7]
zt a[158:159],v205 offset:61440
zm a[4:7],a[132:135],v[52:55],a[4:7]
v_mov_b32 v196, v206
zm a[4:7],a[128:131],v[36:39],a[4:7]
v_mov_b32 v197, v207
zm a[8:11],a[128:131],v[40:43],a[8:11]
v_xor_b32 v200, 0x10000, v200
zm a[8:11],a[132:135],v[56:59],a[8:11]
v_xor_b32 v201, 0x10000, v201
zm a[8:11],a[136:139],v[104:107],a[8:11]
v_xor_b32 v202, 0x10000, v202
zm a[8:11],a[140:143],v[120:123],a[8:11]
v_xor_b32 v203, 0x10000, v203
v_xor_b32 v204, 0x10000, v204
v_xor_b32 v205, 0x10000, v205
v_xor_b32 v206, 0x10000, v206
v_xor_b32 v207, 0x10000, v207
s_waitcnt vmcnt(21) lgkmcnt(0)
s_barrier
zt a[160:161],v200 offset:0
)"
R"(zt a[162:163],v200 offset:4096
zt a[164:165],v200 offset:8192
zm a[24:27],a[156:159],v[120:123],a[24:27]
zt a[166:167],v200 offset:12288
zm a[24:27],a[152:155],v[104:107],a[24:27]
zt a[168:169],v200 offset:16384
zm a[24:27],a[148:151],v[56:59],a[24:27]
zt a[170:171],v200 offset:20480
zt a[172:173],v200 offset:24576
zm a[24:27],a[144:147],v[40:43],a[24:27]
zt a[174:175],v200 offset:28672
zm a[20:23],a[144:147],v[36:39],a[20:23]
zt a[176:177],v200 offset:32768
zm a[20:23],a[148:151],v[52:55],a[20:23]
zt a[178:179],v200 offset:36864
zm a[20:23],a[152:155],v[100:103],a[20:23]
zt a[180:181],v200 offset:40960
zt a[182:183],v200 offset:45056
zm a[20:23],a[156:159],v[116:119],a[20:23]
zt a[184:185],v200 offset:49152
zm a[16:19],a[156:159],v[112:115],a[16:19]
zt a[186:187],v200 offset:53248
zm a[16:19],a[152:155],v[96:99],a[16:19]
zt a[188:189],v200 offset:57344
zm a[16:19],a[148:151],v[48:51],a[16:19]
zt a[190:191],v200 offset:61440
zm a[16:19],a[144:147],v[32:35],a[16:19]
s_waitcnt lgkmcnt(14)
s_waitcnt vmcnt(12)
zm a[192:195],a[160:163],v[0:3],a[192:195]
s_waitcnt lgkmcnt(12)
zt a[128:129],v201 offset:0
zm a[192:195],a[164:167],v[16:19],a[192:195]
s_waitcnt lgkmcnt(11)
zt a[130:131],v201 offset:4096
zm a[192:195],a[168:171],v[64:67],a[192:195]
s_waitcnt lgkmcnt(10)
s_waitcnt vmcnt(0)
zm a[192:195],a[172:175],v[80:83],a[192:195]
s_waitcnt lgkmcnt(8)
zt a[132:133],v201 offset:8192
zm a[192:195],a[176:179],a[64:67],a[192:195]
s_waitcnt lgkmcnt(7)
zt a[134:135],v201 offset:12288
v_lshrrev_b32 v236, 4, v214
zm a[192:195],a[180:183],a[80:83],a[192:195]
s_waitcnt lgkmcnt(6)
v_min_u32 v236, 2, v236
zm a[192:195],a[184:187],a[96:99],a[192:195]
s_waitcnt lgkmcnt(4)
zt a[136:137],v201 offset:16384
zm a[192:195],a[188:191],a[112:115],a[192:195]
s_add_u32 s74, s77, 0x1000
zt a[138:139],v201 offset:20480
zm a[196:199],a[188:191],a[116:119],a[196:199]
v_lshlrev_b32 v236, 10, v236
zm a[196:199],a[184:187],a[100:103],a[196:199]
zt a[140:141],v201 offset:24576
zm a[196:199],a[180:183],a[84:87],a[196:199]
zt a[142:143],v201 offset:28672
v_add_u32 v215, s74, v236
zm a[196:199],a[176:179],a[68:71],a[196:199]
s_mov_b32 s51, 7
zm a[196:199],a[172:175],v[84:87],a[196:199]
zt a[144:145],v201 offset:32768
zm a[196:199],a[168:171],v[68:71],a[196:199]
zt a[146:147],v201 offset:36864
v_mov_b32 v242, 0xffff0000
zm a[196:199],a[164:167],v[20:23],a[196:199]
v_bfe_u32 v237, v214, 4, 4
zm a[196:199],a[160:163],v[4:7],a[196:199]
zt a[148:149],v201 offset:40960
zm a[200:203],a[160:163],v[8:11],a[200:203]
zt a[150:151],v201 offset:45056
v_lshlrev_b32 v237, 11, v237
zm a[200:203],a[164:167],v[24:27],a[200:203]
s_lshl_b32 s74, s64, 17
zm a[200:203],a[168:171],v[72:75],a[200:203]
zt a[152:153],v201 offset:49152
zm a[200:203],a[172:175],v[88:91],a[200:203]
zt a[154:155],v201 offset:53248
v_add_u32 v237, s74, v237
zm a[200:203],a[176:179],a[72:75],a[200:203]
v_bfe_u32 v238, v214, 8, 1
zm a[200:203],a[180:183],a[88:91],a[200:203]
zt a[156:157],v201 offset:57344
zm a[200:203],a[184:187],a[104:107],a[200:203]
zt a[158:159],v201 offset:61440
v_lshl_add_u32 v237, v238, 5, v237
zm a[200:203],a[188:191],a[120:123],a[200:203]
s_waitcnt lgkmcnt(14)
v_lshrrev_b32 v238, 9, v214
v_lshl_add_u32 v237, v238, 4, v237
v_add_u32 v238, 0x8000, v237
v_add_u32 v239, 0x10000, v237
v_add_u32 v240, 0x18000, v237
zm a[208:211],a[128:131],v[0:3],a[208:211]
s_waitcnt lgkmcnt(12)
s_add_u32 s54, s75, 1
zt a[160:161],v202 offset:0
zm a[208:211],a[132:135],v[16:19],a[208:211]
s_waitcnt lgkmcnt(11)
s_and_b32 s54, s54, 3
zt a[162:163],v202 offset:4096
zm a[208:211],a[136:139],v[64:67],a[208:211]
s_waitcnt lgkmcnt(10)
s_lshl_b32 s54, s54, 2
zr v128,a192
zr v129,a193
zm a[208:211],a[140:143],v[80:83],a[208:211]
s_waitcnt lgkmcnt(8)
s_add_u32 s54, s54, s75
zt a[164:165],v202 offset:8192
zr v130,a194
zm a[208:211],a[144:147],a[64:67],a[208:211]
s_waitcnt lgkmcnt(7)
s_lshl_b32 s72, s54, 15
zt a[166:167],v202 offset:12288
zr v131,a195
zm a[208:211],a[148:151],a[80:83],a[208:211]
s_waitcnt lgkmcnt(6)
zr v132,a196
zr v133,a197
zm a[208:211],a[152:155],a[96:99],a[208:211]
s_waitcnt lgkmcnt(4)
s_add_u32 s74, s75, 3
zt a[168:169],v202 offset:16384
zr v134,a198
zm a[208:211],a[156:159],a[112:115],a[208:211]
s_and_b32 s74, s74, 3
zt a[170:171],v202 offset:20480
zr v135,a199
zm a[212:215],a[156:159],a[116:119],a[212:215]
s_lshl_b32 s84, s75, 2
zc v144,v128,v129
zc v145,v130,v131
zm a[212:215],a[152:155],a[100:103],a[212:215]
s_add_u32 s84, s84, s74
zt a[172:173],v202 offset:24576
zc v146,v132,v133
zm a[212:215],a[148:151],a[84:87],a[212:215]
s_lshl_b32 s84, s84, 15
zt a[174:175],v202 offset:28672
zc v147,v134,v135
zm a[212:215],a[144:147],a[68:71],a[212:215]
s_add_u32 s88, s77, 0x100
buffer_store_dwordx4 v[144:147], v214, s[96:99], s72 offen offset:0 sc1
zr v136,a200
zm a[212:215],a[140:143],v[84:87],a[212:215]
s_and_b32 s88, s88, 0x3ff
zt a[176:177],v202 offset:32768
zr v137,a201
zm a[212:215],a[136:139],v[68:71],a[212:215]
s_add_u32 s88, s88, 0x1000
zt a[178:179],v202 offset:36864
zr v138,a202
zm a[212:215],a[132:135],v[20:23],a[212:215]
s_add_u32 s74, s75, 2
zr v139,a203
zr v140,a204
zm a[212:215],a[128:131],v[4:7],a[212:215]
s_and_b32 s74, s74, 3
zt a[180:181],v202 offset:40960
zr v141,a205
zm a[216:219],a[128:131],v[8:11],a[216:219]
s_lshl_b32 s85, s75, 2
zt a[182:183],v202 offset:45056
zr v142,a206
zm a[216:219],a[132:135],v[24:27],a[216:219]
s_add_u32 s85, s85, s74
zr v143,a207
zc v148,v136,v137
zm a[216:219],a[136:139],v[72:75],a[216:219]
s_lshl_b32 s85, s85, 15
zt a[184:185],v202 offset:49152
zc v149,v138,v139
zm a[216:219],a[140:143],v[88:91],a[216:219]
s_add_u32 s89, s77, 0x200
zt a[186:187],v202 offset:53248
zc v150,v140,v141
zm a[216:219],a[144:147],a[72:75],a[216:219]
s_and_b32 s89, s89, 0x3ff
zc v151,v142,v143
zm a[216:219],a[148:151],a[88:91],a[216:219]
s_add_u32 s89, s89, 0x1400
zt a[188:189],v202 offset:57344
buffer_store_dwordx4 v[148:151], v214, s[96:99], s72 offen offset:1024 sc1
zm a[216:219],a[152:155],a[104:107],a[216:219]
s_add_u32 s74, s75, 1
zt a[190:191],v202 offset:61440
zm a[216:219],a[156:159],a[120:123],a[216:219]
s_waitcnt lgkmcnt(14)
s_and_b32 s74, s74, 3
s_lshl_b32 s86, s75, 2
s_add_u32 s86, s86, s74
s_lshl_b32 s86, s86, 15
s_add_u32 s90, s77, 0x300
s_and_b32 s90, s90, 0x3ff
s_add_u32 s90, s90, 0x1800
zm a[224:227],a[160:163],v[0:3],a[224:227]
s_waitcnt lgkmcnt(12)
zt a[128:129],v203 offset:0
zm a[224:227],a[164:167],v[16:19],a[224:227]
s_waitcnt lgkmcnt(11)
zt a[130:131],v203 offset:4096
zm a[224:227],a[168:171],v[64:67],a[224:227]
s_waitcnt lgkmcnt(10)
zr v128,a208
zr v129,a209
zm a[224:227],a[172:175],v[80:83],a[224:227]
s_waitcnt lgkmcnt(8)
zt a[132:133],v203 offset:8192
zr v130,a210
zm a[224:227],a[176:179],a[64:67],a[224:227]
s_waitcnt lgkmcnt(7)
zt a[134:135],v203 offset:12288
zr v131,a211
zm a[224:227],a[180:183],a[80:83],a[224:227]
s_waitcnt lgkmcnt(6)
zr v132,a212
zr v133,a213
zm a[224:227],a[184:187],a[96:99],a[224:227]
s_waitcnt lgkmcnt(4)
zt a[136:137],v203 offset:16384
zr v134,a214
zm a[224:227],a[188:191],a[112:115],a[224:227]
zt a[138:139],v203 offset:20480
zr v135,a215
zm a[228:231],a[188:191],a[116:119],a[228:231]
zc v152,v128,v129
zc v153,v130,v131
zm a[228:231],a[184:187],a[100:103],a[228:231]
zt a[140:141],v203 offset:24576
zc v154,v132,v133
zm a[228:231],a[180:183],a[84:87],a[228:231]
zt a[142:143],v203 offset:28672
zc v155,v134,v135
zm a[228:231],a[176:179],a[68:71],a[228:231]
buffer_store_dwordx4 v[152:155], v214, s[96:99], s72 offen offset:2048 sc1
zr v136,a216
zm a[228:231],a[172:175],v[84:87],a[228:231]
zt a[144:145],v203 offset:32768
zr v137,a217
zm a[228:231],a[168:171],v[68:71],a[228:231]
zt a[146:147],v203 offset:36864
zr v138,a218
zm a[228:231],a[164:167],v[20:23],a[228:231]
zr v139,a219
zr v140,a220
zm a[228:231],a[160:163],v[4:7],a[228:231]
zt a[148:149],v203 offset:40960
zr v141,a221
zm a[232:235],a[160:163],v[8:11],a[232:235]
zt a[150:151],v203 offset:45056
zr v142,a222
zm a[232:235],a[164:167],v[24:27],a[232:235]
zr v143,a223
zc v156,v136,v137
zm a[232:235],a[168:171],v[72:75],a[232:235]
zt a[152:153],v203 offset:49152
zc v157,v138,v139
zm a[232:235],a[172:175],v[88:91],a[232:235]
zt a[154:155],v203 offset:53248
zc v158,v140,v141
zm a[232:235],a[176:179],a[72:75],a[232:235]
zc v159,v142,v143
zm a[232:235],a[180:183],a[88:91],a[232:235]
zt a[156:157],v203 offset:57344
buffer_store_dwordx4 v[156:159], v214, s[96:99], s72 offen offset:3072 sc1
zm a[232:235],a[184:187],a[104:107],a[232:235]
zt a[158:159],v203 offset:61440
zm a[232:235],a[188:191],a[120:123],a[232:235]
s_waitcnt lgkmcnt(14)
zm a[240:243],a[128:131],v[0:3],a[240:243]
s_waitcnt lgkmcnt(12)
s_add_u32 s54, s75, 2
zt a[160:161],v204 offset:0
zm a[240:243],a[132:135],v[16:19],a[240:243]
s_waitcnt lgkmcnt(11)
s_and_b32 s54, s54, 3
zt a[162:163],v204 offset:4096
zm a[240:243],a[136:139],v[64:67],a[240:243]
s_waitcnt lgkmcnt(10)
s_lshl_b32 s54, s54, 2
zr v128,a224
zr v129,a225
zm a[240:243],a[140:143],v[80:83],a[240:243]
s_waitcnt lgkmcnt(8)
s_add_u32 s54, s54, s75
zt a[164:165],v204 offset:8192
zr v130,a226
zm a[240:243],a[144:147],a[64:67],a[240:243]
s_waitcnt lgkmcnt(7)
s_lshl_b32 s72, s54, 15
zt a[166:167],v204 offset:12288
zr v131,a227
zm a[240:243],a[148:151],a[80:83],a[240:243]
s_waitcnt lgkmcnt(6)
zr v132,a228
zr v133,a229
zm a[240:243],a[152:155],a[96:99],a[240:243]
s_waitcnt lgkmcnt(4)
zt a[168:169],v204 offset:16384
zr v134,a230
zm a[240:243],a[156:159],a[112:115],a[240:243]
zt a[170:171],v204 offset:20480
zr v135,a231
zm a[244:247],a[156:159],a[116:119],a[244:247]
zc v144,v128,v129
zc v145,v130,v131
zm a[244:247],a[152:155],a[100:103],a[244:247]
zt a[172:173],v204 offset:24576
zc v146,v132,v133
zm a[244:247],a[148:151],a[84:87],a[244:247]
zt a[174:175],v204 offset:28672
zc v147,v134,v135
zm a[244:247],a[144:147],a[68:71],a[244:247]
buffer_store_dwordx4 v[144:147], v214, s[96:99], s72 offen offset:0 sc1
zr v136,a232
zm a[244:247],a[140:143],v[84:87],a[244:247]
zt a[176:177],v204 offset:32768
zr v137,a233
zm a[244:247],a[136:139],v[68:71],a[244:247]
zt a[178:179],v204 offset:36864
zr v138,a234
zm a[244:247],a[132:135],v[20:23],a[244:247]
zr v139,a235
zr v140,a236
zm a[244:247],a[128:131],v[4:7],a[244:247]
zt a[180:181],v204 offset:40960
zr v141,a237
zm a[248:251],a[128:131],v[8:11],a[248:251]
zt a[182:183],v204 offset:45056
zr v142,a238
zm a[248:251],a[132:135],v[24:27],a[248:251]
zr v143,a239
zc v148,v136,v137
zm a[248:251],a[136:139],v[72:75],a[248:251]
zt a[184:185],v204 offset:49152
zc v149,v138,v139
zm a[248:251],a[140:143],v[88:91],a[248:251]
zt a[186:187],v204 offset:53248
zc v150,v140,v141
zm a[248:251],a[144:147],a[72:75],a[248:251]
zc v151,v142,v143
zm a[248:251],a[148:151],a[88:91],a[248:251]
zt a[188:189],v204 offset:57344
buffer_store_dwordx4 v[148:151], v214, s[96:99], s72 offen offset:1024 sc1
zm a[248:251],a[152:155],a[104:107],a[248:251]
zt a[190:191],v204 offset:61440
zm a[248:251],a[156:159],a[120:123],a[248:251]
s_waitcnt lgkmcnt(14)
zm a[0:3],a[160:163],v[0:3],a[0:3]
s_waitcnt lgkmcnt(12)
zt a[128:129],v205 offset:0
zm a[0:3],a[164:167],v[16:19],a[0:3]
s_waitcnt lgkmcnt(11)
zt a[130:131],v205 offset:4096
zm a[0:3],a[168:171],v[64:67],a[0:3]
s_waitcnt lgkmcnt(10)
zr v128,a240
zr v129,a241
zm a[0:3],a[172:175],v[80:83],a[0:3]
s_waitcnt lgkmcnt(8)
zt a[132:133],v205 offset:8192
zr v130,a242
zm a[0:3],a[176:179],a[64:67],a[0:3]
s_waitcnt lgkmcnt(7)
zt a[134:135],v205 offset:12288
zr v131,a243
zm a[0:3],a[180:183],a[80:83],a[0:3]
s_waitcnt lgkmcnt(6)
zr v132,a244
zr v133,a245
zm a[0:3],a[184:187],a[96:99],a[0:3]
s_waitcnt lgkmcnt(4)
zt a[136:137],v205 offset:16384
zr v134,a246
zm a[0:3],a[188:191],a[112:115],a[0:3]
zt a[138:139],v205 offset:20480
zr v135,a247
zm a[4:7],a[188:191],a[116:119],a[4:7]
zc v152,v128,v129
zc v153,v130,v131
zm a[4:7],a[184:187],a[100:103],a[4:7]
zt a[140:141],v205 offset:24576
zc v154,v132,v133
zm a[4:7],a[180:183],a[84:87],a[4:7]
zt a[142:143],v205 offset:28672
zc v155,v134,v135
zm a[4:7],a[176:179],a[68:71],a[4:7]
buffer_store_dwordx4 v[152:155], v214, s[96:99], s72 offen offset:2048 sc1
zr v136,a248
zm a[4:7],a[172:175],v[84:87],a[4:7]
zt a[144:145],v205 offset:32768
zr v137,a249
zm a[4:7],a[168:171],v[68:71],a[4:7]
zt a[146:147],v205 offset:36864
zr v138,a250
zm a[4:7],a[164:167],v[20:23],a[4:7]
zr v139,a251
zr v140,a252
zm a[4:7],a[160:163],v[4:7],a[4:7]
zt a[148:149],v205 offset:40960
zr v141,a253
zm a[8:11],a[160:163],v[8:11],a[8:11]
zt a[150:151],v205 offset:45056
zr v142,a254
zm a[8:11],a[164:167],v[24:27],a[8:11]
zr v143,a255
zc v156,v136,v137
zm a[8:11],a[168:171],v[72:75],a[8:11]
zt a[152:153],v205 offset:49152
zc v157,v138,v139
zm a[8:11],a[172:175],v[88:91],a[8:11]
zt a[154:155],v205 offset:53248
zc v158,v140,v141
zm a[8:11],a[176:179],a[72:75],a[8:11]
zc v159,v142,v143
zm a[8:11],a[180:183],a[88:91],a[8:11]
zt a[156:157],v205 offset:57344
buffer_store_dwordx4 v[156:159], v214, s[96:99], s72 offen offset:3072 sc1
zm a[8:11],a[184:187],a[104:107],a[8:11]
zt a[158:159],v205 offset:61440
zm a[8:11],a[188:191],a[120:123],a[8:11]
s_waitcnt lgkmcnt(14)
s_waitcnt vmcnt(4)
s_mov_b64 exec, 1
buffer_store_dwordx2 v[216:217], off, s[92:95], s88 sc1
s_mov_b64 exec, -1
zm a[16:19],a[128:131],v[0:3],a[16:19]
s_waitcnt lgkmcnt(12)
s_add_u32 s54, s75, 3
zt a[160:161],v206 offset:0
zm a[16:19],a[132:135],v[16:19],a[16:19]
s_waitcnt lgkmcnt(11)
s_and_b32 s54, s54, 3
zt a[162:163],v206 offset:4096
zm a[16:19],a[136:139],v[64:67],a[16:19]
s_waitcnt lgkmcnt(10)
s_lshl_b32 s54, s54, 2
zr v128,a0
zr v129,a1
zm a[16:19],a[140:143],v[80:83],a[16:19]
s_waitcnt lgkmcnt(8)
s_add_u32 s54, s54, s75
zt a[164:165],v206 offset:8192
zr v130,a2
zm a[16:19],a[144:147],a[64:67],a[16:19]
s_waitcnt lgkmcnt(7)
s_lshl_b32 s72, s54, 15
zt a[166:167],v206 offset:12288
zr v131,a3
zm a[16:19],a[148:151],a[80:83],a[16:19]
s_waitcnt lgkmcnt(6)
zr v132,a4
zr v133,a5
zm a[16:19],a[152:155],a[96:99],a[16:19]
s_waitcnt lgkmcnt(4)
zt a[168:169],v206 offset:16384
zr v134,a6
zm a[16:19],a[156:159],a[112:115],a[16:19]
zt a[170:171],v206 offset:20480
zr v135,a7
zm a[20:23],a[156:159],a[116:119],a[20:23]
zc v144,v128,v129
zc v145,v130,v131
zm a[20:23],a[152:155],a[100:103],a[20:23]
zt a[172:173],v206 offset:24576
zc v146,v132,v133
zm a[20:23],a[148:151],a[84:87],a[20:23]
zt a[174:175],v206 offset:28672
zc v147,v134,v135
zm a[20:23],a[144:147],a[68:71],a[20:23]
buffer_store_dwordx4 v[144:147], v214, s[96:99], s72 offen offset:0 sc1
zr v136,a8
zm a[20:23],a[140:143],v[84:87],a[20:23]
zt a[176:177],v206 offset:32768
zr v137,a9
zm a[20:23],a[136:139],v[68:71],a[20:23]
zt a[178:179],v206 offset:36864
zr v138,a10
zm a[20:23],a[132:135],v[20:23],a[20:23]
zr v139,a11
zr v140,a12
zm a[20:23],a[128:131],v[4:7],a[20:23]
zt a[180:181],v206 offset:40960
zr v141,a13
zm a[24:27],a[128:131],v[8:11],a[24:27]
zt a[182:183],v206 offset:45056
zr v142,a14
zm a[24:27],a[132:135],v[24:27],a[24:27]
zr v143,a15
zc v148,v136,v137
zm a[24:27],a[136:139],v[72:75],a[24:27]
zt a[184:185],v206 offset:49152
zc v149,v138,v139
zm a[24:27],a[140:143],v[88:91],a[24:27]
zt a[186:187],v206 offset:53248
zc v150,v140,v141
zm a[24:27],a[144:147],a[72:75],a[24:27]
zc v151,v142,v143
zm a[24:27],a[148:151],a[88:91],a[24:27]
zt a[188:189],v206 offset:57344
buffer_store_dwordx4 v[148:151], v214, s[96:99], s72 offen offset:1024 sc1
zm a[24:27],a[152:155],a[104:107],a[24:27]
zt a[190:191],v206 offset:61440
zm a[24:27],a[156:159],a[120:123],a[24:27]
s_waitcnt lgkmcnt(14)
zr v128,a16
zr v129,a17
zr v130,a18
zr v131,a19
zr v132,a20
zr v133,a21
zr v134,a22
zr v135,a23
zc v152,v128,v129
zc v153,v130,v131
zc v154,v132,v133
zc v155,v134,v135
buffer_store_dwordx4 v[152:155], v214, s[96:99], s72 offen offset:2048 sc1
zm a[32:35],a[160:163],v[0:3],a[32:35]
s_waitcnt lgkmcnt(12)
zm a[32:35],a[164:167],v[16:19],a[32:35]
s_waitcnt lgkmcnt(10)
zm a[32:35],a[168:171],v[64:67],a[32:35]
s_waitcnt lgkmcnt(8)
zr v136,a24
zr v137,a25
zm a[32:35],a[172:175],v[80:83],a[32:35]
s_waitcnt lgkmcnt(6)
zr v138,a26
zr v139,a27
zm a[32:35],a[176:179],a[64:67],a[32:35]
s_waitcnt lgkmcnt(4)
zr v140,a28
zr v141,a29
zm a[32:35],a[180:183],a[80:83],a[32:35]
s_waitcnt lgkmcnt(2)
zr v142,a30
zr v143,a31
zm a[32:35],a[184:187],a[96:99],a[32:35]
s_waitcnt lgkmcnt(0)
zc v156,v136,v137
zc v157,v138,v139
zm a[32:35],a[188:191],a[112:115],a[32:35]
zc v158,v140,v141
zc v159,v142,v143
zm a[36:39],a[188:191],a[116:119],a[36:39]
buffer_store_dwordx4 v[156:159], v214, s[96:99], s72 offen offset:3072 sc1
zm a[36:39],a[184:187],a[100:103],a[36:39]
zm a[36:39],a[180:183],a[84:87],a[36:39]
zt a[128:129],v207 offset:0
zm a[36:39],a[176:179],a[68:71],a[36:39]
zt a[130:131],v207 offset:4096
zm a[36:39],a[172:175],v[84:87],a[36:39]
zt a[132:133],v207 offset:8192
buffer_load_dwordx2 v[244:245], v215, s[92:95], 0 offen offset:0 sc1
zm a[36:39],a[168:171],v[68:71],a[36:39]
zt a[134:135],v207 offset:12288
zm a[36:39],a[164:167],v[20:23],a[36:39]
zt a[136:137],v207 offset:16384
zm a[36:39],a[160:163],v[4:7],a[36:39]
zt a[138:139],v207 offset:20480
zm a[40:43],a[160:163],v[8:11],a[40:43]
zt a[140:141],v207 offset:24576
zm a[40:43],a[164:167],v[24:27],a[40:43]
zt a[142:143],v207 offset:28672
zm a[40:43],a[168:171],v[72:75],a[40:43]
zt a[144:145],v207 offset:32768
zm a[40:43],a[172:175],v[88:91],a[40:43]
zt a[146:147],v207 offset:36864
zm a[40:43],a[176:179],a[72:75],a[40:43]
zt a[148:149],v207 offset:40960
zm a[40:43],a[180:183],a[88:91],a[40:43]
zt a[150:151],v207 offset:45056
zm a[40:43],a[184:187],a[104:107],a[40:43]
zt a[152:153],v207 offset:49152
zm a[40:43],a[188:191],a[120:123],a[40:43]
s_waitcnt lgkmcnt(11)
zt a[154:155],v207 offset:53248
s_waitcnt vmcnt(6)
s_mov_b64 exec, 1
buffer_store_dwordx2 v[216:217], off, s[92:95], s89 sc1
s_mov_b64 exec, -1
zt a[156:157],v207 offset:57344
zt a[158:159],v207 offset:61440
zm a[48:51],a[128:131],v[0:3],a[48:51]
s_waitcnt lgkmcnt(12)
zt a[160:161],v196 offset:32768
zm a[48:51],a[132:135],v[16:19],a[48:51]
s_waitcnt lgkmcnt(11)
zt a[162:163],v196 offset:36864
zm a[48:51],a[136:139],v[64:67],a[48:51]
s_waitcnt lgkmcnt(10)
zm a[48:51],a[140:143],v[80:83],a[48:51]
s_waitcnt lgkmcnt(8)
zt a[164:165],v196 offset:40960
zm a[48:51],a[144:147],a[64:67],a[48:51]
s_waitcnt lgkmcnt(7)
zt a[166:167],v196 offset:45056
zm a[48:51],a[148:151],a[80:83],a[48:51]
s_waitcnt lgkmcnt(6)
zm a[48:51],a[152:155],a[96:99],a[48:51]
s_waitcnt lgkmcnt(4)
zt a[168:169],v196 offset:49152
zm a[48:51],a[156:159],a[112:115],a[48:51]
zt a[170:171],v196 offset:53248
zm a[52:55],a[156:159],a[116:119],a[52:55]
zm a[52:55],a[152:155],a[100:103],a[52:55]
zt a[172:173],v196 offset:57344
zm a[52:55],a[148:151],a[84:87],a[52:55]
zt a[174:175],v196 offset:61440
zm a[52:55],a[144:147],a[68:71],a[52:55]
zm a[52:55],a[140:143],v[84:87],a[52:55]
zt a[176:177],v197 offset:32768
zm a[52:55],a[136:139],v[68:71],a[52:55]
zt a[178:179],v197 offset:36864
zm a[52:55],a[132:135],v[20:23],a[52:55]
zm a[52:55],a[128:131],v[4:7],a[52:55]
zt a[180:181],v197 offset:40960
zm a[56:59],a[128:131],v[8:11],a[56:59]
zt a[182:183],v197 offset:45056
zm a[56:59],a[132:135],v[24:27],a[56:59]
zm a[56:59],a[136:139],v[72:75],a[56:59]
zt a[184:185],v197 offset:49152
zm a[56:59],a[140:143],v[88:91],a[56:59]
zt a[186:187],v197 offset:53248
zm a[56:59],a[144:147],a[72:75],a[56:59]
zm a[56:59],a[148:151],a[88:91],a[56:59]
zt a[188:189],v197 offset:57344
zm a[56:59],a[152:155],a[104:107],a[56:59]
zt a[190:191],v197 offset:61440
zm a[56:59],a[156:159],a[120:123],a[56:59]
s_waitcnt lgkmcnt(14)
zm a[32:35],a[160:163],v[32:35],a[32:35]
s_waitcnt lgkmcnt(12)
zm a[32:35],a[164:167],v[48:51],a[32:35]
s_waitcnt lgkmcnt(10)
zm a[32:35],a[168:171],v[96:99],a[32:35]
s_waitcnt lgkmcnt(8)
zm a[32:35],a[172:175],v[112:115],a[32:35]
zm a[36:39],a[172:175],v[116:119],a[36:39]
zm a[36:39],a[168:171],v[100:103],a[36:39]
zm a[36:39],a[164:167],v[52:55],a[36:39]
zm a[36:39],a[160:163],v[36:39],a[36:39]
zm a[40:43],a[160:163],v[40:43],a[40:43]
zm a[40:43],a[164:167],v[56:59],a[40:43]
zm a[40:43],a[168:171],v[104:107],a[40:43]
s_waitcnt vmcnt(1)
s_mov_b64 exec, 1
buffer_store_dwordx2 v[216:217], off, s[92:95], s90 sc1
s_mov_b64 exec, -1
buffer_load_dwordx2 v[246:247], v215, s[92:95], 0 offen offset:0 sc1
v_cmp_eq_u64 vcc, v[244:245], v[216:217]
s_nop 0
s_and_b32 s71, vcc_lo, s51
s_xor_b32 s51, s51, s71
s_bitcmp1_b32 s71, 0
s_cbranch_scc0 WL_ka0%=
zl v[160:163],v214,s[96:99],s84 offen offset:0 sc1
zl v[164:167],v214,s[96:99],s84 offen offset:1024 sc1
zl v[168:171],v214,s[96:99],s84 offen offset:2048 sc1
zl v[172:175],v214,s[96:99],s84 offen offset:3072 sc1
WL_ka0%=:
s_bitcmp1_b32 s71, 1
s_cbranch_scc0 WL_ka1%=
zl v[176:179],v214,s[96:99],s85 offen offset:0 sc1
zl v[180:183],v214,s[96:99],s85 offen offset:1024 sc1
zl v[184:187],v214,s[96:99],s85 offen offset:2048 sc1
zl v[188:191],v214,s[96:99],s85 offen offset:3072 sc1
WL_ka1%=:
s_bitcmp1_b32 s71, 2
s_cbranch_scc0 WL_ka2%=
zl v[128:131],v214,s[96:99],s86 offen offset:0 sc1
zl v[132:135],v214,s[96:99],s86 offen offset:1024 sc1
zl v[136:139],v214,s[96:99],s86 offen offset:2048 sc1
zl v[140:143],v214,s[96:99],s86 offen offset:3072 sc1
WL_ka2%=:
zm a[40:43],a[172:175],v[120:123],a[40:43]
s_waitcnt lgkmcnt(6)
zm a[48:51],a[176:179],v[32:35],a[48:51]
s_waitcnt lgkmcnt(4)
zm a[48:51],a[180:183],v[48:51],a[48:51]
s_waitcnt lgkmcnt(2)
zm a[48:51],a[184:187],v[96:99],a[48:51]
s_waitcnt lgkmcnt(0)
zm a[48:51],a[188:191],v[112:115],a[48:51]
zm a[52:55],a[188:191],v[116:119],a[52:55]
zm a[52:55],a[184:187],v[100:103],a[52:55]
zm a[52:55],a[180:183],v[52:55],a[52:55]
zm a[52:55],a[176:179],v[36:39],a[52:55]
zm a[56:59],a[176:179],v[40:43],a[56:59]
zm a[56:59],a[180:183],v[56:59],a[56:59]
zm a[56:59],a[184:187],v[104:107],a[56:59]
zm a[56:59],a[188:191],v[120:123],a[56:59]
s_nop 7
.p2align 3
zr v96,a32
zr v97,a33
zr v98,a34
zr v99,a35
zr v100,a36
zr v101,a37
zr v102,a38
zr v103,a39
zr v104,a40
zr v105,a41
zr v106,a42
zr v107,a43
zr v108,a44
zr v109,a45
zr v110,a46
zr v111,a47
zr v112,a48
zr v113,a49
zr v114,a50
zr v115,a51
zr v116,a52
zr v117,a53
zr v118,a54
zr v119,a55
zr v120,a56
zr v121,a57
zr v122,a58
zr v123,a59
zr v124,a60
zr v125,a61
zr v126,a62
zr v127,a63
s_mov_b32 s54, s51
WL_e1%=:
s_waitcnt vmcnt(0)
v_cmp_eq_u64 vcc, v[246:247], v[216:217]
s_nop 0
s_and_b32 s71, vcc_lo, s51
s_xor_b32 s51, s51, s71
s_bitcmp1_b32 s71, 0
s_cbranch_scc0 WL_kb0%=
zl v[160:163],v214,s[96:99],s84 offen offset:0 sc1
zl v[164:167],v214,s[96:99],s84 offen offset:1024 sc1
zl v[168:171],v214,s[96:99],s84 offen offset:2048 sc1
zl v[172:175],v214,s[96:99],s84 offen offset:3072 sc1
WL_kb0%=:
s_bitcmp1_b32 s71, 1
s_cbranch_scc0 WL_kb1%=
zl v[176:179],v214,s[96:99],s85 offen offset:0 sc1
zl v[180:183],v214,s[96:99],s85 offen offset:1024 sc1
zl v[184:187],v214,s[96:99],s85 offen offset:2048 sc1
zl v[188:191],v214,s[96:99],s85 offen offset:3072 sc1
WL_kb1%=:
s_bitcmp1_b32 s71, 2
s_cbranch_scc0 WL_kb2%=
zl v[128:131],v214,s[96:99],s86 offen offset:0 sc1
zl v[132:135],v214,s[96:99],s86 offen offset:1024 sc1
zl v[136:139],v214,s[96:99],s86 offen offset:2048 sc1
zl v[140:143],v214,s[96:99],s86 offen offset:3072 sc1
WL_kb2%=:
s_and_b32 s74, s51, 3
s_cbranch_scc1 WL_es%=
WL_e2%=:
s_bitcmp1_b32 s54, 0
s_cbranch_scc0 WL_e3%=
s_waitcnt vmcnt(0)
WL_e3%=:
.p2align 3
buffer_load_dwordx2 v[244:245], v215, s[92:95], 0 offen offset:0 sc1
v_lshlrev_b32 v0, 16, v160
v_and_b32 v1, v242, v160
v_pk_add_f32 v[96:97], v[96:97], v[0:1]
v_lshlrev_b32 v2, 16, v161
v_and_b32 v3, v242, v161
v_pk_add_f32 v[98:99], v[98:99], v[2:3]
v_lshlrev_b32 v4, 16, v162
v_and_b32 v5, v242, v162
v_pk_add_f32 v[100:101], v[100:101], v[4:5]
v_lshlrev_b32 v6, 16, v163
v_and_b32 v7, v242, v163
v_pk_add_f32 v[102:103], v[102:103], v[6:7]
buffer_load_dwordx2 v[246:247], v215, s[92:95], 0 offen offset:0 sc1
v_lshlrev_b32 v0, 16, v164
v_and_b32 v1, v242, v164
v_pk_add_f32 v[104:105], v[104:105], v[0:1]
v_lshlrev_b32 v2, 16, v165
v_and_b32 v3, v242, v165
v_pk_add_f32 v[106:107], v[106:107], v[2:3]
v_lshlrev_b32 v4, 16, v166
v_and_b32 v5, v242, v166
v_pk_add_f32 v[108:109], v[108:109], v[4:5]
v_lshlrev_b32 v6, 16, v167
v_and_b32 v7, v242, v167
v_pk_add_f32 v[110:111], v[110:111], v[6:7]
buffer_load_dwordx2 v[248:249], v215, s[92:95], 0 offen offset:0 sc1
v_lshlrev_b32 v0, 16, v168
v_and_b32 v1, v242, v168
v_pk_add_f32 v[112:113], v[112:113], v[0:1]
v_lshlrev_b32 v2, 16, v169
v_and_b32 v3, v242, v169
v_pk_add_f32 v[114:115], v[114:115], v[2:3]
v_lshlrev_b32 v4, 16, v170
v_and_b32 v5, v242, v170
v_pk_add_f32 v[116:117], v[116:117], v[4:5]
v_lshlrev_b32 v6, 16, v171
v_and_b32 v7, v242, v171
v_pk_add_f32 v[118:119], v[118:119], v[6:7]
buffer_load_dwordx2 v[250:251], v215, s[92:95], 0 offen offset:0 sc1
v_lshlrev_b32 v0, 16, v172
v_and_b32 v1, v242, v172
v_pk_add_f32 v[120:121], v[120:121], v[0:1]
v_lshlrev_b32 v2, 16, v173
v_and_b32 v3, v242, v173
v_pk_add_f32 v[122:123], v[122:123], v[2:3]
v_lshlrev_b32 v4, 16, v174
v_and_b32 v5, v242, v174
v_pk_add_f32 v[124:125], v[124:125], v[4:5]
v_lshlrev_b32 v6, 16, v175
v_and_b32 v7, v242, v175
v_pk_add_f32 v[126:127], v[126:127], v[6:7]
s_bitcmp1_b32 s51, 2
s_cbranch_scc0 WL_e4%=
WL_pq%=:
s_waitcnt vmcnt(3)
v_cmp_ne_u64 vcc, v[244:245], v[216:217]
s_nop 1
s_cbranch_vccz WL_pqd%=
buffer_load_dwordx2 v[244:245], v215, s[92:95], 0 offen offset:0 sc1
s_sleep 2
s_waitcnt vmcnt(3)
v_cmp_ne_u64 vcc, v[246:247], v[216:217]
s_nop 1
s_cbranch_vccz WL_pqd%=
buffer_load_dwordx2 v[246:247], v215, s[92:95], 0 offen offset:0 sc1
s_sleep 2
s_waitcnt vmcnt(3)
v_cmp_ne_u64 vcc, v[248:249], v[216:217]
s_nop 1
s_cbranch_vccz WL_pqd%=
buffer_load_dwordx2 v[248:249], v215, s[92:95], 0 offen offset:0 sc1
s_sleep 2
s_waitcnt vmcnt(3)
v_cmp_ne_u64 vcc, v[250:251], v[216:217]
s_nop 1
s_cbranch_vccz WL_pqd%=
buffer_load_dwordx2 v[250:251], v215, s[92:95], 0 offen offset:0 sc1
s_sleep 2
s_add_u32 s63, s63, 1
s_cmp_lt_u32 s63, 0x100000
s_cbranch_scc1 WL_pq%=
WL_pqd%=:
zl v[128:131],v214,s[96:99],s86 offen offset:0 sc1
zl v[132:135],v214,s[96:99],s86 offen offset:1024 sc1
zl v[136:139],v214,s[96:99],s86 offen offset:2048 sc1
zl v[140:143],v214,s[96:99],s86 offen offset:3072 sc1
s_branch WL_e5%=
WL_e4%=:
s_waitcnt vmcnt(4)
WL_e5%=:
.p2align 3
v_lshlrev_b32 v0, 16, v176
v_and_b32 v1, v242, v176
v_pk_add_f32 v[96:97], v[96:97], v[0:1]
v_lshlrev_b32 v2, 16, v177
v_and_b32 v3, v242, v177
v_pk_add_f32 v[98:99], v[98:99], v[2:3]
v_lshlrev_b32 v4, 16, v178
v_and_b32 v5, v242, v178
v_pk_add_f32 v[100:101], v[100:101], v[4:5]
v_lshlrev_b32 v6, 16, v179
v_and_b32 v7, v242, v179
v_pk_add_f32 v[102:103], v[102:103], v[6:7]
v_lshlrev_b32 v0, 16, v180
v_and_b32 v1, v242, v180
v_pk_add_f32 v[104:105], v[104:105], v[0:1]
v_lshlrev_b32 v2, 16, v181
v_and_b32 v3, v242, v181
v_pk_add_f32 v[106:107], v[106:107], v[2:3]
v_lshlrev_b32 v4, 16, v182
v_and_b32 v5, v242, v182
v_pk_add_f32 v[108:109], v[108:109], v[4:5]
v_lshlrev_b32 v6, 16, v183
v_and_b32 v7, v242, v183
v_pk_add_f32 v[110:111], v[110:111], v[6:7]
v_lshlrev_b32 v0, 16, v184
v_and_b32 v1, v242, v184
v_pk_add_f32 v[112:113], v[112:113], v[0:1]
v_lshlrev_b32 v2, 16, v185
v_and_b32 v3, v242, v185
v_pk_add_f32 v[114:115], v[114:115], v[2:3]
v_lshlrev_b32 v4, 16, v186
v_and_b32 v5, v242, v186
v_pk_add_f32 v[116:117], v[116:117], v[4:5]
v_lshlrev_b32 v6, 16, v187
v_and_b32 v7, v242, v187
v_pk_add_f32 v[118:119], v[118:119], v[6:7]
v_lshlrev_b32 v0, 16, v188
v_and_b32 v1, v242, v188
v_pk_add_f32 v[120:121], v[120:121], v[0:1]
v_lshlrev_b32 v2, 16, v189
v_and_b32 v3, v242, v189
v_pk_add_f32 v[122:123], v[122:123], v[2:3]
v_lshlrev_b32 v4, 16, v190
v_and_b32 v5, v242, v190
v_pk_add_f32 v[124:125], v[124:125], v[4:5]
v_lshlrev_b32 v6, 16, v191
v_and_b32 v7, v242, v191
v_pk_add_f32 v[126:127], v[126:127], v[6:7]
s_bitcmp1_b32 s51, 2
s_cbranch_scc0 WL_e6%=
s_waitcnt vmcnt(0)
WL_e6%=:
s_cmp_eq_u32 s65, 0
s_cselect_b32 s94, s94, 0
s_mov_b64 exec, 1
buffer_store_dwordx2 v[216:217], off, s[92:95], 0 offset:3072 sc1
s_mov_b64 exec, -1
.p2align 3
v_lshlrev_b32 v0, 16, v128
v_and_b32 v1, v242, v128
v_pk_add_f32 v[96:97], v[96:97], v[0:1]
v_lshlrev_b32 v2, 16, v129
v_and_b32 v3, v242, v129
v_pk_add_f32 v[98:99], v[98:99], v[2:3]
v_lshlrev_b32 v4, 16, v130
v_and_b32 v5, v242, v130
v_pk_add_f32 v[100:101], v[100:101], v[4:5]
v_lshlrev_b32 v6, 16, v131
v_and_b32 v7, v242, v131
v_pk_add_f32 v[102:103], v[102:103], v[6:7]
v_lshlrev_b32 v0, 16, v132
v_and_b32 v1, v242, v132
v_pk_add_f32 v[104:105], v[104:105], v[0:1]
v_lshlrev_b32 v2, 16, v133
v_and_b32 v3, v242, v133
v_pk_add_f32 v[106:107], v[106:107], v[2:3]
v_lshlrev_b32 v4, 16, v134
v_and_b32 v5, v242, v134
v_pk_add_f32 v[108:109], v[108:109], v[4:5]
v_lshlrev_b32 v6, 16, v135
v_and_b32 v7, v242, v135
v_pk_add_f32 v[110:111], v[110:111], v[6:7]
v_lshlrev_b32 v0, 16, v136
v_and_b32 v1, v242, v136
v_pk_add_f32 v[112:113], v[112:113], v[0:1]
v_lshlrev_b32 v2, 16, v137
v_and_b32 v3, v242, v137
v_pk_add_f32 v[114:115], v[114:115], v[2:3]
v_lshlrev_b32 v4, 16, v138
v_and_b32 v5, v242, v138
v_pk_add_f32 v[116:117], v[116:117], v[4:5]
v_lshlrev_b32 v6, 16, v139
v_and_b32 v7, v242, v139
v_pk_add_f32 v[118:119], v[118:119], v[6:7]
v_lshlrev_b32 v0, 16, v140
v_and_b32 v1, v242, v140
v_pk_add_f32 v[120:121], v[120:121], v[0:1]
v_lshlrev_b32 v2, 16, v141
v_and_b32 v3, v242, v141
v_pk_add_f32 v[122:123], v[122:123], v[2:3]
v_lshlrev_b32 v4, 16, v142
v_and_b32 v5, v242, v142
v_pk_add_f32 v[124:125], v[124:125], v[4:5]
v_lshlrev_b32 v6, 16, v143
v_and_b32 v7, v242, v143
v_pk_add_f32 v[126:127], v[126:127], v[6:7]
zc v16,v96,v97
zc v17,v98,v99
zc v18,v112,v113
zc v19,v114,v115
zc v20,v100,v101
zc v21,v102,v103
zc v22,v116,v117
zc v23,v118,v119
zc v24,v104,v105
zc v25,v106,v107
zc v26,v120,v121
zc v27,v122,v123
zc v28,v108,v109
zc v29,v110,v111
zc v30,v124,v125
zc v31,v126,v127
v_permlane16_swap_b32 v16, v18
v_permlane16_swap_b32 v17, v19
buffer_store_dwordx4 v[16:19], v237, s[28:31], 0 offen offset:0 sc1
v_permlane16_swap_b32 v20, v22
v_permlane16_swap_b32 v21, v23
buffer_store_dwordx4 v[20:23], v238, s[28:31], 0 offen offset:0 sc1
v_permlane16_swap_b32 v24, v26
v_permlane16_swap_b32 v25, v27
buffer_store_dwordx4 v[24:27], v239, s[28:31], 0 offen offset:0 sc1
v_permlane16_swap_b32 v28, v30
v_permlane16_swap_b32 v29, v31
s_endpgm
WL_es%=:
s_sleep 1
buffer_load_dwordx2 v[246:247], v215, s[92:95], 0 offen offset:0 sc1
s_add_u32 s63, s63, 1
s_cmp_lt_u32 s63, 0x100000
s_cbranch_scc1 WL_e1%=
s_waitcnt vmcnt(0)
)"
R"(s_branch WL_e2%=
WL_ps1%=:
s_sleep 1
buffer_load_dwordx2 v[226:227], v215, s[92:95], 0 offen offset:1024 sc1
s_add_u32 s63, s63, 1
s_waitcnt vmcnt(0)
s_cmp_lt_u32 s63, 0x100000
s_cbranch_scc1 WL_pc1%=
s_branch WL_pd1%=
WL_ps0%=:
s_sleep 1
buffer_load_dwordx2 v[226:227], v215, s[92:95], 0 offen offset:0 sc1
s_add_u32 s63, s63, 1
s_waitcnt vmcnt(0)
s_cmp_lt_u32 s63, 0x100000
s_cbranch_scc1 WL_pc0%=
s_branch WL_pd0%=
L_hot_end%=:
L_fail%=:
s_waitcnt vmcnt(0) lgkmcnt(0)
s_barrier
s_mov_b32 s76, -1
s_cmp_eq_u32 s51, 8
s_cselect_b32 s78, 0, s78
s_branch L_top%=
WL_safe%=:
v_add_u32 v244, 0x20000, v196
v_add_u32 v245, 0x20000, v197
v_add_u32 v246, 0x20000, v198
v_add_u32 v247, 0x20000, v199
s_lshl_b32 s60, s64, 10
s_add_u32 s71, s60, 0x20000
s_mov_b32 s54, 0
WL_sp0%=:
s_add_u32 m0, s71, 0
s_nop 0
zl v212,s[96:99],s60 offen offset:0 lds
s_add_u32 s60, s60, 0x1000
s_add_u32 m0, s71, 0x1000
s_nop 0
zl v212,s[96:99],s60 offen offset:0 lds
s_add_u32 s60, s60, 0x1000
s_add_u32 m0, s71, 0x2000
s_nop 0
zl v212,s[96:99],s60 offen offset:0 lds
s_add_u32 s60, s60, 0x1000
s_add_u32 m0, s71, 0x3000
s_nop 0
zl v212,s[96:99],s60 offen offset:0 lds
s_add_u32 s60, s60, 0x1000
s_waitcnt vmcnt(0)
s_barrier
zk a[160:163],v244 offset:0
zk a[164:167],v245 offset:0
zk a[168:171],v246 offset:0
zk a[172:175],v247 offset:0
zk a[176:179],v244 offset:4096
zk a[180:183],v245 offset:4096
zk a[184:187],v246 offset:4096
zk a[188:191],v247 offset:4096
s_waitcnt lgkmcnt(0)
zm v[32:35],a[160:163],a[192:195],0
zm v[32:35],a[164:167],a[196:199],v[32:35]
zm v[32:35],a[168:171],a[200:203],v[32:35]
zm v[32:35],a[172:175],a[204:207],v[32:35]
zm v[36:39],a[188:191],a[204:207],0
zm v[36:39],a[184:187],a[200:203],v[36:39]
zm v[36:39],a[180:183],a[196:199],v[36:39]
zm v[36:39],a[176:179],a[192:195],v[36:39]
zm v[44:47],a[176:179],a[208:211],0
zm v[44:47],a[180:183],a[212:215],v[44:47]
zm v[44:47],a[184:187],a[216:219],v[44:47]
zm v[44:47],a[188:191],a[220:223],v[44:47]
zm v[40:43],a[172:175],a[220:223],0
zm v[40:43],a[168:171],a[216:219],v[40:43]
zm v[40:43],a[164:167],a[212:215],v[40:43]
zm v[40:43],a[160:163],a[208:211],v[40:43]
zm v[48:51],a[160:163],a[224:227],0
zm v[48:51],a[164:167],a[228:231],v[48:51]
zm v[48:51],a[168:171],a[232:235],v[48:51]
zm v[48:51],a[172:175],a[236:239],v[48:51]
zm v[52:55],a[188:191],a[236:239],0
zm v[52:55],a[184:187],a[232:235],v[52:55]
zm v[52:55],a[180:183],a[228:231],v[52:55]
zm v[52:55],a[176:179],a[224:227],v[52:55]
s_nop 7
v_max3_f32 v218, v218, v32, v33
v_max3_f32 v219, v219, v40, v41
v_max3_f32 v220, v220, v48, v49
v_max3_f32 v221, v221, v56, v57
v_max3_f32 v218, v218, v34, v35
v_max3_f32 v219, v219, v42, v43
v_max3_f32 v220, v220, v50, v51
v_max3_f32 v221, v221, v58, v59
v_max3_f32 v218, v218, v36, v37
v_max3_f32 v219, v219, v44, v45
v_max3_f32 v220, v220, v52, v53
v_max3_f32 v221, v221, v60, v61
v_max3_f32 v218, v218, v38, v39
v_max3_f32 v219, v219, v46, v47
v_max3_f32 v220, v220, v54, v55
v_max3_f32 v221, v221, v62, v63
zk a[160:163],v244 offset:8192
zk a[164:167],v245 offset:8192
zk a[168:171],v246 offset:8192
zk a[172:175],v247 offset:8192
zk a[176:179],v244 offset:12288
zk a[180:183],v245 offset:12288
zk a[184:187],v246 offset:12288
zk a[188:191],v247 offset:12288
s_waitcnt lgkmcnt(0)
zm v[32:35],a[160:163],a[192:195],0
zm v[32:35],a[164:167],a[196:199],v[32:35]
zm v[32:35],a[168:171],a[200:203],v[32:35]
zm v[32:35],a[172:175],a[204:207],v[32:35]
zm v[36:39],a[188:191],a[204:207],0
zm v[36:39],a[184:187],a[200:203],v[36:39]
zm v[36:39],a[180:183],a[196:199],v[36:39]
zm v[36:39],a[176:179],a[192:195],v[36:39]
zm v[44:47],a[176:179],a[208:211],0
zm v[44:47],a[180:183],a[212:215],v[44:47]
zm v[44:47],a[184:187],a[216:219],v[44:47]
zm v[44:47],a[188:191],a[220:223],v[44:47]
zm v[40:43],a[172:175],a[220:223],0
zm v[40:43],a[168:171],a[216:219],v[40:43]
zm v[40:43],a[164:167],a[212:215],v[40:43]
zm v[40:43],a[160:163],a[208:211],v[40:43]
zm v[48:51],a[160:163],a[224:227],0
zm v[48:51],a[164:167],a[228:231],v[48:51]
zm v[48:51],a[168:171],a[232:235],v[48:51]
zm v[48:51],a[172:175],a[236:239],v[48:51]
zm v[52:55],a[188:191],a[236:239],0
zm v[52:55],a[184:187],a[232:235],v[52:55]
zm v[52:55],a[180:183],a[228:231],v[52:55]
zm v[52:55],a[176:179],a[224:227],v[52:55]
s_nop 7
v_max3_f32 v218, v218, v32, v33
v_max3_f32 v219, v219, v40, v41
v_max3_f32 v220, v220, v48, v49
v_max3_f32 v221, v221, v56, v57
v_max3_f32 v218, v218, v34, v35
v_max3_f32 v219, v219, v42, v43
v_max3_f32 v220, v220, v50, v51
v_max3_f32 v221, v221, v58, v59
v_max3_f32 v218, v218, v36, v37
v_max3_f32 v219, v219, v44, v45
v_max3_f32 v220, v220, v52, v53
v_max3_f32 v221, v221, v60, v61
v_max3_f32 v218, v218, v38, v39
v_max3_f32 v219, v219, v46, v47
v_max3_f32 v220, v220, v54, v55
v_max3_f32 v221, v221, v62, v63
s_barrier
s_add_u32 s54, s54, 1
s_cmp_lt_u32 s54, 16
s_cbranch_scc1 WL_sp0%=
s_mov_b32 s54, 0
WL_sp1%=:
s_add_u32 m0, s71, 0
s_nop 0
zl v212,s[96:99],s60 offen offset:0 lds
s_add_u32 s60, s60, 0x1000
s_add_u32 m0, s71, 0x1000
s_nop 0
zl v212,s[96:99],s60 offen offset:0 lds
s_add_u32 s60, s60, 0x1000
s_add_u32 m0, s71, 0x2000
s_nop 0
zl v212,s[96:99],s60 offen offset:0 lds
s_add_u32 s60, s60, 0x1000
s_add_u32 m0, s71, 0x3000
s_nop 0
zl v212,s[96:99],s60 offen offset:0 lds
s_add_u32 s60, s60, 0x1000
s_waitcnt vmcnt(0)
s_barrier
zk a[160:163],v244 offset:0
zk a[164:167],v245 offset:0
zk a[168:171],v246 offset:0
zk a[172:175],v247 offset:0
zk a[176:179],v244 offset:4096
zk a[180:183],v245 offset:4096
zk a[184:187],v246 offset:4096
zk a[188:191],v247 offset:4096
s_waitcnt lgkmcnt(0)
zm v[32:35],a[160:163],v[128:131],0
zm v[32:35],a[164:167],v[144:147],v[32:35]
zm v[32:35],a[168:171],v[160:163],v[32:35]
zm v[32:35],a[172:175],v[176:179],v[32:35]
zm v[36:39],a[188:191],v[176:179],0
zm v[36:39],a[184:187],v[160:163],v[36:39]
zm v[36:39],a[180:183],v[144:147],v[36:39]
zm v[36:39],a[176:179],v[128:131],v[36:39]
zm v[44:47],a[176:179],v[132:135],0
zm v[44:47],a[180:183],v[148:151],v[44:47]
zm v[44:47],a[184:187],v[164:167],v[44:47]
zm v[44:47],a[188:191],v[180:183],v[44:47]
zm v[40:43],a[172:175],v[180:183],0
zm v[40:43],a[168:171],v[164:167],v[40:43]
zm v[40:43],a[164:167],v[148:151],v[40:43]
zm v[40:43],a[160:163],v[132:135],v[40:43]
zm v[48:51],a[160:163],v[136:139],0
zm v[48:51],a[164:167],v[152:155],v[48:51]
zm v[48:51],a[168:171],v[168:171],v[48:51]
zm v[48:51],a[172:175],v[184:187],v[48:51]
zm v[52:55],a[188:191],v[184:187],0
zm v[52:55],a[184:187],v[168:171],v[52:55]
zm v[52:55],a[180:183],v[152:155],v[52:55]
zm v[52:55],a[176:179],v[136:139],v[52:55]
s_nop 7
v_max3_f32 v222, v222, v32, v33
v_max3_f32 v223, v223, v40, v41
v_max3_f32 v224, v224, v48, v49
v_max3_f32 v225, v225, v56, v57
v_max3_f32 v222, v222, v34, v35
v_max3_f32 v223, v223, v42, v43
v_max3_f32 v224, v224, v50, v51
v_max3_f32 v225, v225, v58, v59
v_max3_f32 v222, v222, v36, v37
v_max3_f32 v223, v223, v44, v45
v_max3_f32 v224, v224, v52, v53
v_max3_f32 v225, v225, v60, v61
v_max3_f32 v222, v222, v38, v39
v_max3_f32 v223, v223, v46, v47
v_max3_f32 v224, v224, v54, v55
v_max3_f32 v225, v225, v62, v63
zk a[160:163],v244 offset:8192
zk a[164:167],v245 offset:8192
zk a[168:171],v246 offset:8192
zk a[172:175],v247 offset:8192
zk a[176:179],v244 offset:12288
zk a[180:183],v245 offset:12288
zk a[184:187],v246 offset:12288
zk a[188:191],v247 offset:12288
s_waitcnt lgkmcnt(0)
zm v[32:35],a[160:163],v[128:131],0
zm v[32:35],a[164:167],v[144:147],v[32:35]
zm v[32:35],a[168:171],v[160:163],v[32:35]
zm v[32:35],a[172:175],v[176:179],v[32:35]
zm v[36:39],a[188:191],v[176:179],0
zm v[36:39],a[184:187],v[160:163],v[36:39]
zm v[36:39],a[180:183],v[144:147],v[36:39]
zm v[36:39],a[176:179],v[128:131],v[36:39]
zm v[44:47],a[176:179],v[132:135],0
zm v[44:47],a[180:183],v[148:151],v[44:47]
zm v[44:47],a[184:187],v[164:167],v[44:47]
zm v[44:47],a[188:191],v[180:183],v[44:47]
zm v[40:43],a[172:175],v[180:183],0
zm v[40:43],a[168:171],v[164:167],v[40:43]
zm v[40:43],a[164:167],v[148:151],v[40:43]
zm v[40:43],a[160:163],v[132:135],v[40:43]
zm v[48:51],a[160:163],v[136:139],0
zm v[48:51],a[164:167],v[152:155],v[48:51]
zm v[48:51],a[168:171],v[168:171],v[48:51]
zm v[48:51],a[172:175],v[184:187],v[48:51]
zm v[52:55],a[188:191],v[184:187],0
zm v[52:55],a[184:187],v[168:171],v[52:55]
zm v[52:55],a[180:183],v[152:155],v[52:55]
zm v[52:55],a[176:179],v[136:139],v[52:55]
s_nop 7
v_max3_f32 v222, v222, v32, v33
v_max3_f32 v223, v223, v40, v41
v_max3_f32 v224, v224, v48, v49
v_max3_f32 v225, v225, v56, v57
v_max3_f32 v222, v222, v34, v35
v_max3_f32 v223, v223, v42, v43
v_max3_f32 v224, v224, v50, v51
v_max3_f32 v225, v225, v58, v59
v_max3_f32 v222, v222, v36, v37
v_max3_f32 v223, v223, v44, v45
v_max3_f32 v224, v224, v52, v53
v_max3_f32 v225, v225, v60, v61
v_max3_f32 v222, v222, v38, v39
v_max3_f32 v223, v223, v46, v47
v_max3_f32 v224, v224, v54, v55
v_max3_f32 v225, v225, v62, v63
s_barrier
s_add_u32 s54, s54, 1
s_cmp_lt_u32 s54, 16
s_cbranch_scc1 WL_sp1%=
s_branch WL_start%=
L_safe%=:
v_add_u32 v244, 0x20000, v196
v_add_u32 v245, 0x20000, v197
v_add_u32 v246, 0x20000, v198
v_add_u32 v247, 0x20000, v199
s_lshl_b32 s60, s64, 10
s_add_u32 s71, s60, 0x20000
s_mov_b32 s54, 0
L_sp0%=:
s_add_u32 m0, s71, 0
s_nop 0
zl v212,s[96:99],s60 offen offset:0 lds
s_add_u32 s60, s60, 0x1000
s_add_u32 m0, s71, 0x1000
s_nop 0
zl v212,s[96:99],s60 offen offset:0 lds
s_add_u32 s60, s60, 0x1000
s_add_u32 m0, s71, 0x2000
s_nop 0
zl v212,s[96:99],s60 offen offset:0 lds
s_add_u32 s60, s60, 0x1000
s_add_u32 m0, s71, 0x3000
s_nop 0
zl v212,s[96:99],s60 offen offset:0 lds
s_add_u32 s60, s60, 0x1000
s_waitcnt vmcnt(0)
s_barrier
zk a[160:163],v244 offset:0
zk a[164:167],v245 offset:0
zk a[168:171],v246 offset:0
zk a[172:175],v247 offset:0
zk a[176:179],v244 offset:4096
zk a[180:183],v245 offset:4096
zk a[184:187],v246 offset:4096
zk a[188:191],v247 offset:4096
s_waitcnt lgkmcnt(0)
zm v[32:35],a[160:163],a[192:195],0
zm v[32:35],a[164:167],a[196:199],v[32:35]
zm v[32:35],a[168:171],a[200:203],v[32:35]
zm v[32:35],a[172:175],a[204:207],v[32:35]
zm v[36:39],a[188:191],a[204:207],0
zm v[36:39],a[184:187],a[200:203],v[36:39]
zm v[36:39],a[180:183],a[196:199],v[36:39]
zm v[36:39],a[176:179],a[192:195],v[36:39]
zm v[44:47],a[176:179],a[208:211],0
zm v[44:47],a[180:183],a[212:215],v[44:47]
zm v[44:47],a[184:187],a[216:219],v[44:47]
zm v[44:47],a[188:191],a[220:223],v[44:47]
zm v[40:43],a[172:175],a[220:223],0
zm v[40:43],a[168:171],a[216:219],v[40:43]
zm v[40:43],a[164:167],a[212:215],v[40:43]
zm v[40:43],a[160:163],a[208:211],v[40:43]
zm v[48:51],a[160:163],a[224:227],0
zm v[48:51],a[164:167],a[228:231],v[48:51]
zm v[48:51],a[168:171],a[232:235],v[48:51]
zm v[48:51],a[172:175],a[236:239],v[48:51]
zm v[52:55],a[188:191],a[236:239],0
zm v[52:55],a[184:187],a[232:235],v[52:55]
zm v[52:55],a[180:183],a[228:231],v[52:55]
zm v[52:55],a[176:179],a[224:227],v[52:55]
zm v[60:63],a[176:179],a[240:243],0
zm v[60:63],a[180:183],a[244:247],v[60:63]
zm v[60:63],a[184:187],a[248:251],v[60:63]
zm v[60:63],a[188:191],a[252:255],v[60:63]
zm v[56:59],a[172:175],a[252:255],0
zm v[56:59],a[168:171],a[248:251],v[56:59]
zm v[56:59],a[164:167],a[244:247],v[56:59]
zm v[56:59],a[160:163],a[240:243],v[56:59]
s_nop 7
v_max3_f32 v218, v218, v32, v33
v_max3_f32 v219, v219, v40, v41
v_max3_f32 v220, v220, v48, v49
v_max3_f32 v221, v221, v56, v57
v_max3_f32 v218, v218, v34, v35
v_max3_f32 v219, v219, v42, v43
v_max3_f32 v220, v220, v50, v51
v_max3_f32 v221, v221, v58, v59
v_max3_f32 v218, v218, v36, v37
v_max3_f32 v219, v219, v44, v45
v_max3_f32 v220, v220, v52, v53
v_max3_f32 v221, v221, v60, v61
v_max3_f32 v218, v218, v38, v39
v_max3_f32 v219, v219, v46, v47
v_max3_f32 v220, v220, v54, v55
v_max3_f32 v221, v221, v62, v63
zk a[160:163],v244 offset:8192
zk a[164:167],v245 offset:8192
zk a[168:171],v246 offset:8192
zk a[172:175],v247 offset:8192
zk a[176:179],v244 offset:12288
zk a[180:183],v245 offset:12288
zk a[184:187],v246 offset:12288
zk a[188:191],v247 offset:12288
s_waitcnt lgkmcnt(0)
zm v[32:35],a[160:163],a[192:195],0
zm v[32:35],a[164:167],a[196:199],v[32:35]
zm v[32:35],a[168:171],a[200:203],v[32:35]
zm v[32:35],a[172:175],a[204:207],v[32:35]
zm v[36:39],a[188:191],a[204:207],0
zm v[36:39],a[184:187],a[200:203],v[36:39]
zm v[36:39],a[180:183],a[196:199],v[36:39]
zm v[36:39],a[176:179],a[192:195],v[36:39]
zm v[44:47],a[176:179],a[208:211],0
zm v[44:47],a[180:183],a[212:215],v[44:47]
zm v[44:47],a[184:187],a[216:219],v[44:47]
zm v[44:47],a[188:191],a[220:223],v[44:47]
zm v[40:43],a[172:175],a[220:223],0
zm v[40:43],a[168:171],a[216:219],v[40:43]
zm v[40:43],a[164:167],a[212:215],v[40:43]
zm v[40:43],a[160:163],a[208:211],v[40:43]
zm v[48:51],a[160:163],a[224:227],0
zm v[48:51],a[164:167],a[228:231],v[48:51]
zm v[48:51],a[168:171],a[232:235],v[48:51]
zm v[48:51],a[172:175],a[236:239],v[48:51]
zm v[52:55],a[188:191],a[236:239],0
zm v[52:55],a[184:187],a[232:235],v[52:55]
zm v[52:55],a[180:183],a[228:231],v[52:55]
zm v[52:55],a[176:179],a[224:227],v[52:55]
zm v[60:63],a[176:179],a[240:243],0
zm v[60:63],a[180:183],a[244:247],v[60:63]
zm v[60:63],a[184:187],a[248:251],v[60:63]
zm v[60:63],a[188:191],a[252:255],v[60:63]
zm v[56:59],a[172:175],a[252:255],0
zm v[56:59],a[168:171],a[248:251],v[56:59]
zm v[56:59],a[164:167],a[244:247],v[56:59]
zm v[56:59],a[160:163],a[240:243],v[56:59]
s_nop 7
v_max3_f32 v218, v218, v32, v33
v_max3_f32 v219, v219, v40, v41
v_max3_f32 v220, v220, v48, v49
v_max3_f32 v221, v221, v56, v57
v_max3_f32 v218, v218, v34, v35
v_max3_f32 v219, v219, v42, v43
v_max3_f32 v220, v220, v50, v51
v_max3_f32 v221, v221, v58, v59
v_max3_f32 v218, v218, v36, v37
v_max3_f32 v219, v219, v44, v45
v_max3_f32 v220, v220, v52, v53
v_max3_f32 v221, v221, v60, v61
v_max3_f32 v218, v218, v38, v39
v_max3_f32 v219, v219, v46, v47
v_max3_f32 v220, v220, v54, v55
v_max3_f32 v221, v221, v62, v63
s_barrier
s_add_u32 s54, s54, 1
s_cmp_lt_u32 s54, 16
s_cbranch_scc1 L_sp0%=
s_mov_b32 s54, 0
L_sp1%=:
s_add_u32 m0, s71, 0
s_nop 0
zl v212,s[96:99],s60 offen offset:0 lds
s_add_u32 s60, s60, 0x1000
s_add_u32 m0, s71, 0x1000
s_nop 0
zl v212,s[96:99],s60 offen offset:0 lds
s_add_u32 s60, s60, 0x1000
s_add_u32 m0, s71, 0x2000
s_nop 0
zl v212,s[96:99],s60 offen offset:0 lds
s_add_u32 s60, s60, 0x1000
s_add_u32 m0, s71, 0x3000
s_nop 0
zl v212,s[96:99],s60 offen offset:0 lds
s_add_u32 s60, s60, 0x1000
s_waitcnt vmcnt(0)
s_barrier
zk a[160:163],v244 offset:0
zk a[164:167],v245 offset:0
zk a[168:171],v246 offset:0
zk a[172:175],v247 offset:0
zk a[176:179],v244 offset:4096
zk a[180:183],v245 offset:4096
zk a[184:187],v246 offset:4096
zk a[188:191],v247 offset:4096
s_waitcnt lgkmcnt(0)
zm v[32:35],a[160:163],v[128:131],0
zm v[32:35],a[164:167],v[144:147],v[32:35]
zm v[32:35],a[168:171],v[160:163],v[32:35]
zm v[32:35],a[172:175],v[176:179],v[32:35]
zm v[36:39],a[188:191],v[176:179],0
zm v[36:39],a[184:187],v[160:163],v[36:39]
zm v[36:39],a[180:183],v[144:147],v[36:39]
zm v[36:39],a[176:179],v[128:131],v[36:39]
zm v[44:47],a[176:179],v[132:135],0
zm v[44:47],a[180:183],v[148:151],v[44:47]
zm v[44:47],a[184:187],v[164:167],v[44:47]
zm v[44:47],a[188:191],v[180:183],v[44:47]
zm v[40:43],a[172:175],v[180:183],0
zm v[40:43],a[168:171],v[164:167],v[40:43]
zm v[40:43],a[164:167],v[148:151],v[40:43]
zm v[40:43],a[160:163],v[132:135],v[40:43]
zm v[48:51],a[160:163],v[136:139],0
zm v[48:51],a[164:167],v[152:155],v[48:51]
zm v[48:51],a[168:171],v[168:171],v[48:51]
zm v[48:51],a[172:175],v[184:187],v[48:51]
zm v[52:55],a[188:191],v[184:187],0
zm v[52:55],a[184:187],v[168:171],v[52:55]
zm v[52:55],a[180:183],v[152:155],v[52:55]
zm v[52:55],a[176:179],v[136:139],v[52:55]
zm v[60:63],a[176:179],v[140:143],0
zm v[60:63],a[180:183],v[156:159],v[60:63]
zm v[60:63],a[184:187],v[172:175],v[60:63]
zm v[60:63],a[188:191],v[188:191],v[60:63]
zm v[56:59],a[172:175],v[188:191],0
zm v[56:59],a[168:171],v[172:175],v[56:59]
zm v[56:59],a[164:167],v[156:159],v[56:59]
zm v[56:59],a[160:163],v[140:143],v[56:59]
s_nop 7
v_max3_f32 v222, v222, v32, v33
v_max3_f32 v223, v223, v40, v41
v_max3_f32 v224, v224, v48, v49
v_max3_f32 v225, v225, v56, v57
v_max3_f32 v222, v222, v34, v35
v_max3_f32 v223, v223, v42, v43
v_max3_f32 v224, v224, v50, v51
v_max3_f32 v225, v225, v58, v59
v_max3_f32 v222, v222, v36, v37
v_max3_f32 v223, v223, v44, v45
v_max3_f32 v224, v224, v52, v53
v_max3_f32 v225, v225, v60, v61
v_max3_f32 v222, v222, v38, v39
v_max3_f32 v223, v223, v46, v47
v_max3_f32 v224, v224, v54, v55
v_max3_f32 v225, v225, v62, v63
zk a[160:163],v244 offset:8192
zk a[164:167],v245 offset:8192
zk a[168:171],v246 offset:8192
zk a[172:175],v247 offset:8192
zk a[176:179],v244 offset:12288
zk a[180:183],v245 offset:12288
zk a[184:187],v246 offset:12288
zk a[188:191],v247 offset:12288
s_waitcnt lgkmcnt(0)
zm v[32:35],a[160:163],v[128:131],0
zm v[32:35],a[164:167],v[144:147],v[32:35]
zm v[32:35],a[168:171],v[160:163],v[32:35]
zm v[32:35],a[172:175],v[176:179],v[32:35]
zm v[36:39],a[188:191],v[176:179],0
zm v[36:39],a[184:187],v[160:163],v[36:39]
zm v[36:39],a[180:183],v[144:147],v[36:39]
zm v[36:39],a[176:179],v[128:131],v[36:39]
zm v[44:47],a[176:179],v[132:135],0
zm v[44:47],a[180:183],v[148:151],v[44:47]
zm v[44:47],a[184:187],v[164:167],v[44:47]
zm v[44:47],a[188:191],v[180:183],v[44:47]
zm v[40:43],a[172:175],v[180:183],0
zm v[40:43],a[168:171],v[164:167],v[40:43]
zm v[40:43],a[164:167],v[148:151],v[40:43]
zm v[40:43],a[160:163],v[132:135],v[40:43]
zm v[48:51],a[160:163],v[136:139],0
zm v[48:51],a[164:167],v[152:155],v[48:51]
zm v[48:51],a[168:171],v[168:171],v[48:51]
zm v[48:51],a[172:175],v[184:187],v[48:51]
zm v[52:55],a[188:191],v[184:187],0
zm v[52:55],a[184:187],v[168:171],v[52:55]
zm v[52:55],a[180:183],v[152:155],v[52:55]
zm v[52:55],a[176:179],v[136:139],v[52:55]
zm v[60:63],a[176:179],v[140:143],0
zm v[60:63],a[180:183],v[156:159],v[60:63]
zm v[60:63],a[184:187],v[172:175],v[60:63]
zm v[60:63],a[188:191],v[188:191],v[60:63]
zm v[56:59],a[172:175],v[188:191],0
zm v[56:59],a[168:171],v[172:175],v[56:59]
zm v[56:59],a[164:167],v[156:159],v[56:59]
zm v[56:59],a[160:163],v[140:143],v[56:59]
s_nop 7
v_max3_f32 v222, v222, v32, v33
v_max3_f32 v223, v223, v40, v41
v_max3_f32 v224, v224, v48, v49
v_max3_f32 v225, v225, v56, v57
v_max3_f32 v222, v222, v34, v35
v_max3_f32 v223, v223, v42, v43
v_max3_f32 v224, v224, v50, v51
v_max3_f32 v225, v225, v58, v59
v_max3_f32 v222, v222, v36, v37
v_max3_f32 v223, v223, v44, v45
v_max3_f32 v224, v224, v52, v53
v_max3_f32 v225, v225, v60, v61
v_max3_f32 v222, v222, v38, v39
v_max3_f32 v223, v223, v46, v47
v_max3_f32 v224, v224, v54, v55
v_max3_f32 v225, v225, v62, v63
s_barrier
s_add_u32 s54, s54, 1
s_cmp_lt_u32 s54, 16
s_cbranch_scc1 L_sp1%=
s_branch L_start%=
.purgem zm
.purgem zt
.purgem zc
.purgem zr
.purgem zw
.purgem zl
.purgem ze
.purgem zk
.purgem zp
)"
  :
  : [wid] "s"(wid), [bid] "s"(bid), [ka] "s"(ka)
  : "v0", "v1", "v2", "v3", "v4", "v5", "v6", "v7", "v8", "v9", "v10", "v11", "v12", "v13", "v14", "v15", "v16", "v17", "v18", "v19", "v20", "v21", "v22", "v23", "v24", "v25", "v26", "v27", "v28", "v29", "v30", "v31", "v32", "v33", "v34", "v35", "v36", "v37", "v38", "v39", "v40", "v41", "v42", "v43", "v44", "v45", "v46", "v47", "v48", "v49", "v50", "v51", "v52", "v53", "v54", "v55", "v56", "v57", "v58", "v59", "v60", "v61", "v62", "v63", "v64", "v65", "v66", "v67", "v68", "v69", "v70", "v71", "v72", "v73", "v74", "v75", "v76", "v77", "v78", "v79", "v80", "v81", "v82", "v83", "v84", "v85", "v86", "v87", "v88", "v89", "v90", "v91", "v92", "v93", "v94", "v95", "v96", "v97", "v98", "v99", "v100", "v101", "v102", "v103", "v104", "v105", "v106", "v107", "v108", "v109", "v110", "v111", "v112", "v113", "v114", "v115", "v116", "v117", "v118", "v119", "v120", "v121", "v122", "v123", "v124", "v125", "v126", "v127", "v128", "v129", "v130", "v131", "v132", "v133", "v134", "v135", "v136", "v137", "v138", "v139", "v140", "v141", "v142", "v143", "v144", "v145", "v146", "v147", "v148", "v149", "v150", "v151", "v152", "v153", "v154", "v155", "v156", "v157", "v158", "v159", "v160", "v161", "v162", "v163", "v164", "v165", "v166", "v167", "v168", "v169", "v170", "v171", "v172", "v173", "v174", "v175", "v176", "v177", "v178", "v179", "v180", "v181", "v182", "v183", "v184", "v185", "v186", "v187", "v188", "v189", "v190", "v191", "v192", "v193", "v194", "v195", "v196", "v197", "v198", "v199", "v200", "v201", "v202", "v203", "v204", "v205", "v206", "v207", "v208", "v209", "v210", "v211", "v212", "v213", "v214", "v215", "v216", "v217", "v218", "v219", "v220", "v221", "v222", "v223", "v224", "v225", "v226", "v227", "v228", "v229", "v230", "v231", "v232", "v233", "v234", "v235", "v236", "v237", "v238", "v239", "v240", "v241", "v242", "v243", "v244", "v245", "v246", "v247", "v248", "v249", "v250", "v251", "v252", "v253", "v254", "v255", "a0", "a1", "a2", "a3", "a4", "a5", "a6", "a7", "a8", "a9", "a10", "a11", "a12", "a13", "a14", "a15", "a16", "a17", "a18", "a19", "a20", "a21", "a22", "a23", "a24", "a25", "a26", "a27", "a28", "a29", "a30", "a31", "a32", "a33", "a34", "a35", "a36", "a37", "a38", "a39", "a40", "a41", "a42", "a43", "a44", "a45", "a46", "a47", "a48", "a49", "a50", "a51", "a52", "a53", "a54", "a55", "a56", "a57", "a58", "a59", "a60", "a61", "a62", "a63", "a64", "a65", "a66", "a67", "a68", "a69", "a70", "a71", "a72", "a73", "a74", "a75", "a76", "a77", "a78", "a79", "a80", "a81", "a82", "a83", "a84", "a85", "a86", "a87", "a88", "a89", "a90", "a91", "a92", "a93", "a94", "a95", "a96", "a97", "a98", "a99", "a100", "a101", "a102", "a103", "a104", "a105", "a106", "a107", "a108", "a109", "a110", "a111", "a112", "a113", "a114", "a115", "a116", "a117", "a118", "a119", "a120", "a121", "a122", "a123", "a124", "a125", "a126", "a127", "a128", "a129", "a130", "a131", "a132", "a133", "a134", "a135", "a136", "a137", "a138", "a139", "a140", "a141", "a142", "a143", "a144", "a145", "a146", "a147", "a148", "a149", "a150", "a151", "a152", "a153", "a154", "a155", "a156", "a157", "a158", "a159", "a160", "a161", "a162", "a163", "a164", "a165", "a166", "a167", "a168", "a169", "a170", "a171", "a172", "a173", "a174", "a175", "a176", "a177", "a178", "a179", "a180", "a181", "a182", "a183", "a184", "a185", "a186", "a187", "a188", "a189", "a190", "a191", "a192", "a193", "a194", "a195", "a196", "a197", "a198", "a199", "a200", "a201", "a202", "a203", "a204", "a205", "a206", "a207", "a208", "a209", "a210", "a211", "a212", "a213", "a214", "a215", "a216", "a217", "a218", "a219", "a220", "a221", "a222", "a223", "a224", "a225", "a226", "a227", "a228", "a229", "a230", "a231", "a232", "a233", "a234", "a235", "a236", "a237", "a238", "a239", "a240", "a241", "a242", "a243", "a244", "a245", "a246", "a247", "a248", "a249", "a250", "a251", "a252", "a253", "a254", "a255", "s6", "s7", "s8", "s9", "s10", "s11", "s12", "s13", "s14", "s15", "s16", "s17", "s18", "s19", "s20", "s21", "s22", "s23", "s24", "s25", "s26", "s27", "s28", "s29", "s30", "s31", "s36", "s37", "s38", "s39", "s40", "s41", "s42", "s43", "s44", "s45", "s46", "s47", "s48", "s49", "s50", "s51", "s52", "s53", "s54", "s55", "s56", "s57", "s58", "s59", "s60", "s61", "s62", "s63", "s64", "s65", "s66", "s67", "s68", "s69", "s70", "s71", "s72", "s73", "s74", "s75", "s76", "s77", "s78", "s79", "s80", "s81", "s82", "s83", "s84", "s85", "s86", "s87", "s88", "s89", "s90", "s91", "s92", "s93", "s94", "s95", "s96", "s97", "s98", "s99", "vcc", "scc", "memory");
}

extern "C" void kernel_launch(void* const* d_in, const int* in_sizes, int n_in, void* d_out, int out_size, void* d_ws, size_t ws_size, hipStream_t stream) {
  Ptrs P;
  for (int i = 0; i < 2; ++i) {
    P.x[i] = (const char*)d_in[5 * i + 0]; P.k[i] = (const char*)d_in[5 * i + 1]; P.v[i] = (const char*)d_in[5 * i + 2];
    P.wq[i] = (const char*)d_in[5 * i + 3]; P.wo[i] = (const char*)d_in[5 * i + 4];
  }
  P.ws = (char*)d_ws; P.out = (char*)d_out;
  hipLaunchKernelGGL(fused_attn, dim3(256), dim3(256), 0, stream, P);
}
```
